```python
import torch

def _softmax(x, axis=-1):
    return torch.softmax(x, dim=axis)
def _gelu(x):
    return torch.nn.functional.gelu(x, approximate="tanh")
def _silu(x):
    return torch.nn.functional.silu(x)
def _sigmoid(x):
    return torch.sigmoid(x)
def _layernorm(x, axis=-1, eps=1e-5):
    m = x.mean(dim=axis, keepdim=True)
    v = x.var(dim=axis, keepdim=True, unbiased=False)
    return (x - m) / torch.sqrt(v + eps)
def _scatter_add(tgt, idx, src, axis=0):
    return tgt.clone().index_add_(axis, idx, src)
def _segment_sum(src, seg, n):
    out = torch.zeros((n,) + src.shape[1:], dtype=src.dtype)
    return out.index_add_(0, seg, src)
def _pad(x, pw):
    flat = tuple(v for pair in reversed(pw) for v in pair)
    return torch.nn.functional.pad(x, flat)

kernel_name = 'gated_mix__spectral_gfnet__swiglu_ffn__prompt'

def setup_inputs():

    _w = (lambda a: (lambda t: t.to(torch.bfloat16) if t.is_floating_point() else t)(torch.as_tensor(a)))
    def _A_setup():
        B, S, D = 1, 2048, 1024
        Df = D // 2 + 1
        return {
            "x": _w((lambda *s: torch.randn(*s, dtype=torch.bfloat16) * 0.05)(B, S, D)),
            "w_r": _w((lambda *s: torch.randn(*s, dtype=torch.bfloat16) * 1.6)(S, Df) * 0.02),
            "w_i": _w((lambda *s: torch.randn(*s, dtype=torch.bfloat16) * 1.6)(S, Df) * 0.02),
        }
    _A_in = _A_setup()

    def _B_setup():
        B, S, D, F = 1, 2048, 1024, 3584
        return {
            "x": _w((lambda *s: torch.randn(*s, dtype=torch.bfloat16) * 0.05)(B, S, D)),
            "w_gate": _w((lambda *s: torch.randn(*s, dtype=torch.bfloat16) * 0.05)(D, F)),
            "w_up": _w((lambda *s: torch.randn(*s, dtype=torch.bfloat16) * 0.05)(D, F)),
            "w_down": _w((lambda *s: torch.randn(*s, dtype=torch.bfloat16) * 0.05)(F, D)),
        }
    _B_in = _B_setup()

    return {
        "A_x": _A_in["x"],
        "A_w_r": _A_in["w_r"],
        "A_w_i": _A_in["w_i"],
        "B_x": _B_in["x"],
        "B_w_gate": _B_in["w_gate"],
        "B_w_up": _B_in["w_up"],
        "B_w_down": _B_in["w_down"],
        "gate_w": _w((lambda *s: torch.randn(*s, dtype=torch.bfloat16) * 0.05)(1024) * 16.0),
    }

def reference(A_x, A_w_r, A_w_i, B_x, B_w_gate, B_w_up, B_w_down, gate_w):
    def _A_ref(x, w_r, w_i):
        B, S, D = x.shape
        X = (lambda x, axes=(-2,-1): torch.fft.rfft2(x.float(), dim=axes))(x, axes=(-2, -1))
        W = w_r + 1j * w_i
        X = X * W
        y = (lambda x, s, axes=(-2,-1): torch.fft.irfft2(x, s=s, dim=axes).to(torch.bfloat16))(X, (S, D), axes=(-2, -1))
        return y

    def _B_ref(x, w_gate, w_up, w_down):
        g = _silu(x @ w_gate)
        u = x @ w_up
        return (g * u) @ w_down

    x = A_x
    g = _sigmoid((x * gate_w).sum(-1, keepdims=True))
    a = _A_ref(x, A_w_r, A_w_i)
    b = _B_ref(x, B_w_gate, B_w_up, B_w_down)
    return g * a + (1 - g) * b
```

```cpp
#include <hip/hip_runtime.h>
#include <hip/hip_bf16.h>
typedef unsigned short u16;
constexpr int S=2048, D=1024, DF=513, F=3584;
__device__ inline float bf2f(u16 v){ return __uint_as_float(((unsigned)v)<<16); }
__device__ inline u16 f2bf(float f){ unsigned u=__float_as_uint(f); u += 0x7fffu + ((u>>16)&1u); return (u16)(u>>16); }
__device__ inline float rbf(float f){ return bf2f(f2bf(f)); }

template<int N,int LG> __device__ void fft_lds(float2* buf, int sign){
  int t=threadIdx.x;
  for(int s=1;s<=LG;++s){
    int half=1<<(s-1);
    int j=t&(half-1), base=(t>>(s-1))<<s;
    float ang = sign*(float)j/(float)(half);
    float sn,cs; sincospif(ang,&sn,&cs);
    float2 a=buf[base+j], b=buf[base+j+half];
    float2 w={b.x*cs-b.y*sn, b.x*sn+b.y*cs};
    buf[base+j]={a.x+w.x,a.y+w.y}; buf[base+j+half]={a.x-w.x,a.y-w.y};
    __syncthreads();
  }
}
__global__ __launch_bounds__(512) void k_row_fwd(const u16* x, float2* X1){
  __shared__ float2 buf[D]; int s=blockIdx.x;
  for(int i=threadIdx.x;i<D;i+=512){ int r=__brev(i)>>22; buf[r]={bf2f(x[s*D+i]),0.f}; }
  __syncthreads(); fft_lds<D,10>(buf,-1);
  for(int i=threadIdx.x;i<DF;i+=512) X1[(size_t)i*S+s]=buf[i];
}
__global__ __launch_bounds__(1024) void k_col(float2* X1, const u16* wr, const u16* wi){
  __shared__ float2 buf[S]; __shared__ float2 b2[S]; int k=blockIdx.x;
  for(int i=threadIdx.x;i<S;i+=1024){ int r=__brev(i)>>21; buf[r]=X1[(size_t)k*S+i]; }
  __syncthreads(); fft_lds<S,11>(buf,-1);
  for(int i=threadIdx.x;i<S;i+=1024){ float a=bf2f(wr[i*DF+k]), b=bf2f(wi[i*DF+k]); float2 v=buf[i]; int r=__brev(i)>>21; b2[r]={v.x*a-v.y*b, v.x*b+v.y*a}; }
  __syncthreads(); fft_lds<S,11>(b2,+1);
  for(int i=threadIdx.x;i<S;i+=1024) X1[(size_t)k*S+i]=b2[i];
}
__global__ __launch_bounds__(512) void k_row_inv(const float2* X1, float* a){
  __shared__ float2 buf[D]; int s=blockIdx.x;
  for(int i=threadIdx.x;i<D;i+=512){ int r=__brev(i)>>22; float2 v; if(i<=512){ v=X1[(size_t)i*S+s]; if(i==0||i==512) v.y=0.f;} else { v=X1[(size_t)(D-i)*S+s]; v.y=-v.y;} buf[r]=v; }
  __syncthreads(); fft_lds<D,10>(buf,+1);
  for(int i=threadIdx.x;i<D;i+=512) a[s*D+i]=rbf(buf[i].x*(1.0f/(float)(S*D)));
}
__global__ void k_gate(const u16* x,const u16* gw,float* g){
  int s=blockIdx.x; float acc=0; for(int i=threadIdx.x;i<D;i+=64) acc+=rbf(bf2f(x[s*D+i])*bf2f(gw[i]));
  for(int o=32;o;o>>=1) acc+=__shfl_xor(acc,o);
  if(threadIdx.x==0){ float z=rbf(acc); g[s]=rbf(1.f/(1.f+expf(-z))); }
}
template<int MODE> __global__ __launch_bounds__(256) void k_gemm(const u16* A,const u16* B,const u16* B2,int N,int K,u16* out,const float* a,const float* g){
  __shared__ float As[16][65], Bs[16][64], Bs2[16][64];
  int bm=blockIdx.y*64,bn=blockIdx.x*64,t=threadIdx.x,tx=t&15,ty=t>>4;
  float c[4][4]={},c2[4][4]={};
  for(int k0=0;k0<K;k0+=16){
    for(int i=t;i<1024;i+=256){ int r=i>>4,kk=i&15; As[kk][r]=bf2f(A[(size_t)(bm+r)*K+k0+kk]); int k2=i>>6,n=i&63; Bs[k2][n]=bf2f(B[(size_t)(k0+k2)*N+bn+n]); if(MODE==0) Bs2[k2][n]=bf2f(B2[(size_t)(k0+k2)*N+bn+n]); }
    __syncthreads();
    for(int kk=0;kk<16;++kk){ float av[4],bv[4],bv2[4]; for(int i=0;i<4;++i){av[i]=As[kk][ty*4+i]; bv[i]=Bs[kk][tx*4+i]; if(MODE==0) bv2[i]=Bs2[kk][tx*4+i];}
      for(int i=0;i<4;++i)for(int j=0;j<4;++j){ c[i][j]+=av[i]*bv[j]; if(MODE==0) c2[i][j]+=av[i]*bv2[j]; } }
    __syncthreads();
  }
  for(int i=0;i<4;++i)for(int j=0;j<4;++j){ int r=bm+ty*4+i,n=bn+tx*4+j;
    if(MODE==0){ float gg=rbf(c[i][j]); float sl=rbf(gg/(1.f+expf(-gg))); out[(size_t)r*N+n]=f2bf(sl*rbf(c2[i][j])); }
    else { float b=rbf(c[i][j]); float gv=g[r]; out[(size_t)r*N+n]=f2bf(rbf(gv*a[(size_t)r*N+n])+rbf(rbf(1.f-gv)*b)); } }
}
extern "C" void kernel_launch(void* const* d_in, const int* in_sizes, int n_in, void* d_out, int out_size, void* d_ws, size_t ws_size, hipStream_t stream){
  const u16* x=(const u16*)d_in[0]; const u16* wr=(const u16*)d_in[1]; const u16* wi=(const u16*)d_in[2];
  const u16* wg=(const u16*)d_in[4]; const u16* wu=(const u16*)d_in[5]; const u16* wd=(const u16*)d_in[6]; const u16* gw=(const u16*)d_in[7];
  char* ws=(char*)d_ws; float2* X1=(float2*)ws; float* a=(float*)(ws+(16<<20)); float* g=(float*)(ws+(32<<20)); u16* h=(u16*)(ws+(40<<20));
  k_row_fwd<<<S,512,0,stream>>>(x,X1);
  k_col<<<DF,1024,0,stream>>>(X1,wr,wi);
  k_row_inv<<<S,512,0,stream>>>(X1,a);
  k_gate<<<S,64,0,stream>>>(x,gw,g);
  k_gemm<0><<<dim3(F/64,S/64),256,0,stream>>>(x,wg,wu,F,D,h,nullptr,nullptr);
  k_gemm<1><<<dim3(D/64,S/64),256,0,stream>>>(h,wd,nullptr,D,F,(u16*)d_out,a,g);
}
```

```cpp
#include <hip/hip_runtime.h>
#define WS_REC 0
#define WS_CNT 4096
#define WS_G 24576
#define WS_ST (64<<10)
#define WS_U (1<<20)
#define WS_V (5<<20)
#define WS_A (9<<20)
#define WS_H (13<<20)
#define WS_X (28<<20)
#define BRING 65536
typedef unsigned short u16; typedef unsigned long long u64;
typedef float f32x32 __attribute__((ext_vector_type(32)));
typedef float f32x16 __attribute__((ext_vector_type(16)));
typedef float f32x8 __attribute__((ext_vector_type(8)));
typedef float f32x4 __attribute__((ext_vector_type(4)));
typedef float f32x2 __attribute__((ext_vector_type(2)));
typedef __bf16 bf16x2 __attribute__((ext_vector_type(2)));
typedef __bf16 bf16x4 __attribute__((ext_vector_type(4)));
typedef __bf16 bf16x8 __attribute__((ext_vector_type(8)));
typedef unsigned u32x2 __attribute__((ext_vector_type(2)));
typedef unsigned u32x4 __attribute__((ext_vector_type(4)));
typedef unsigned u32x8 __attribute__((ext_vector_type(8)));
typedef unsigned u32x16 __attribute__((ext_vector_type(16)));
#define DI __device__ __forceinline__
#define LAS __attribute__((address_space(3)))
DI unsigned rfl(unsigned x) { return __builtin_amdgcn_readfirstlane(x); }
DI unsigned sgpr(unsigned x) { unsigned r; asm("s_nop 0\n\tv_readfirstlane_b32 %0, %1" : "=s"(r) : "v"(x)); return r; }
DI unsigned cvtpk(float lo, float hi) { f32x2 v = {lo, hi}; bf16x2 b = __builtin_convertvector(v, bf16x2); return __builtin_bit_cast(unsigned, b); }
DI float blo(unsigned u) { return __uint_as_float(u << 16); }
DI float bhi(unsigned u) { return __uint_as_float(u & 0xffff0000u); }
DI float swiglu(float g, float u) { const float e = __builtin_amdgcn_exp2f(g * -1.44269504f); return g * u * __builtin_amdgcn_rcpf(1.f + e); }
#define STAMP_ARG
#define STAMP_PASS
#define STAMP(k)
#define G1_START \
  "v_mbcnt_lo_u32_b32 v240, -1, 0\n\t" \
  "v_mbcnt_hi_u32_b32 v240, -1, v240\n\t" \
  "s_and_b32 s52, s85, 3\n\t" \
  "s_mul_i32 s52, s52, 114688\n\t" \
  "s_add_u32 s52, s52, 3072\n\t" \
  "v_lshrrev_b32 v241, 4, v240\n\t" \
  "v_and_b32 v242, 15, v240\n\t" \
  "v_lshlrev_b32 v243, 1, v241\n\t" \
  "v_xor_b32 v242, v242, v243\n\t" \
  "v_lshlrev_b32 v242, 4, v242\n\t" \
  "v_mov_b32 v243, 7168\n\t" \
  "v_mad_u32_u24 v243, v241, v243, s52\n\t" \
  "v_add_u32 v236, v243, v242\n\t" \
  "v_add_u32 v237, 27648, v236\n\t" \
  "v_xor_b32 v238, 0x80, v242\n\t" \
  "v_add_u32 v243, 55296, v243\n\t" \
  "v_add_u32 v238, v238, v243\n\t" \
  "v_add_u32 v239, 27648, v238\n\t" \
  "s_mov_b32 m0, s51\n\t" \
  "s_nop 0\n\t" \
  "buffer_load_dwordx4 v236, s[44:47], 0 offen offset:0 lds\n\t" \
  "buffer_load_dwordx4 v237, s[44:47], 0 offen offset:1024 lds\n\t" \
  "buffer_load_dwordx4 v238, s[44:47], 0 offen offset:2048 lds\n\t" \
  "buffer_load_dwordx4 v239, s[44:47], 0 offen offset:3072 lds\n\t" \
  "s_lshl_b32 s52, s85, 16\n\t" \
  "s_add_u32 s52, s52, 3072\n\t" \
  "v_lshrrev_b32 v243, 3, v240\n\t" \
  "v_and_b32 v234, 7, v240\n\t" \
  "v_lshl_add_u32 v243, v243, 11, s52\n\t" \
  "v_xor_b32 v234, v234, v241\n\t" \
  "v_lshlrev_b32 v234, 4, v234\n\t" \
  "v_add_u32 v232, v243, v234\n\t" \
  "v_xor_b32 v234, 64, v234\n\t" \
  "v_add_u32 v243, 15360, v243\n\t" \
  "v_add_u32 v233, v243, v234\n\t" \
  "v_add_u32 v234, 30720, v232\n\t" \
  "v_add_u32 v235, 30720, v233\n\t" \
  "s_mov_b32 m0, s50\n\t" \
  "v_lshlrev_b32 v243, 4, v240\n\t" \
  "buffer_load_dwordx4 v232, s[40:43], 0 offen offset:0 lds\n\t" \
  "buffer_load_dwordx4 v233, s[40:43], 0 offen offset:1024 lds\n\t" \
  "buffer_load_dwordx4 v234, s[40:43], 0 offen offset:2048 lds\n\t" \
  "buffer_load_dwordx4 v235, s[40:43], 0 offen offset:3072 lds\n\t"
#define G1_LOOP \
  "s_lshl_b32 s52, s85, 12\n\t" \
  "s_sub_u32 s53, s50, s52\n\t" \
  "s_lshr_b32 s52, s85, 2\n\t" \
  "s_lshl_b32 s52, s52, 14\n\t" \
  "s_add_u32 s52, s52, s53\n\t" \
  "v_and_b32 v224, 15, v240\n\t" \
  "v_lshrrev_b32 v225, 1, v224\n\t" \
  "v_lshl_add_u32 v224, v224, 7, s52\n\t" \
  "v_xor_b32 v225, v225, v241\n\t" \
  "v_lshlrev_b32 v226, 4, v225\n\t" \
  "v_xor_b32 v225, 64, v226\n\t" \
  "v_add_u32 v225, v225, v224\n\t" \
  "v_add_u32 v224, v226, v224\n\t" \
  "s_add_u32 s53, s53, 65536\n\t" \
  "s_and_b32 s52, s85, 3\n\t" \
  "s_lshl_b32 s52, s52, 6\n\t" \
  "v_lshlrev_b32 v226, 3, v240\n\t" \
  "v_lshl_add_u32 v227, v241, 11, s53\n\t" \
  "v_and_b32 v228, 0x60, v226\n\t" \
  "v_and_b32 v229, 24, v226\n\t" \
  "v_lshl_add_u32 v227, v228, 3, v227\n\t" \
  "v_and_b32 v226, 0xe0, v226\n\t" \
  "v_add_u32 v227, v227, v229\n\t" \
  "v_xor_b32 v226, s52, v226\n\t" \
  "v_add_u32 v228, v226, v227\n\t" \
  "v_xad_u32 v229, v226, 32, v227\n\t" \
  "s_lshl_b32 s52, s84, 3\n\t" \
  "s_cmp_eq_u32 s85, 0\n\t" \
  "s_cselect_b32 s52, s52, 0x40000000\n\t" \
  "v_mov_b32 v230, 0x40000000\n\t" \
  "v_writelane_b32 v230, s52, 0\n\t" \
  "s_lshl_b32 s52, s85, 14\n\t" \
  "s_add_u32 s52, s52, 3072\n\t" \
  "v_lshl_add_u32 v245, v241, 11, s52\n\t" \
  "v_add_u32 v244, v245, v242\n\t" \
  "v_xor_b32 v242, 0x80, v242\n\t" \
  "v_add_u32 v245, 7168, v245\n\t" \
  "v_add_u32 v245, v245, v242\n\t" \
  "buffer_load_dwordx2 v[248:249], v230, s[72:75], 0 offen sc1\n\t" \
  "s_mov_b32 s53, 0\n\t" \
  "s_mov_b32 s58, 0x8000\n\t" \
  "s_mov_b32 s59, 0x8000\n\t" \
  "s_mov_b32 s48, 128\n\t" \
  "s_mov_b32 s49, 458752\n\t" \
  "s_mov_b32 vcc_lo, s82\n\t" \
  "s_mov_b32 vcc_hi, s82\n\t" \
  "v_mov_b32 v253, 0\n\t" \
  "v_mov_b32 v226, v228\n\t" \
  "v_mov_b32 v227, v229\n\t" \
  "v_mov_b32 v240, v228\n\t" \
  "v_mov_b32 v241, v229\n\t" \
  "s_nop 0\n\t" \
  "s_add_u32 m0, s51, s58\n\t" \
  "s_nop 0\n\t" \
  "buffer_load_dwordx4 v236, s[44:47], s49 offen offset:0 lds\n\t" \
  "buffer_load_dwordx4 v237, s[44:47], s49 offen offset:1024 lds\n\t" \
  "buffer_load_dwordx4 v238, s[44:47], s49 offen offset:2048 lds\n\t" \
  "buffer_load_dwordx4 v239, s[44:47], s49 offen offset:3072 lds\n\t" \
  "s_add_u32 s49, s49, 458752\n\t" \
  "s_add_u32 s58, s58, 0x8000\n\t" \
  "s_cmp_eq_u32 s58, 0x18000\n\t" \
  "s_cselect_b32 s58, 0, s58\n\t" \
  "s_add_u32 m0, s50, s59\n\t" \
  "s_nop 0\n\t" \
  "buffer_load_dwordx4 v232, s[40:43], s48 offen offset:0 lds\n\t" \
  "buffer_load_dwordx4 v233, s[40:43], s48 offen offset:1024 lds\n\t" \
  "buffer_load_dwordx4 v234, s[40:43], s48 offen offset:2048 lds\n\t" \
  "buffer_load_dwordx4 v235, s[40:43], s48 offen offset:3072 lds\n\t" \
  "s_add_u32 s48, s48, 128\n\t" \
  "s_xor_b32 s59, s59, 0x8000\n\t" \
  "s_waitcnt vmcnt(9)\n\t" \
  "s_barrier\n\t" \
  "ds_read_b64_tr_b16 v[160:161], v226 offset:0\n\t" \
  "ds_read_b128 v[128:131], v224 offset:0\n\t" \
  "ds_read_b64_tr_b16 v[162:163], v226 offset:1024\n\t" \
  "ds_read_b128 v[132:135], v224 offset:2048\n\t" \
  "ds_read_b64_tr_b16 v[164:165], v227 offset:0\n\t" \
  "ds_read_b128 v[136:139], v224 offset:4096\n\t" \
  "ds_read_b64_tr_b16 v[166:167], v227 offset:1024\n\t" \
  "ds_read_b128 v[140:143], v224 offset:6144\n\t" \
  "ds_read_b64_tr_b16 v[168:169], v226 offset:16384\n\t" \
  "ds_read_b128 v[144:147], v224 offset:8192\n\t" \
  "ds_read_b64_tr_b16 v[170:171], v226 offset:17408\n\t" \
  "ds_read_b128 v[148:151], v224 offset:10240\n\t" \
  "ds_read_b64_tr_b16 v[172:173], v227 offset:16384\n\t" \
  "ds_read_b128 v[152:155], v224 offset:12288\n\t" \
  "ds_read_b64_tr_b16 v[174:175], v227 offset:17408\n\t" \
  "ds_read_b128 v[156:159], v224 offset:14336\n\t" \
  "s_waitcnt lgkmcnt(0)\n\t" \
  "s_cbranch_vccnz 3f\n\t" \
  "v_mfma_f32_16x16x32_bf16 v[0:3], v[160:163], v[128:131], 0\n\t" \
  "s_add_u32 s53, s53, 0x8000\n\t" \
  "s_cmp_eq_u32 s53, 0x18000\n\t" \
  "s_cselect_b32 s53, 0, s53\n\t" \
  "ds_read_b64_tr_b16 v[208:209], v240 offset:8192\n\t" \
  "v_mfma_f32_16x16x32_bf16 v[16:19], v[160:163], v[132:135], 0\n\t" \
  "s_add_u32 m0, s51, s58\n\t" \
  "ds_read_b128 v[176:179], v225 offset:0\n\t" \
  "v_mfma_f32_16x16x32_bf16 v[32:35], v[160:163], v[136:139], 0\n\t" \
  "buffer_load_dwordx4 v236, s[44:47], s49 offen offset:0 lds\n\t" \
  "ds_read_b64_tr_b16 v[210:211], v240 offset:9216\n\t" \
  "v_mfma_f32_16x16x32_bf16 v[48:51], v[160:163], v[140:143], 0\n\t" \
  "ds_read_b128 v[180:183], v225 offset:2048\n\t" \
  "v_mfma_f32_16x16x32_bf16 v[64:67], v[160:163], v[144:147], 0\n\t" \
  "ds_read_b64_tr_b16 v[212:213], v241 offset:8192\n\t" \
  "v_mfma_f32_16x16x32_bf16 v[80:83], v[160:163], v[148:151], 0\n\t" \
  "ds_read_b128 v[184:187], v225 offset:4096\n\t" \
  "v_mfma_f32_16x16x32_bf16 v[96:99], v[160:163], v[152:155], 0\n\t" \
  "ds_read_b64_tr_b16 v[214:215], v241 offset:9216\n\t" \
  "v_mfma_f32_16x16x32_bf16 v[112:115], v[160:163], v[156:159], 0\n\t" \
  "ds_read_b128 v[188:191], v225 offset:6144\n\t" \
  "v_mfma_f32_16x16x32_bf16 v[4:7], v[164:167], v[128:131], 0\n\t" \
  "ds_read_b64_tr_b16 v[216:217], v240 offset:24576\n\t" \
  "v_mfma_f32_16x16x32_bf16 v[20:23], v[164:167], v[132:135], 0\n\t" \
  "buffer_load_dwordx4 v237, s[44:47], s49 offen offset:1024 lds\n\t" \
  "ds_read_b128 v[192:195], v225 offset:8192\n\t" \
  "v_mfma_f32_16x16x32_bf16 v[36:39], v[164:167], v[136:139], 0\n\t" \
  "ds_read_b64_tr_b16 v[218:219], v240 offset:25600\n\t" \
  "v_mfma_f32_16x16x32_bf16 v[52:55], v[164:167], v[140:143], 0\n\t" \
  "ds_read_b128 v[196:199], v225 offset:10240\n\t" \
  "v_mfma_f32_16x16x32_bf16 v[68:71], v[164:167], v[144:147], 0\n\t" \
  "ds_read_b64_tr_b16 v[220:221], v241 offset:24576\n\t" \
  "v_mfma_f32_16x16x32_bf16 v[84:87], v[164:167], v[148:151], 0\n\t" \
  "ds_read_b128 v[200:203], v225 offset:12288\n\t" \
  "v_mfma_f32_16x16x32_bf16 v[100:103], v[164:167], v[152:155], 0\n\t" \
  "ds_read_b64_tr_b16 v[222:223], v241 offset:25600\n\t" \
  "v_mfma_f32_16x16x32_bf16 v[116:119], v[164:167], v[156:159], 0\n\t" \
  "ds_read_b128 v[204:207], v225 offset:14336\n\t" \
  "v_mfma_f32_16x16x32_bf16 v[8:11], v[168:171], v[128:131], 0\n\t" \
  "buffer_load_dwordx4 v238, s[44:47], s49 offen offset:2048 lds\n\t" \
  "v_mfma_f32_16x16x32_bf16 v[24:27], v[168:171], v[132:135], 0\n\t" \
  "v_mfma_f32_16x16x32_bf16 v[40:43], v[168:171], v[136:139], 0\n\t" \
  "v_mfma_f32_16x16x32_bf16 v[56:59], v[168:171], v[140:143], 0\n\t" \
  "v_mfma_f32_16x16x32_bf16 v[72:75], v[168:171], v[144:147], 0\n\t" \
  "v_mfma_f32_16x16x32_bf16 v[88:91], v[168:171], v[148:151], 0\n\t" \
  "v_mfma_f32_16x16x32_bf16 v[104:107], v[168:171], v[152:155], 0\n\t" \
  "v_mfma_f32_16x16x32_bf16 v[120:123], v[168:171], v[156:159], 0\n\t" \
  "buffer_load_dwordx4 v239, s[44:47], s49 offen offset:3072 lds\n\t" \
  "v_mfma_f32_16x16x32_bf16 v[12:15], v[172:175], v[128:131], 0\n\t" \
  "s_add_u32 s49, s49, 458752\n\t" \
  "s_add_u32 s58, s58, 0x8000\n\t" \
  "s_cmp_eq_u32 s58, 0x18000\n\t" \
  "s_cselect_b32 s58, 0, s58\n\t" \
  "v_xor_b32 v224, 0x8000, v224\n\t" \
  "v_mfma_f32_16x16x32_bf16 v[28:31], v[172:175], v[132:135], 0\n\t" \
  "v_add_u32 v226, s53, v228\n\t" \
  "v_mfma_f32_16x16x32_bf16 v[44:47], v[172:175], v[136:139], 0\n\t" \
  "v_add_u32 v227, s53, v229\n\t" \
  "v_mfma_f32_16x16x32_bf16 v[60:63], v[172:175], v[140:143], 0\n\t" \
  "v_mfma_f32_16x16x32_bf16 v[76:79], v[172:175], v[144:147], 0\n\t" \
  "v_mfma_f32_16x16x32_bf16 v[92:95], v[172:175], v[148:151], 0\n\t" \
  "v_mfma_f32_16x16x32_bf16 v[108:111], v[172:175], v[152:155], 0\n\t" \
  "v_mfma_f32_16x16x32_bf16 v[124:127], v[172:175], v[156:159], 0\n\t" \
  "s_waitcnt vmcnt(4) lgkmcnt(0)\n\t" \
  "s_barrier\n\t" \
  "v_mfma_f32_16x16x32_bf16 v[0:3], v[208:211], v[176:179], v[0:3]\n\t" \
  "v_add_u32 v231, 1, v248\n\t" \
  "v_cmp_eq_u32 s[54:55], v231, v249\n\t" \
  "s_nop 1\n\t" \
  "v_addc_co_u32 v231, s[54:55], 0, v231, s[54:55]\n\t" \
  "buffer_store_dword v231, v230, s[72:75], 0 offen sc1\n\t" \
  "ds_read_b64_tr_b16 v[160:161], v226 offset:0\n\t" \
  "v_mfma_f32_16x16x32_bf16 v[16:19], v[208:211], v[180:183], v[16:19]\n\t" \
  "s_add_u32 m0, s50, s59\n\t" \
  "ds_read_b128 v[128:131], v224 offset:0\n\t" \
  "v_mfma_f32_16x16x32_bf16 v[32:35], v[208:211], v[184:187], v[32:35]\n\t" \
  "buffer_load_dwordx4 v232, s[40:43], s48 offen offset:0 lds\n\t" \
  "ds_read_b64_tr_b16 v[162:163], v226 offset:1024\n\t" \
  "v_mfma_f32_16x16x32_bf16 v[48:51], v[208:211], v[188:191], v[48:51]\n\t" \
  "ds_read_b128 v[132:135], v224 offset:2048\n\t" \
  "v_mfma_f32_16x16x32_bf16 v[64:67], v[208:211], v[192:195], v[64:67]\n\t" \
  "ds_read_b64_tr_b16 v[164:165], v227 offset:0\n\t" \
  "v_mfma_f32_16x16x32_bf16 v[80:83], v[208:211], v[196:199], v[80:83]\n\t" \
  "ds_read_b128 v[136:139], v224 offset:4096\n\t" \
  "v_mfma_f32_16x16x32_bf16 v[96:99], v[208:211], v[200:203], v[96:99]\n\t" \
  "ds_read_b64_tr_b16 v[166:167], v227 offset:1024\n\t" \
  "v_mfma_f32_16x16x32_bf16 v[112:115], v[208:211], v[204:207], v[112:115]\n\t" \
  "ds_read_b128 v[140:143], v224 offset:6144\n\t" \
  "v_mfma_f32_16x16x32_bf16 v[4:7], v[212:215], v[176:179], v[4:7]\n\t" \
  "ds_read_b64_tr_b16 v[168:169], v226 offset:16384\n\t" \
  "v_mfma_f32_16x16x32_bf16 v[20:23], v[212:215], v[180:183], v[20:23]\n\t" \
  "buffer_load_dwordx4 v233, s[40:43], s48 offen offset:1024 lds\n\t" \
  "ds_read_b128 v[144:147], v224 offset:8192\n\t" \
  "v_mfma_f32_16x16x32_bf16 v[36:39], v[212:215], v[184:187], v[36:39]\n\t" \
  "ds_read_b64_tr_b16 v[170:171], v226 offset:17408\n\t" \
  "v_mfma_f32_16x16x32_bf16 v[52:55], v[212:215], v[188:191], v[52:55]\n\t" \
  "ds_read_b128 v[148:151], v224 offset:10240\n\t" \
  "v_mfma_f32_16x16x32_bf16 v[68:71], v[212:215], v[192:195], v[68:71]\n\t" \
  "ds_read_b64_tr_b16 v[172:173], v227 offset:16384\n\t" \
  "v_mfma_f32_16x16x32_bf16 v[84:87], v[212:215], v[196:199], v[84:87]\n\t" \
  "ds_read_b128 v[152:155], v224 offset:12288\n\t" \
  "v_mfma_f32_16x16x32_bf16 v[100:103], v[212:215], v[200:203], v[100:103]\n\t" \
  "ds_read_b64_tr_b16 v[174:175], v227 offset:17408\n\t" \
  "v_mfma_f32_16x16x32_bf16 v[116:119], v[212:215], v[204:207], v[116:119]\n\t" \
  "ds_read_b128 v[156:159], v224 offset:14336\n\t" \
  "v_mfma_f32_16x16x32_bf16 v[8:11], v[216:219], v[176:179], v[8:11]\n\t" \
  "buffer_load_dwordx4 v234, s[40:43], s48 offen offset:2048 lds\n\t" \
  "v_mfma_f32_16x16x32_bf16 v[24:27], v[216:219], v[180:183], v[24:27]\n\t" \
  "v_mfma_f32_16x16x32_bf16 v[40:43], v[216:219], v[184:187], v[40:43]\n\t" \
  "v_mfma_f32_16x16x32_bf16 v[56:59], v[216:219], v[188:191], v[56:59]\n\t" \
  "v_mfma_f32_16x16x32_bf16 v[72:75], v[216:219], v[192:195], v[72:75]\n\t" \
  "v_mfma_f32_16x16x32_bf16 v[88:91], v[216:219], v[196:199], v[88:91]\n\t" \
  "v_mfma_f32_16x16x32_bf16 v[104:107], v[216:219], v[200:203], v[104:107]\n\t" \
  "v_mfma_f32_16x16x32_bf16 v[120:123], v[216:219], v[204:207], v[120:123]\n\t" \
  "buffer_load_dwordx4 v235, s[40:43], s48 offen offset:3072 lds\n\t" \
  "v_mfma_f32_16x16x32_bf16 v[12:15], v[220:223], v[176:179], v[12:15]\n\t" \
  "s_add_u32 s48, s48, 128\n\t" \
  "s_xor_b32 s59, s59, 0x8000\n\t" \
  "v_xor_b32 v225, 0x8000, v225\n\t" \
  "v_mfma_f32_16x16x32_bf16 v[28:31], v[220:223], v[180:183], v[28:31]\n\t" \
  "v_add_u32 v240, s53, v228\n\t" \
  "v_mfma_f32_16x16x32_bf16 v[44:47], v[220:223], v[184:187], v[44:47]\n\t" \
  "v_add_u32 v241, s53, v229\n\t" \
  "v_mfma_f32_16x16x32_bf16 v[60:63], v[220:223], v[188:191], v[60:63]\n\t" \
  "v_mfma_f32_16x16x32_bf16 v[76:79], v[220:223], v[192:195], v[76:79]\n\t" \
  "v_mfma_f32_16x16x32_bf16 v[92:95], v[220:223], v[196:199], v[92:95]\n\t" \
  "v_mfma_f32_16x16x32_bf16 v[108:111], v[220:223], v[200:203], v[108:111]\n\t" \
  "v_mfma_f32_16x16x32_bf16 v[124:127], v[220:223], v[204:207], v[124:127]\n\t" \
  "s_waitcnt lgkmcnt(0)\n\t" \
  "v_mfma_f32_16x16x32_bf16 v[0:3], v[160:163], v[128:131], v[0:3]\n\t" \
  "s_add_u32 s53, s53, 0x8000\n\t" \
  "s_cmp_eq_u32 s53, 0x18000\n\t" \
  "s_cselect_b32 s53, 0, s53\n\t" \
  "ds_read_b64_tr_b16 v[208:209], v240 offset:8192\n\t" \
  "v_mfma_f32_16x16x32_bf16 v[16:19], v[160:163], v[132:135], v[16:19]\n\t" \
  "s_add_u32 m0, s51, s58\n\t" \
  "ds_read_b128 v[176:179], v225 offset:0\n\t" \
  "v_mfma_f32_16x16x32_bf16 v[32:35], v[160:163], v[136:139], v[32:35]\n\t" \
  "buffer_load_dwordx4 v236, s[44:47], s49 offen offset:0 lds\n\t" \
  "ds_read_b64_tr_b16 v[210:211], v240 offset:9216\n\t" \
  "v_mfma_f32_16x16x32_bf16 v[48:51], v[160:163], v[140:143], v[48:51]\n\t" \
  "ds_read_b128 v[180:183], v225 offset:2048\n\t" \
  "v_mfma_f32_16x16x32_bf16 v[64:67], v[160:163], v[144:147], v[64:67]\n\t" \
  "ds_read_b64_tr_b16 v[212:213], v241 offset:8192\n\t" \
  "v_mfma_f32_16x16x32_bf16 v[80:83], v[160:163], v[148:151], v[80:83]\n\t" \
  "ds_read_b128 v[184:187], v225 offset:4096\n\t" \
  "v_mfma_f32_16x16x32_bf16 v[96:99], v[160:163], v[152:155], v[96:99]\n\t" \
  "ds_read_b64_tr_b16 v[214:215], v241 offset:9216\n\t" \
  "v_mfma_f32_16x16x32_bf16 v[112:115], v[160:163], v[156:159], v[112:115]\n\t" \
  "ds_read_b128 v[188:191], v225 offset:6144\n\t" \
  "v_mfma_f32_16x16x32_bf16 v[4:7], v[164:167], v[128:131], v[4:7]\n\t" \
  "ds_read_b64_tr_b16 v[216:217], v240 offset:24576\n\t" \
  "v_mfma_f32_16x16x32_bf16 v[20:23], v[164:167], v[132:135], v[20:23]\n\t" \
  "buffer_load_dwordx4 v237, s[44:47], s49 offen offset:1024 lds\n\t" \
  "ds_read_b128 v[192:195], v225 offset:8192\n\t" \
  "v_mfma_f32_16x16x32_bf16 v[36:39], v[164:167], v[136:139], v[36:39]\n\t" \
  "ds_read_b64_tr_b16 v[218:219], v240 offset:25600\n\t" \
  "v_mfma_f32_16x16x32_bf16 v[52:55], v[164:167], v[140:143], v[52:55]\n\t" \
  "ds_read_b128 v[196:199], v225 offset:10240\n\t" \
  "v_mfma_f32_16x16x32_bf16 v[68:71], v[164:167], v[144:147], v[68:71]\n\t" \
  "ds_read_b64_tr_b16 v[220:221], v241 offset:24576\n\t" \
  "v_mfma_f32_16x16x32_bf16 v[84:87], v[164:167], v[148:151], v[84:87]\n\t" \
  "ds_read_b128 v[200:203], v225 offset:12288\n\t" \
  "v_mfma_f32_16x16x32_bf16 v[100:103], v[164:167], v[152:155], v[100:103]\n\t" \
  "ds_read_b64_tr_b16 v[222:223], v241 offset:25600\n\t" \
  "v_mfma_f32_16x16x32_bf16 v[116:119], v[164:167], v[156:159], v[116:119]\n\t" \
  "ds_read_b128 v[204:207], v225 offset:14336\n\t" \
  "v_mfma_f32_16x16x32_bf16 v[8:11], v[168:171], v[128:131], v[8:11]\n\t" \
  "buffer_load_dwordx4 v238, s[44:47], s49 offen offset:2048 lds\n\t" \
  "v_mfma_f32_16x16x32_bf16 v[24:27], v[168:171], v[132:135], v[24:27]\n\t" \
  "v_mfma_f32_16x16x32_bf16 v[40:43], v[168:171], v[136:139], v[40:43]\n\t" \
  "v_mfma_f32_16x16x32_bf16 v[56:59], v[168:171], v[140:143], v[56:59]\n\t" \
  "v_mfma_f32_16x16x32_bf16 v[72:75], v[168:171], v[144:147], v[72:75]\n\t" \
  "v_mfma_f32_16x16x32_bf16 v[88:91], v[168:171], v[148:151], v[88:91]\n\t" \
  "v_mfma_f32_16x16x32_bf16 v[104:107], v[168:171], v[152:155], v[104:107]\n\t" \
  "v_mfma_f32_16x16x32_bf16 v[120:123], v[168:171], v[156:159], v[120:123]\n\t" \
  "buffer_load_dwordx4 v239, s[44:47], s49 offen offset:3072 lds\n\t" \
  "v_mfma_f32_16x16x32_bf16 v[12:15], v[172:175], v[128:131], v[12:15]\n\t" \
  "s_add_u32 s49, s49, 458752\n\t" \
  "s_add_u32 s58, s58, 0x8000\n\t" \
  "s_cmp_eq_u32 s58, 0x18000\n\t" \
  "s_cselect_b32 s58, 0, s58\n\t" \
  "v_xor_b32 v224, 0x8000, v224\n\t" \
  "v_mfma_f32_16x16x32_bf16 v[28:31], v[172:175], v[132:135], v[28:31]\n\t" \
  "v_add_u32 v226, s53, v228\n\t" \
  "v_mfma_f32_16x16x32_bf16 v[44:47], v[172:175], v[136:139], v[44:47]\n\t" \
  "v_add_u32 v227, s53, v229\n\t" \
  "v_mfma_f32_16x16x32_bf16 v[60:63], v[172:175], v[140:143], v[60:63]\n\t" \
  "v_mfma_f32_16x16x32_bf16 v[76:79], v[172:175], v[144:147], v[76:79]\n\t" \
  "v_mfma_f32_16x16x32_bf16 v[92:95], v[172:175], v[148:151], v[92:95]\n\t" \
  "v_mfma_f32_16x16x32_bf16 v[108:111], v[172:175], v[152:155], v[108:111]\n\t" \
  "v_mfma_f32_16x16x32_bf16 v[124:127], v[172:175], v[156:159], v[124:127]\n\t" \
  "s_waitcnt vmcnt(4) lgkmcnt(0)\n\t" \
  "s_barrier\n\t" \
  "v_mfma_f32_16x16x32_bf16 v[0:3], v[208:211], v[176:179], v[0:3]\n\t" \
  "ds_read_b64_tr_b16 v[160:161], v226 offset:0\n\t" \
  "v_mfma_f32_16x16x32_bf16 v[16:19], v[208:211], v[180:183], v[16:19]\n\t" \
  "s_add_u32 m0, s50, s59\n\t" \
  "ds_read_b128 v[128:131], v224 offset:0\n\t" \
  "v_mfma_f32_16x16x32_bf16 v[32:35], v[208:211], v[184:187], v[32:35]\n\t" \
  "buffer_load_dwordx4 v232, s[40:43], s48 offen offset:0 lds\n\t" \
  "ds_read_b64_tr_b16 v[162:163], v226 offset:1024\n\t" \
  "v_mfma_f32_16x16x32_bf16 v[48:51], v[208:211], v[188:191], v[48:51]\n\t" \
  "ds_read_b128 v[132:135], v224 offset:2048\n\t" \
  "v_mfma_f32_16x16x32_bf16 v[64:67], v[208:211], v[192:195], v[64:67]\n\t" \
  "ds_read_b64_tr_b16 v[164:165], v227 offset:0\n\t" \
  "v_mfma_f32_16x16x32_bf16 v[80:83], v[208:211], v[196:199], v[80:83]\n\t" \
  "ds_read_b128 v[136:139], v224 offset:4096\n\t" \
  "v_mfma_f32_16x16x32_bf16 v[96:99], v[208:211], v[200:203], v[96:99]\n\t" \
  "ds_read_b64_tr_b16 v[166:167], v227 offset:1024\n\t" \
  "v_mfma_f32_16x16x32_bf16 v[112:115], v[208:211], v[204:207], v[112:115]\n\t" \
  "ds_read_b128 v[140:143], v224 offset:6144\n\t" \
  "v_mfma_f32_16x16x32_bf16 v[4:7], v[212:215], v[176:179], v[4:7]\n\t" \
  "ds_read_b64_tr_b16 v[168:169], v226 offset:16384\n\t" \
  "v_mfma_f32_16x16x32_bf16 v[20:23], v[212:215], v[180:183], v[20:23]\n\t" \
  "buffer_load_dwordx4 v233, s[40:43], s48 offen offset:1024 lds\n\t" \
  "ds_read_b128 v[144:147], v224 offset:8192\n\t" \
  "v_mfma_f32_16x16x32_bf16 v[36:39], v[212:215], v[184:187], v[36:39]\n\t" \
  "ds_read_b64_tr_b16 v[170:171], v226 offset:17408\n\t" \
  "v_mfma_f32_16x16x32_bf16 v[52:55], v[212:215], v[188:191], v[52:55]\n\t" \
  "ds_read_b128 v[148:151], v224 offset:10240\n\t" \
  "v_mfma_f32_16x16x32_bf16 v[68:71], v[212:215], v[192:195], v[68:71]\n\t" \
  "ds_read_b64_tr_b16 v[172:173], v227 offset:16384\n\t" \
  "v_mfma_f32_16x16x32_bf16 v[84:87], v[212:215], v[196:199], v[84:87]\n\t" \
  "ds_read_b128 v[152:155], v224 offset:12288\n\t" \
  "v_mfma_f32_16x16x32_bf16 v[100:103], v[212:215], v[200:203], v[100:103]\n\t" \
  "ds_read_b64_tr_b16 v[174:175], v227 offset:17408\n\t" \
  "v_mfma_f32_16x16x32_bf16 v[116:119], v[212:215], v[204:207], v[116:119]\n\t" \
  "ds_read_b128 v[156:159], v224 offset:14336\n\t" \
  "v_mfma_f32_16x16x32_bf16 v[8:11], v[216:219], v[176:179], v[8:11]\n\t" \
  "buffer_load_dwordx4 v234, s[40:43], s48 offen offset:2048 lds\n\t" \
  "v_mfma_f32_16x16x32_bf16 v[24:27], v[216:219], v[180:183], v[24:27]\n\t" \
  "v_mfma_f32_16x16x32_bf16 v[40:43], v[216:219], v[184:187], v[40:43]\n\t" \
  "v_mfma_f32_16x16x32_bf16 v[56:59], v[216:219], v[188:191], v[56:59]\n\t" \
  "v_mfma_f32_16x16x32_bf16 v[72:75], v[216:219], v[192:195], v[72:75]\n\t" \
  "v_mfma_f32_16x16x32_bf16 v[88:91], v[216:219], v[196:199], v[88:91]\n\t" \
  "v_mfma_f32_16x16x32_bf16 v[104:107], v[216:219], v[200:203], v[104:107]\n\t" \
  "v_mfma_f32_16x16x32_bf16 v[120:123], v[216:219], v[204:207], v[120:123]\n\t" \
  "buffer_load_dwordx4 v235, s[40:43], s48 offen offset:3072 lds\n\t" \
  "v_mfma_f32_16x16x32_bf16 v[12:15], v[220:223], v[176:179], v[12:15]\n\t" \
  "s_add_u32 s48, s48, 128\n\t" \
  "s_xor_b32 s59, s59, 0x8000\n\t" \
  "v_xor_b32 v225, 0x8000, v225\n\t" \
  "v_mfma_f32_16x16x32_bf16 v[28:31], v[220:223], v[180:183], v[28:31]\n\t" \
  "v_add_u32 v240, s53, v228\n\t" \
  "v_mfma_f32_16x16x32_bf16 v[44:47], v[220:223], v[184:187], v[44:47]\n\t" \
  "v_add_u32 v241, s53, v229\n\t" \
  "v_mfma_f32_16x16x32_bf16 v[60:63], v[220:223], v[188:191], v[60:63]\n\t" \
  "v_mfma_f32_16x16x32_bf16 v[76:79], v[220:223], v[192:195], v[76:79]\n\t" \
  "v_mfma_f32_16x16x32_bf16 v[92:95], v[220:223], v[196:199], v[92:95]\n\t" \
  "v_mfma_f32_16x16x32_bf16 v[108:111], v[220:223], v[200:203], v[108:111]\n\t" \
  "v_mfma_f32_16x16x32_bf16 v[124:127], v[220:223], v[204:207], v[124:127]\n\t" \
  "s_waitcnt lgkmcnt(0)\n\t" \
  "s_mov_b32 s52, 5\n\t" \
  ".p2align 3\n\t" \
  "1:\n\t" \
  "v_mfma_f32_16x16x32_bf16 v[0:3], v[160:163], v[128:131], v[0:3]\n\t" \
  "s_add_u32 s53, s53, 0x8000\n\t" \
  "s_cmp_eq_u32 s53, 0x18000\n\t" \
  "s_cselect_b32 s53, 0, s53\n\t" \
  "ds_read_b64_tr_b16 v[208:209], v240 offset:8192\n\t" \
  "v_mfma_f32_16x16x32_bf16 v[16:19], v[160:163], v[132:135], v[16:19]\n\t" \
  "s_add_u32 m0, s51, s58\n\t" \
  "ds_read_b128 v[176:179], v225 offset:0\n\t" \
  "v_mfma_f32_16x16x32_bf16 v[32:35], v[160:163], v[136:139], v[32:35]\n\t" \
  "buffer_load_dwordx4 v236, s[44:47], s49 offen offset:0 lds\n\t" \
  "ds_read_b64_tr_b16 v[210:211], v240 offset:9216\n\t" \
  "v_mfma_f32_16x16x32_bf16 v[48:51], v[160:163], v[140:143], v[48:51]\n\t" \
  "ds_read_b128 v[180:183], v225 offset:2048\n\t" \
  "v_mfma_f32_16x16x32_bf16 v[64:67], v[160:163], v[144:147], v[64:67]\n\t" \
  "ds_read_b64_tr_b16 v[212:213], v241 offset:8192\n\t" \
  "v_mfma_f32_16x16x32_bf16 v[80:83], v[160:163], v[148:151], v[80:83]\n\t" \
  "ds_read_b128 v[184:187], v225 offset:4096\n\t" \
  "v_mfma_f32_16x16x32_bf16 v[96:99], v[160:163], v[152:155], v[96:99]\n\t" \
  "ds_read_b64_tr_b16 v[214:215], v241 offset:9216\n\t" \
  "v_mfma_f32_16x16x32_bf16 v[112:115], v[160:163], v[156:159], v[112:115]\n\t" \
  "ds_read_b128 v[188:191], v225 offset:6144\n\t" \
  "v_mfma_f32_16x16x32_bf16 v[4:7], v[164:167], v[128:131], v[4:7]\n\t" \
  "ds_read_b64_tr_b16 v[216:217], v240 offset:24576\n\t" \
  "v_mfma_f32_16x16x32_bf16 v[20:23], v[164:167], v[132:135], v[20:23]\n\t" \
  "buffer_load_dwordx4 v237, s[44:47], s49 offen offset:1024 lds\n\t" \
  "ds_read_b128 v[192:195], v225 offset:8192\n\t" \
  "v_mfma_f32_16x16x32_bf16 v[36:39], v[164:167], v[136:139], v[36:39]\n\t" \
  "ds_read_b64_tr_b16 v[218:219], v240 offset:25600\n\t" \
  "v_mfma_f32_16x16x32_bf16 v[52:55], v[164:167], v[140:143], v[52:55]\n\t" \
  "ds_read_b128 v[196:199], v225 offset:10240\n\t" \
  "v_mfma_f32_16x16x32_bf16 v[68:71], v[164:167], v[144:147], v[68:71]\n\t" \
  "ds_read_b64_tr_b16 v[220:221], v241 offset:24576\n\t" \
  "v_mfma_f32_16x16x32_bf16 v[84:87], v[164:167], v[148:151], v[84:87]\n\t" \
  "ds_read_b128 v[200:203], v225 offset:12288\n\t" \
  "v_mfma_f32_16x16x32_bf16 v[100:103], v[164:167], v[152:155], v[100:103]\n\t" \
  "ds_read_b64_tr_b16 v[222:223], v241 offset:25600\n\t" \
  "v_mfma_f32_16x16x32_bf16 v[116:119], v[164:167], v[156:159], v[116:119]\n\t" \
  "ds_read_b128 v[204:207], v225 offset:14336\n\t" \
  "v_mfma_f32_16x16x32_bf16 v[8:11], v[168:171], v[128:131], v[8:11]\n\t" \
  "buffer_load_dwordx4 v238, s[44:47], s49 offen offset:2048 lds\n\t" \
  "v_mfma_f32_16x16x32_bf16 v[24:27], v[168:171], v[132:135], v[24:27]\n\t" \
  "v_mfma_f32_16x16x32_bf16 v[40:43], v[168:171], v[136:139], v[40:43]\n\t" \
  "v_mfma_f32_16x16x32_bf16 v[56:59], v[168:171], v[140:143], v[56:59]\n\t" \
  "v_mfma_f32_16x16x32_bf16 v[72:75], v[168:171], v[144:147], v[72:75]\n\t" \
  "v_mfma_f32_16x16x32_bf16 v[88:91], v[168:171], v[148:151], v[88:91]\n\t" \
  "v_mfma_f32_16x16x32_bf16 v[104:107], v[168:171], v[152:155], v[104:107]\n\t" \
  "v_mfma_f32_16x16x32_bf16 v[120:123], v[168:171], v[156:159], v[120:123]\n\t" \
  "buffer_load_dwordx4 v239, s[44:47], s49 offen offset:3072 lds\n\t" \
  "v_mfma_f32_16x16x32_bf16 v[12:15], v[172:175], v[128:131], v[12:15]\n\t" \
  "s_add_u32 s49, s49, 458752\n\t" \
  "s_add_u32 s58, s58, 0x8000\n\t" \
  "s_cmp_eq_u32 s58, 0x18000\n\t" \
  "s_cselect_b32 s58, 0, s58\n\t" \
  "v_xor_b32 v224, 0x8000, v224\n\t" \
  "v_mfma_f32_16x16x32_bf16 v[28:31], v[172:175], v[132:135], v[28:31]\n\t" \
  "v_add_u32 v226, s53, v228\n\t" \
  "v_mfma_f32_16x16x32_bf16 v[44:47], v[172:175], v[136:139], v[44:47]\n\t" \
  "v_add_u32 v227, s53, v229\n\t" \
  "v_mfma_f32_16x16x32_bf16 v[60:63], v[172:175], v[140:143], v[60:63]\n\t" \
  "v_mfma_f32_16x16x32_bf16 v[76:79], v[172:175], v[144:147], v[76:79]\n\t" \
  "v_mfma_f32_16x16x32_bf16 v[92:95], v[172:175], v[148:151], v[92:95]\n\t" \
  "v_mfma_f32_16x16x32_bf16 v[108:111], v[172:175], v[152:155], v[108:111]\n\t" \
  "v_mfma_f32_16x16x32_bf16 v[124:127], v[172:175], v[156:159], v[124:127]\n\t" \
  "s_waitcnt vmcnt(4) lgkmcnt(0)\n\t" \
  "s_barrier\n\t" \
  "v_mfma_f32_16x16x32_bf16 v[0:3], v[208:211], v[176:179], v[0:3]\n\t" \
  "ds_read_b64_tr_b16 v[160:161], v226 offset:0\n\t" \
  "v_mfma_f32_16x16x32_bf16 v[16:19], v[208:211], v[180:183], v[16:19]\n\t" \
  "s_add_u32 m0, s50, s59\n\t" \
  "ds_read_b128 v[128:131], v224 offset:0\n\t" \
  "v_mfma_f32_16x16x32_bf16 v[32:35], v[208:211], v[184:187], v[32:35]\n\t" \
  "buffer_load_dwordx4 v232, s[40:43], s48 offen offset:0 lds\n\t" \
  "ds_read_b64_tr_b16 v[162:163], v226 offset:1024\n\t" \
  "v_mfma_f32_16x16x32_bf16 v[48:51], v[208:211], v[188:191], v[48:51]\n\t" \
  "ds_read_b128 v[132:135], v224 offset:2048\n\t" \
  "v_mfma_f32_16x16x32_bf16 v[64:67], v[208:211], v[192:195], v[64:67]\n\t" \
  "ds_read_b64_tr_b16 v[164:165], v227 offset:0\n\t" \
  "v_mfma_f32_16x16x32_bf16 v[80:83], v[208:211], v[196:199], v[80:83]\n\t" \
  "ds_read_b128 v[136:139], v224 offset:4096\n\t" \
  "v_mfma_f32_16x16x32_bf16 v[96:99], v[208:211], v[200:203], v[96:99]\n\t" \
  "ds_read_b64_tr_b16 v[166:167], v227 offset:1024\n\t" \
  "v_mfma_f32_16x16x32_bf16 v[112:115], v[208:211], v[204:207], v[112:115]\n\t" \
  "ds_read_b128 v[140:143], v224 offset:6144\n\t" \
  "v_mfma_f32_16x16x32_bf16 v[4:7], v[212:215], v[176:179], v[4:7]\n\t" \
  "ds_read_b64_tr_b16 v[168:169], v226 offset:16384\n\t" \
  "v_mfma_f32_16x16x32_bf16 v[20:23], v[212:215], v[180:183], v[20:23]\n\t" \
  "buffer_load_dwordx4 v233, s[40:43], s48 offen offset:1024 lds\n\t" \
  "ds_read_b128 v[144:147], v224 offset:8192\n\t" \
  "v_mfma_f32_16x16x32_bf16 v[36:39], v[212:215], v[184:187], v[36:39]\n\t" \
  "ds_read_b64_tr_b16 v[170:171], v226 offset:17408\n\t" \
  "v_mfma_f32_16x16x32_bf16 v[52:55], v[212:215], v[188:191], v[52:55]\n\t" \
  "ds_read_b128 v[148:151], v224 offset:10240\n\t" \
  "v_mfma_f32_16x16x32_bf16 v[68:71], v[212:215], v[192:195], v[68:71]\n\t" \
  "ds_read_b64_tr_b16 v[172:173], v227 offset:16384\n\t" \
  "v_mfma_f32_16x16x32_bf16 v[84:87], v[212:215], v[196:199], v[84:87]\n\t" \
  "ds_read_b128 v[152:155], v224 offset:12288\n\t" \
  "v_mfma_f32_16x16x32_bf16 v[100:103], v[212:215], v[200:203], v[100:103]\n\t" \
  "ds_read_b64_tr_b16 v[174:175], v227 offset:17408\n\t" \
  "v_mfma_f32_16x16x32_bf16 v[116:119], v[212:215], v[204:207], v[116:119]\n\t" \
  "ds_read_b128 v[156:159], v224 offset:14336\n\t" \
  "v_mfma_f32_16x16x32_bf16 v[8:11], v[216:219], v[176:179], v[8:11]\n\t" \
  "buffer_load_dwordx4 v234, s[40:43], s48 offen offset:2048 lds\n\t" \
  "v_mfma_f32_16x16x32_bf16 v[24:27], v[216:219], v[180:183], v[24:27]\n\t" \
  "v_mfma_f32_16x16x32_bf16 v[40:43], v[216:219], v[184:187], v[40:43]\n\t" \
  "v_mfma_f32_16x16x32_bf16 v[56:59], v[216:219], v[188:191], v[56:59]\n\t" \
  "v_mfma_f32_16x16x32_bf16 v[72:75], v[216:219], v[192:195], v[72:75]\n\t" \
  "v_mfma_f32_16x16x32_bf16 v[88:91], v[216:219], v[196:199], v[88:91]\n\t" \
  "v_mfma_f32_16x16x32_bf16 v[104:107], v[216:219], v[200:203], v[104:107]\n\t" \
  "v_mfma_f32_16x16x32_bf16 v[120:123], v[216:219], v[204:207], v[120:123]\n\t" \
  "buffer_load_dwordx4 v235, s[40:43], s48 offen offset:3072 lds\n\t" \
  "v_mfma_f32_16x16x32_bf16 v[12:15], v[220:223], v[176:179], v[12:15]\n\t" \
  "s_add_u32 s48, s48, 128\n\t" \
  "s_xor_b32 s59, s59, 0x8000\n\t" \
  "v_xor_b32 v225, 0x8000, v225\n\t" \
  "v_mfma_f32_16x16x32_bf16 v[28:31], v[220:223], v[180:183], v[28:31]\n\t" \
  "v_add_u32 v240, s53, v228\n\t" \
  "v_mfma_f32_16x16x32_bf16 v[44:47], v[220:223], v[184:187], v[44:47]\n\t" \
  "v_add_u32 v241, s53, v229\n\t" \
  "v_mfma_f32_16x16x32_bf16 v[60:63], v[220:223], v[188:191], v[60:63]\n\t" \
  "v_mfma_f32_16x16x32_bf16 v[76:79], v[220:223], v[192:195], v[76:79]\n\t" \
  "v_mfma_f32_16x16x32_bf16 v[92:95], v[220:223], v[196:199], v[92:95]\n\t" \
  "v_mfma_f32_16x16x32_bf16 v[108:111], v[220:223], v[200:203], v[108:111]\n\t" \
  "v_mfma_f32_16x16x32_bf16 v[124:127], v[220:223], v[204:207], v[124:127]\n\t" \
  "s_waitcnt lgkmcnt(0)\n\t" \
  "v_mfma_f32_16x16x32_bf16 v[0:3], v[160:163], v[128:131], v[0:3]\n\t" \
  "s_add_u32 s53, s53, 0x8000\n\t" \
  "s_cmp_eq_u32 s53, 0x18000\n\t" \
  "s_cselect_b32 s53, 0, s53\n\t" \
  "ds_read_b64_tr_b16 v[208:209], v240 offset:8192\n\t" \
  "v_mfma_f32_16x16x32_bf16 v[16:19], v[160:163], v[132:135], v[16:19]\n\t" \
  "s_add_u32 m0, s51, s58\n\t" \
  "ds_read_b128 v[176:179], v225 offset:0\n\t" \
  "v_mfma_f32_16x16x32_bf16 v[32:35], v[160:163], v[136:139], v[32:35]\n\t" \
  "buffer_load_dwordx4 v236, s[44:47], s49 offen offset:0 lds\n\t" \
  "ds_read_b64_tr_b16 v[210:211], v240 offset:9216\n\t" \
  "v_mfma_f32_16x16x32_bf16 v[48:51], v[160:163], v[140:143], v[48:51]\n\t" \
  "ds_read_b128 v[180:183], v225 offset:2048\n\t" \
  "v_mfma_f32_16x16x32_bf16 v[64:67], v[160:163], v[144:147], v[64:67]\n\t" \
  "ds_read_b64_tr_b16 v[212:213], v241 offset:8192\n\t" \
  "v_mfma_f32_16x16x32_bf16 v[80:83], v[160:163], v[148:151], v[80:83]\n\t" \
  "ds_read_b128 v[184:187], v225 offset:4096\n\t" \
  "v_mfma_f32_16x16x32_bf16 v[96:99], v[160:163], v[152:155], v[96:99]\n\t" \
  "ds_read_b64_tr_b16 v[214:215], v241 offset:9216\n\t" \
  "v_mfma_f32_16x16x32_bf16 v[112:115], v[160:163], v[156:159], v[112:115]\n\t" \
  "ds_read_b128 v[188:191], v225 offset:6144\n\t" \
  "v_mfma_f32_16x16x32_bf16 v[4:7], v[164:167], v[128:131], v[4:7]\n\t" \
  "ds_read_b64_tr_b16 v[216:217], v240 offset:24576\n\t" \
  "v_mfma_f32_16x16x32_bf16 v[20:23], v[164:167], v[132:135], v[20:23]\n\t" \
  "buffer_load_dwordx4 v237, s[44:47], s49 offen offset:1024 lds\n\t" \
  "ds_read_b128 v[192:195], v225 offset:8192\n\t" \
  "v_mfma_f32_16x16x32_bf16 v[36:39], v[164:167], v[136:139], v[36:39]\n\t" \
  "ds_read_b64_tr_b16 v[218:219], v240 offset:25600\n\t" \
  "v_mfma_f32_16x16x32_bf16 v[52:55], v[164:167], v[140:143], v[52:55]\n\t" \
  "ds_read_b128 v[196:199], v225 offset:10240\n\t" \
  "v_mfma_f32_16x16x32_bf16 v[68:71], v[164:167], v[144:147], v[68:71]\n\t" \
  "ds_read_b64_tr_b16 v[220:221], v241 offset:24576\n\t" \
  "v_mfma_f32_16x16x32_bf16 v[84:87], v[164:167], v[148:151], v[84:87]\n\t" \
  "ds_read_b128 v[200:203], v225 offset:12288\n\t" \
  "v_mfma_f32_16x16x32_bf16 v[100:103], v[164:167], v[152:155], v[100:103]\n\t" \
  "ds_read_b64_tr_b16 v[222:223], v241 offset:25600\n\t" \
  "v_mfma_f32_16x16x32_bf16 v[116:119], v[164:167], v[156:159], v[116:119]\n\t" \
  "ds_read_b128 v[204:207], v225 offset:14336\n\t" \
  "v_mfma_f32_16x16x32_bf16 v[8:11], v[168:171], v[128:131], v[8:11]\n\t" \
  "buffer_load_dwordx4 v238, s[44:47], s49 offen offset:2048 lds\n\t" \
  "v_mfma_f32_16x16x32_bf16 v[24:27], v[168:171], v[132:135], v[24:27]\n\t" \
  "v_mfma_f32_16x16x32_bf16 v[40:43], v[168:171], v[136:139], v[40:43]\n\t" \
  "v_mfma_f32_16x16x32_bf16 v[56:59], v[168:171], v[140:143], v[56:59]\n\t" \
  "v_mfma_f32_16x16x32_bf16 v[72:75], v[168:171], v[144:147], v[72:75]\n\t" \
  "v_mfma_f32_16x16x32_bf16 v[88:91], v[168:171], v[148:151], v[88:91]\n\t" \
  "v_mfma_f32_16x16x32_bf16 v[104:107], v[168:171], v[152:155], v[104:107]\n\t" \
  "v_mfma_f32_16x16x32_bf16 v[120:123], v[168:171], v[156:159], v[120:123]\n\t" \
  "buffer_load_dwordx4 v239, s[44:47], s49 offen offset:3072 lds\n\t" \
  "v_mfma_f32_16x16x32_bf16 v[12:15], v[172:175], v[128:131], v[12:15]\n\t" \
  "s_add_u32 s49, s49, 458752\n\t" \
  "s_add_u32 s58, s58, 0x8000\n\t" \
  "s_cmp_eq_u32 s58, 0x18000\n\t" \
  "s_cselect_b32 s58, 0, s58\n\t" \
  "v_xor_b32 v224, 0x8000, v224\n\t" \
  "v_mfma_f32_16x16x32_bf16 v[28:31], v[172:175], v[132:135], v[28:31]\n\t" \
  "v_add_u32 v226, s53, v228\n\t" \
  "v_mfma_f32_16x16x32_bf16 v[44:47], v[172:175], v[136:139], v[44:47]\n\t" \
  "v_add_u32 v227, s53, v229\n\t" \
  "v_mfma_f32_16x16x32_bf16 v[60:63], v[172:175], v[140:143], v[60:63]\n\t" \
  "v_mfma_f32_16x16x32_bf16 v[76:79], v[172:175], v[144:147], v[76:79]\n\t" \
  "v_mfma_f32_16x16x32_bf16 v[92:95], v[172:175], v[148:151], v[92:95]\n\t" \
  "v_mfma_f32_16x16x32_bf16 v[108:111], v[172:175], v[152:155], v[108:111]\n\t" \
  "v_mfma_f32_16x16x32_bf16 v[124:127], v[172:175], v[156:159], v[124:127]\n\t" \
  "s_waitcnt vmcnt(4) lgkmcnt(0)\n\t" \
  "s_barrier\n\t" \
  "v_mfma_f32_16x16x32_bf16 v[0:3], v[208:211], v[176:179], v[0:3]\n\t" \
  "ds_read_b64_tr_b16 v[160:161], v226 offset:0\n\t" \
  "v_mfma_f32_16x16x32_bf16 v[16:19], v[208:211], v[180:183], v[16:19]\n\t" \
  "s_add_u32 m0, s50, s59\n\t" \
  "ds_read_b128 v[128:131], v224 offset:0\n\t" \
  "v_mfma_f32_16x16x32_bf16 v[32:35], v[208:211], v[184:187], v[32:35]\n\t" \
  "buffer_load_dwordx4 v232, s[40:43], s48 offen offset:0 lds\n\t" \
  "ds_read_b64_tr_b16 v[162:163], v226 offset:1024\n\t" \
  "v_mfma_f32_16x16x32_bf16 v[48:51], v[208:211], v[188:191], v[48:51]\n\t" \
  "ds_read_b128 v[132:135], v224 offset:2048\n\t" \
  "v_mfma_f32_16x16x32_bf16 v[64:67], v[208:211], v[192:195], v[64:67]\n\t" \
  "ds_read_b64_tr_b16 v[164:165], v227 offset:0\n\t" \
  "v_mfma_f32_16x16x32_bf16 v[80:83], v[208:211], v[196:199], v[80:83]\n\t" \
  "ds_read_b128 v[136:139], v224 offset:4096\n\t" \
  "v_mfma_f32_16x16x32_bf16 v[96:99], v[208:211], v[200:203], v[96:99]\n\t" \
  "ds_read_b64_tr_b16 v[166:167], v227 offset:1024\n\t" \
  "v_mfma_f32_16x16x32_bf16 v[112:115], v[208:211], v[204:207], v[112:115]\n\t" \
  "ds_read_b128 v[140:143], v224 offset:6144\n\t" \
  "v_mfma_f32_16x16x32_bf16 v[4:7], v[212:215], v[176:179], v[4:7]\n\t" \
  "ds_read_b64_tr_b16 v[168:169], v226 offset:16384\n\t" \
  "v_mfma_f32_16x16x32_bf16 v[20:23], v[212:215], v[180:183], v[20:23]\n\t" \
  "buffer_load_dwordx4 v233, s[40:43], s48 offen offset:1024 lds\n\t" \
  "ds_read_b128 v[144:147], v224 offset:8192\n\t" \
  "v_mfma_f32_16x16x32_bf16 v[36:39], v[212:215], v[184:187], v[36:39]\n\t" \
  "ds_read_b64_tr_b16 v[170:171], v226 offset:17408\n\t" \
  "v_mfma_f32_16x16x32_bf16 v[52:55], v[212:215], v[188:191], v[52:55]\n\t" \
  "ds_read_b128 v[148:151], v224 offset:10240\n\t" \
  "v_mfma_f32_16x16x32_bf16 v[68:71], v[212:215], v[192:195], v[68:71]\n\t" \
  "ds_read_b64_tr_b16 v[172:173], v227 offset:16384\n\t" \
  "v_mfma_f32_16x16x32_bf16 v[84:87], v[212:215], v[196:199], v[84:87]\n\t" \
  "ds_read_b128 v[152:155], v224 offset:12288\n\t" \
  "v_mfma_f32_16x16x32_bf16 v[100:103], v[212:215], v[200:203], v[100:103]\n\t" \
  "ds_read_b64_tr_b16 v[174:175], v227 offset:17408\n\t" \
  "v_mfma_f32_16x16x32_bf16 v[116:119], v[212:215], v[204:207], v[116:119]\n\t" \
  "ds_read_b128 v[156:159], v224 offset:14336\n\t" \
  "v_mfma_f32_16x16x32_bf16 v[8:11], v[216:219], v[176:179], v[8:11]\n\t" \
  "buffer_load_dwordx4 v234, s[40:43], s48 offen offset:2048 lds\n\t" \
  "v_mfma_f32_16x16x32_bf16 v[24:27], v[216:219], v[180:183], v[24:27]\n\t" \
  "v_mfma_f32_16x16x32_bf16 v[40:43], v[216:219], v[184:187], v[40:43]\n\t" \
  "v_mfma_f32_16x16x32_bf16 v[56:59], v[216:219], v[188:191], v[56:59]\n\t" \
  "v_mfma_f32_16x16x32_bf16 v[72:75], v[216:219], v[192:195], v[72:75]\n\t" \
  "v_mfma_f32_16x16x32_bf16 v[88:91], v[216:219], v[196:199], v[88:91]\n\t" \
  "v_mfma_f32_16x16x32_bf16 v[104:107], v[216:219], v[200:203], v[104:107]\n\t" \
  "v_mfma_f32_16x16x32_bf16 v[120:123], v[216:219], v[204:207], v[120:123]\n\t" \
  "buffer_load_dwordx4 v235, s[40:43], s48 offen offset:3072 lds\n\t" \
  "v_mfma_f32_16x16x32_bf16 v[12:15], v[220:223], v[176:179], v[12:15]\n\t" \
  "s_add_u32 s48, s48, 128\n\t" \
  "s_xor_b32 s59, s59, 0x8000\n\t" \
  "v_xor_b32 v225, 0x8000, v225\n\t" \
  "v_mfma_f32_16x16x32_bf16 v[28:31], v[220:223], v[180:183], v[28:31]\n\t" \
  "v_add_u32 v240, s53, v228\n\t" \
  "v_mfma_f32_16x16x32_bf16 v[44:47], v[220:223], v[184:187], v[44:47]\n\t" \
  "v_add_u32 v241, s53, v229\n\t" \
  "v_mfma_f32_16x16x32_bf16 v[60:63], v[220:223], v[188:191], v[60:63]\n\t" \
  "s_sub_u32 s52, s52, 1\n\t" \
  "v_mfma_f32_16x16x32_bf16 v[76:79], v[220:223], v[192:195], v[76:79]\n\t" \
  "s_cmp_lg_u32 s52, 0\n\t" \
  "v_mfma_f32_16x16x32_bf16 v[92:95], v[220:223], v[196:199], v[92:95]\n\t" \
  "v_mfma_f32_16x16x32_bf16 v[108:111], v[220:223], v[200:203], v[108:111]\n\t" \
  "v_mfma_f32_16x16x32_bf16 v[124:127], v[220:223], v[204:207], v[124:127]\n\t" \
  "s_waitcnt lgkmcnt(0)\n\t" \
  "s_cbranch_scc1 1b\n\t" \
  "s_branch 4f\n\t" \
  "3:\n\t" \
  "v_mfma_f32_16x16x32_bf16 v[0:3], v[160:163], v[128:131], 0\n\t" \
  "global_load_dword v254, v[250:251], off\n\t" \
  "v_lshl_add_u64 v[250:251], v[252:253], 0, v[250:251]\n\t" \
  "global_load_dword v254, v[246:247], off sc1\n\t" \
  "s_add_u32 s53, s53, 0x8000\n\t" \
  "s_cmp_eq_u32 s53, 0x18000\n\t" \
  "s_cselect_b32 s53, 0, s53\n\t" \
  "ds_read_b64_tr_b16 v[208:209], v240 offset:8192\n\t" \
  "v_mfma_f32_16x16x32_bf16 v[16:19], v[160:163], v[132:135], 0\n\t" \
  "s_add_u32 m0, s51, s58\n\t" \
  "ds_read_b128 v[176:179], v225 offset:0\n\t" \
  "v_mfma_f32_16x16x32_bf16 v[32:35], v[160:163], v[136:139], 0\n\t" \
  "buffer_load_dwordx4 v236, s[44:47], s49 offen offset:0 lds\n\t" \
  "ds_read_b64_tr_b16 v[210:211], v240 offset:9216\n\t" \
  "v_mfma_f32_16x16x32_bf16 v[48:51], v[160:163], v[140:143], 0\n\t" \
  "ds_read_b128 v[180:183], v225 offset:2048\n\t" \
  "v_mfma_f32_16x16x32_bf16 v[64:67], v[160:163], v[144:147], 0\n\t" \
  "ds_read_b64_tr_b16 v[212:213], v241 offset:8192\n\t" \
  "v_mfma_f32_16x16x32_bf16 v[80:83], v[160:163], v[148:151], 0\n\t" \
  "ds_read_b128 v[184:187], v225 offset:4096\n\t" \
  "v_mfma_f32_16x16x32_bf16 v[96:99], v[160:163], v[152:155], 0\n\t" \
  "ds_read_b64_tr_b16 v[214:215], v241 offset:9216\n\t" \
  "v_mfma_f32_16x16x32_bf16 v[112:115], v[160:163], v[156:159], 0\n\t" \
  "ds_read_b128 v[188:191], v225 offset:6144\n\t" \
  "v_mfma_f32_16x16x32_bf16 v[4:7], v[164:167], v[128:131], 0\n\t" \
  "ds_read_b64_tr_b16 v[216:217], v240 offset:24576\n\t" \
  "v_mfma_f32_16x16x32_bf16 v[20:23], v[164:167], v[132:135], 0\n\t" \
  "buffer_load_dwordx4 v237, s[44:47], s49 offen offset:1024 lds\n\t" \
  "ds_read_b128 v[192:195], v225 offset:8192\n\t" \
  "v_mfma_f32_16x16x32_bf16 v[36:39], v[164:167], v[136:139], 0\n\t" \
  "ds_read_b64_tr_b16 v[218:219], v240 offset:25600\n\t" \
  "v_mfma_f32_16x16x32_bf16 v[52:55], v[164:167], v[140:143], 0\n\t" \
  "ds_read_b128 v[196:199], v225 offset:10240\n\t" \
  "v_mfma_f32_16x16x32_bf16 v[68:71], v[164:167], v[144:147], 0\n\t" \
  "ds_read_b64_tr_b16 v[220:221], v241 offset:24576\n\t" \
  "v_mfma_f32_16x16x32_bf16 v[84:87], v[164:167], v[148:151], 0\n\t" \
  "ds_read_b128 v[200:203], v225 offset:12288\n\t" \
  "v_mfma_f32_16x16x32_bf16 v[100:103], v[164:167], v[152:155], 0\n\t" \
  "ds_read_b64_tr_b16 v[222:223], v241 offset:25600\n\t" \
  "v_mfma_f32_16x16x32_bf16 v[116:119], v[164:167], v[156:159], 0\n\t" \
  "ds_read_b128 v[204:207], v225 offset:14336\n\t" \
  "v_mfma_f32_16x16x32_bf16 v[8:11], v[168:171], v[128:131], 0\n\t" \
  "buffer_load_dwordx4 v238, s[44:47], s49 offen offset:2048 lds\n\t" \
  "v_mfma_f32_16x16x32_bf16 v[24:27], v[168:171], v[132:135], 0\n\t" \
  "v_mfma_f32_16x16x32_bf16 v[40:43], v[168:171], v[136:139], 0\n\t" \
  "v_mfma_f32_16x16x32_bf16 v[56:59], v[168:171], v[140:143], 0\n\t" \
  "v_mfma_f32_16x16x32_bf16 v[72:75], v[168:171], v[144:147], 0\n\t" \
  "v_mfma_f32_16x16x32_bf16 v[88:91], v[168:171], v[148:151], 0\n\t" \
  "v_mfma_f32_16x16x32_bf16 v[104:107], v[168:171], v[152:155], 0\n\t" \
  "v_mfma_f32_16x16x32_bf16 v[120:123], v[168:171], v[156:159], 0\n\t" \
  "buffer_load_dwordx4 v239, s[44:47], s49 offen offset:3072 lds\n\t" \
  "v_mfma_f32_16x16x32_bf16 v[12:15], v[172:175], v[128:131], 0\n\t" \
  "s_add_u32 s49, s49, 458752\n\t" \
  "s_add_u32 s58, s58, 0x8000\n\t" \
  "s_cmp_eq_u32 s58, 0x18000\n\t" \
  "s_cselect_b32 s58, 0, s58\n\t" \
  "v_xor_b32 v224, 0x8000, v224\n\t" \
  "v_mfma_f32_16x16x32_bf16 v[28:31], v[172:175], v[132:135], 0\n\t" \
  "v_add_u32 v226, s53, v228\n\t" \
  "v_mfma_f32_16x16x32_bf16 v[44:47], v[172:175], v[136:139], 0\n\t" \
  "v_add_u32 v227, s53, v229\n\t" \
  "v_mfma_f32_16x16x32_bf16 v[60:63], v[172:175], v[140:143], 0\n\t" \
  "v_mfma_f32_16x16x32_bf16 v[76:79], v[172:175], v[144:147], 0\n\t" \
  "v_mfma_f32_16x16x32_bf16 v[92:95], v[172:175], v[148:151], 0\n\t" \
  "v_mfma_f32_16x16x32_bf16 v[108:111], v[172:175], v[152:155], 0\n\t" \
  "v_mfma_f32_16x16x32_bf16 v[124:127], v[172:175], v[156:159], 0\n\t" \
  "s_waitcnt vmcnt(6) lgkmcnt(0)\n\t" \
  "s_barrier\n\t" \
  "v_mfma_f32_16x16x32_bf16 v[0:3], v[208:211], v[176:179], v[0:3]\n\t" \
  "v_add_u32 v231, 1, v248\n\t" \
  "v_cmp_eq_u32 s[54:55], v231, v249\n\t" \
  "s_nop 1\n\t" \
  "v_addc_co_u32 v231, s[54:55], 0, v231, s[54:55]\n\t" \
  "buffer_store_dword v231, v230, s[72:75], 0 offen sc1\n\t" \
  "ds_read_b64_tr_b16 v[160:161], v226 offset:0\n\t" \
  "v_mfma_f32_16x16x32_bf16 v[16:19], v[208:211], v[180:183], v[16:19]\n\t" \
  "s_add_u32 m0, s50, s59\n\t" \
  "ds_read_b128 v[128:131], v224 offset:0\n\t" \
  "v_mfma_f32_16x16x32_bf16 v[32:35], v[208:211], v[184:187], v[32:35]\n\t" \
  "buffer_load_dwordx4 v232, s[40:43], s48 offen offset:0 lds\n\t" \
  "ds_read_b64_tr_b16 v[162:163], v226 offset:1024\n\t" \
  "v_mfma_f32_16x16x32_bf16 v[48:51], v[208:211], v[188:191], v[48:51]\n\t" \
  "ds_read_b128 v[132:135], v224 offset:2048\n\t" \
  "v_mfma_f32_16x16x32_bf16 v[64:67], v[208:211], v[192:195], v[64:67]\n\t" \
  "ds_read_b64_tr_b16 v[164:165], v227 offset:0\n\t" \
  "v_mfma_f32_16x16x32_bf16 v[80:83], v[208:211], v[196:199], v[80:83]\n\t" \
  "ds_read_b128 v[136:139], v224 offset:4096\n\t" \
  "v_mfma_f32_16x16x32_bf16 v[96:99], v[208:211], v[200:203], v[96:99]\n\t" \
  "ds_read_b64_tr_b16 v[166:167], v227 offset:1024\n\t" \
  "v_mfma_f32_16x16x32_bf16 v[112:115], v[208:211], v[204:207], v[112:115]\n\t" \
  "ds_read_b128 v[140:143], v224 offset:6144\n\t" \
  "v_mfma_f32_16x16x32_bf16 v[4:7], v[212:215], v[176:179], v[4:7]\n\t" \
  "ds_read_b64_tr_b16 v[168:169], v226 offset:16384\n\t" \
  "v_mfma_f32_16x16x32_bf16 v[20:23], v[212:215], v[180:183], v[20:23]\n\t" \
  "buffer_load_dwordx4 v233, s[40:43], s48 offen offset:1024 lds\n\t" \
  "ds_read_b128 v[144:147], v224 offset:8192\n\t" \
  "v_mfma_f32_16x16x32_bf16 v[36:39], v[212:215], v[184:187], v[36:39]\n\t" \
  "ds_read_b64_tr_b16 v[170:171], v226 offset:17408\n\t" \
  "v_mfma_f32_16x16x32_bf16 v[52:55], v[212:215], v[188:191], v[52:55]\n\t" \
  "ds_read_b128 v[148:151], v224 offset:10240\n\t" \
  "v_mfma_f32_16x16x32_bf16 v[68:71], v[212:215], v[192:195], v[68:71]\n\t" \
  "ds_read_b64_tr_b16 v[172:173], v227 offset:16384\n\t" \
  "v_mfma_f32_16x16x32_bf16 v[84:87], v[212:215], v[196:199], v[84:87]\n\t" \
  "ds_read_b128 v[152:155], v224 offset:12288\n\t" \
  "v_mfma_f32_16x16x32_bf16 v[100:103], v[212:215], v[200:203], v[100:103]\n\t" \
  "ds_read_b64_tr_b16 v[174:175], v227 offset:17408\n\t" \
  "v_mfma_f32_16x16x32_bf16 v[116:119], v[212:215], v[204:207], v[116:119]\n\t" \
  "ds_read_b128 v[156:159], v224 offset:14336\n\t" \
  "v_mfma_f32_16x16x32_bf16 v[8:11], v[216:219], v[176:179], v[8:11]\n\t" \
  "buffer_load_dwordx4 v234, s[40:43], s48 offen offset:2048 lds\n\t" \
  "v_mfma_f32_16x16x32_bf16 v[24:27], v[216:219], v[180:183], v[24:27]\n\t" \
  "v_mfma_f32_16x16x32_bf16 v[40:43], v[216:219], v[184:187], v[40:43]\n\t" \
  "v_mfma_f32_16x16x32_bf16 v[56:59], v[216:219], v[188:191], v[56:59]\n\t" \
  "v_mfma_f32_16x16x32_bf16 v[72:75], v[216:219], v[192:195], v[72:75]\n\t" \
  "v_mfma_f32_16x16x32_bf16 v[88:91], v[216:219], v[196:199], v[88:91]\n\t" \
  "v_mfma_f32_16x16x32_bf16 v[104:107], v[216:219], v[200:203], v[104:107]\n\t" \
  "v_mfma_f32_16x16x32_bf16 v[120:123], v[216:219], v[204:207], v[120:123]\n\t" \
  "buffer_load_dwordx4 v235, s[40:43], s48 offen offset:3072 lds\n\t" \
  "v_mfma_f32_16x16x32_bf16 v[12:15], v[220:223], v[176:179], v[12:15]\n\t" \
  "s_add_u32 s48, s48, 128\n\t" \
  "s_xor_b32 s59, s59, 0x8000\n\t" \
  "v_xor_b32 v225, 0x8000, v225\n\t" \
  "v_mfma_f32_16x16x32_bf16 v[28:31], v[220:223], v[180:183], v[28:31]\n\t" \
  "v_add_u32 v240, s53, v228\n\t" \
  "v_mfma_f32_16x16x32_bf16 v[44:47], v[220:223], v[184:187], v[44:47]\n\t" \
  "v_add_u32 v241, s53, v229\n\t" \
  "v_mfma_f32_16x16x32_bf16 v[60:63], v[220:223], v[188:191], v[60:63]\n\t" \
  "v_mfma_f32_16x16x32_bf16 v[76:79], v[220:223], v[192:195], v[76:79]\n\t" \
  "v_mfma_f32_16x16x32_bf16 v[92:95], v[220:223], v[196:199], v[92:95]\n\t" \
  "v_mfma_f32_16x16x32_bf16 v[108:111], v[220:223], v[200:203], v[108:111]\n\t" \
  "v_mfma_f32_16x16x32_bf16 v[124:127], v[220:223], v[204:207], v[124:127]\n\t" \
  "s_waitcnt lgkmcnt(0)\n\t" \
  "v_mfma_f32_16x16x32_bf16 v[0:3], v[160:163], v[128:131], v[0:3]\n\t" \
  "global_load_dword v254, v[250:251], off\n\t" \
  "v_lshl_add_u64 v[250:251], v[252:253], 0, v[250:251]\n\t" \
  "s_add_u32 s53, s53, 0x8000\n\t" \
  "s_cmp_eq_u32 s53, 0x18000\n\t" \
  "s_cselect_b32 s53, 0, s53\n\t" \
  "ds_read_b64_tr_b16 v[208:209], v240 offset:8192\n\t" \
  "v_mfma_f32_16x16x32_bf16 v[16:19], v[160:163], v[132:135], v[16:19]\n\t" \
  "s_add_u32 m0, s51, s58\n\t" \
  "ds_read_b128 v[176:179], v225 offset:0\n\t" \
  "v_mfma_f32_16x16x32_bf16 v[32:35], v[160:163], v[136:139], v[32:35]\n\t" \
  "buffer_load_dwordx4 v236, s[44:47], s49 offen offset:0 lds\n\t" \
  "ds_read_b64_tr_b16 v[210:211], v240 offset:9216\n\t" \
  "v_mfma_f32_16x16x32_bf16 v[48:51], v[160:163], v[140:143], v[48:51]\n\t" \
  "ds_read_b128 v[180:183], v225 offset:2048\n\t" \
  "v_mfma_f32_16x16x32_bf16 v[64:67], v[160:163], v[144:147], v[64:67]\n\t" \
  "ds_read_b64_tr_b16 v[212:213], v241 offset:8192\n\t" \
  "v_mfma_f32_16x16x32_bf16 v[80:83], v[160:163], v[148:151], v[80:83]\n\t" \
  "ds_read_b128 v[184:187], v225 offset:4096\n\t" \
  "v_mfma_f32_16x16x32_bf16 v[96:99], v[160:163], v[152:155], v[96:99]\n\t" \
  "ds_read_b64_tr_b16 v[214:215], v241 offset:9216\n\t" \
  "v_mfma_f32_16x16x32_bf16 v[112:115], v[160:163], v[156:159], v[112:115]\n\t" \
  "ds_read_b128 v[188:191], v225 offset:6144\n\t" \
  "v_mfma_f32_16x16x32_bf16 v[4:7], v[164:167], v[128:131], v[4:7]\n\t" \
  "ds_read_b64_tr_b16 v[216:217], v240 offset:24576\n\t" \
  "v_mfma_f32_16x16x32_bf16 v[20:23], v[164:167], v[132:135], v[20:23]\n\t" \
  "buffer_load_dwordx4 v237, s[44:47], s49 offen offset:1024 lds\n\t" \
  "ds_read_b128 v[192:195], v225 offset:8192\n\t" \
  "v_mfma_f32_16x16x32_bf16 v[36:39], v[164:167], v[136:139], v[36:39]\n\t" \
  "ds_read_b64_tr_b16 v[218:219], v240 offset:25600\n\t" \
  "v_mfma_f32_16x16x32_bf16 v[52:55], v[164:167], v[140:143], v[52:55]\n\t" \
  "ds_read_b128 v[196:199], v225 offset:10240\n\t" \
  "v_mfma_f32_16x16x32_bf16 v[68:71], v[164:167], v[144:147], v[68:71]\n\t" \
  "ds_read_b64_tr_b16 v[220:221], v241 offset:24576\n\t" \
  "v_mfma_f32_16x16x32_bf16 v[84:87], v[164:167], v[148:151], v[84:87]\n\t" \
  "ds_read_b128 v[200:203], v225 offset:12288\n\t" \
  "v_mfma_f32_16x16x32_bf16 v[100:103], v[164:167], v[152:155], v[100:103]\n\t" \
  "ds_read_b64_tr_b16 v[222:223], v241 offset:25600\n\t" \
  "v_mfma_f32_16x16x32_bf16 v[116:119], v[164:167], v[156:159], v[116:119]\n\t" \
  "ds_read_b128 v[204:207], v225 offset:14336\n\t" \
  "v_mfma_f32_16x16x32_bf16 v[8:11], v[168:171], v[128:131], v[8:11]\n\t" \
  "buffer_load_dwordx4 v238, s[44:47], s49 offen offset:2048 lds\n\t" \
  "v_mfma_f32_16x16x32_bf16 v[24:27], v[168:171], v[132:135], v[24:27]\n\t" \
  "v_mfma_f32_16x16x32_bf16 v[40:43], v[168:171], v[136:139], v[40:43]\n\t" \
  "v_mfma_f32_16x16x32_bf16 v[56:59], v[168:171], v[140:143], v[56:59]\n\t" \
  "v_mfma_f32_16x16x32_bf16 v[72:75], v[168:171], v[144:147], v[72:75]\n\t" \
  "v_mfma_f32_16x16x32_bf16 v[88:91], v[168:171], v[148:151], v[88:91]\n\t" \
  "v_mfma_f32_16x16x32_bf16 v[104:107], v[168:171], v[152:155], v[104:107]\n\t" \
  "v_mfma_f32_16x16x32_bf16 v[120:123], v[168:171], v[156:159], v[120:123]\n\t" \
  "buffer_load_dwordx4 v239, s[44:47], s49 offen offset:3072 lds\n\t" \
  "v_mfma_f32_16x16x32_bf16 v[12:15], v[172:175], v[128:131], v[12:15]\n\t" \
  "s_add_u32 s49, s49, 458752\n\t" \
  "s_add_u32 s58, s58, 0x8000\n\t" \
  "s_cmp_eq_u32 s58, 0x18000\n\t" \
  "s_cselect_b32 s58, 0, s58\n\t" \
  "v_xor_b32 v224, 0x8000, v224\n\t" \
  "v_mfma_f32_16x16x32_bf16 v[28:31], v[172:175], v[132:135], v[28:31]\n\t" \
  "v_add_u32 v226, s53, v228\n\t" \
  "v_mfma_f32_16x16x32_bf16 v[44:47], v[172:175], v[136:139], v[44:47]\n\t" \
  "v_add_u32 v227, s53, v229\n\t" \
  "v_mfma_f32_16x16x32_bf16 v[60:63], v[172:175], v[140:143], v[60:63]\n\t" \
  "v_mfma_f32_16x16x32_bf16 v[76:79], v[172:175], v[144:147], v[76:79]\n\t" \
  "v_mfma_f32_16x16x32_bf16 v[92:95], v[172:175], v[148:151], v[92:95]\n\t" \
  "v_mfma_f32_16x16x32_bf16 v[108:111], v[172:175], v[152:155], v[108:111]\n\t" \
  "v_mfma_f32_16x16x32_bf16 v[124:127], v[172:175], v[156:159], v[124:127]\n\t" \
  "s_waitcnt vmcnt(5) lgkmcnt(0)\n\t" \
  "s_barrier\n\t" \
  "v_mfma_f32_16x16x32_bf16 v[0:3], v[208:211], v[176:179], v[0:3]\n\t" \
  "ds_read_b64_tr_b16 v[160:161], v226 offset:0\n\t" \
  "v_mfma_f32_16x16x32_bf16 v[16:19], v[208:211], v[180:183], v[16:19]\n\t" \
  "s_add_u32 m0, s50, s59\n\t" \
  "ds_read_b128 v[128:131], v224 offset:0\n\t" \
  "v_mfma_f32_16x16x32_bf16 v[32:35], v[208:211], v[184:187], v[32:35]\n\t" \
  "buffer_load_dwordx4 v232, s[40:43], s48 offen offset:0 lds\n\t" \
  "ds_read_b64_tr_b16 v[162:163], v226 offset:1024\n\t" \
  "v_mfma_f32_16x16x32_bf16 v[48:51], v[208:211], v[188:191], v[48:51]\n\t" \
  "ds_read_b128 v[132:135], v224 offset:2048\n\t" \
  "v_mfma_f32_16x16x32_bf16 v[64:67], v[208:211], v[192:195], v[64:67]\n\t" \
  "ds_read_b64_tr_b16 v[164:165], v227 offset:0\n\t" \
  "v_mfma_f32_16x16x32_bf16 v[80:83], v[208:211], v[196:199], v[80:83]\n\t" \
  "ds_read_b128 v[136:139], v224 offset:4096\n\t" \
  "v_mfma_f32_16x16x32_bf16 v[96:99], v[208:211], v[200:203], v[96:99]\n\t" \
  "ds_read_b64_tr_b16 v[166:167], v227 offset:1024\n\t" \
  "v_mfma_f32_16x16x32_bf16 v[112:115], v[208:211], v[204:207], v[112:115]\n\t" \
  "ds_read_b128 v[140:143], v224 offset:6144\n\t" \
  "v_mfma_f32_16x16x32_bf16 v[4:7], v[212:215], v[176:179], v[4:7]\n\t" \
  "ds_read_b64_tr_b16 v[168:169], v226 offset:16384\n\t" \
  "v_mfma_f32_16x16x32_bf16 v[20:23], v[212:215], v[180:183], v[20:23]\n\t" \
  "buffer_load_dwordx4 v233, s[40:43], s48 offen offset:1024 lds\n\t" \
  "ds_read_b128 v[144:147], v224 offset:8192\n\t" \
  "v_mfma_f32_16x16x32_bf16 v[36:39], v[212:215], v[184:187], v[36:39]\n\t" \
  "ds_read_b64_tr_b16 v[170:171], v226 offset:17408\n\t" \
  "v_mfma_f32_16x16x32_bf16 v[52:55], v[212:215], v[188:191], v[52:55]\n\t" \
  "ds_read_b128 v[148:151], v224 offset:10240\n\t" \
  "v_mfma_f32_16x16x32_bf16 v[68:71], v[212:215], v[192:195], v[68:71]\n\t" \
  "ds_read_b64_tr_b16 v[172:173], v227 offset:16384\n\t" \
  "v_mfma_f32_16x16x32_bf16 v[84:87], v[212:215], v[196:199], v[84:87]\n\t" \
  "ds_read_b128 v[152:155], v224 offset:12288\n\t" \
  "v_mfma_f32_16x16x32_bf16 v[100:103], v[212:215], v[200:203], v[100:103]\n\t" \
  "ds_read_b64_tr_b16 v[174:175], v227 offset:17408\n\t" \
  "v_mfma_f32_16x16x32_bf16 v[116:119], v[212:215], v[204:207], v[116:119]\n\t" \
  "ds_read_b128 v[156:159], v224 offset:14336\n\t" \
  "v_mfma_f32_16x16x32_bf16 v[8:11], v[216:219], v[176:179], v[8:11]\n\t" \
  "buffer_load_dwordx4 v234, s[40:43], s48 offen offset:2048 lds\n\t" \
  "v_mfma_f32_16x16x32_bf16 v[24:27], v[216:219], v[180:183], v[24:27]\n\t" \
  "v_mfma_f32_16x16x32_bf16 v[40:43], v[216:219], v[184:187], v[40:43]\n\t" \
  "v_mfma_f32_16x16x32_bf16 v[56:59], v[216:219], v[188:191], v[56:59]\n\t" \
  "v_mfma_f32_16x16x32_bf16 v[72:75], v[216:219], v[192:195], v[72:75]\n\t" \
  "v_mfma_f32_16x16x32_bf16 v[88:91], v[216:219], v[196:199], v[88:91]\n\t" \
  "v_mfma_f32_16x16x32_bf16 v[104:107], v[216:219], v[200:203], v[104:107]\n\t" \
  "v_mfma_f32_16x16x32_bf16 v[120:123], v[216:219], v[204:207], v[120:123]\n\t" \
  "buffer_load_dwordx4 v235, s[40:43], s48 offen offset:3072 lds\n\t" \
  "v_mfma_f32_16x16x32_bf16 v[12:15], v[220:223], v[176:179], v[12:15]\n\t" \
  "s_add_u32 s48, s48, 128\n\t" \
  "s_xor_b32 s59, s59, 0x8000\n\t" \
  "v_xor_b32 v225, 0x8000, v225\n\t" \
  "v_mfma_f32_16x16x32_bf16 v[28:31], v[220:223], v[180:183], v[28:31]\n\t" \
  "v_add_u32 v240, s53, v228\n\t" \
  "v_mfma_f32_16x16x32_bf16 v[44:47], v[220:223], v[184:187], v[44:47]\n\t" \
  "v_add_u32 v241, s53, v229\n\t" \
  "v_mfma_f32_16x16x32_bf16 v[60:63], v[220:223], v[188:191], v[60:63]\n\t" \
  "v_mfma_f32_16x16x32_bf16 v[76:79], v[220:223], v[192:195], v[76:79]\n\t" \
  "v_mfma_f32_16x16x32_bf16 v[92:95], v[220:223], v[196:199], v[92:95]\n\t" \
  "v_mfma_f32_16x16x32_bf16 v[108:111], v[220:223], v[200:203], v[108:111]\n\t" \
  "v_mfma_f32_16x16x32_bf16 v[124:127], v[220:223], v[204:207], v[124:127]\n\t" \
  "s_waitcnt lgkmcnt(0)\n\t" \
  "s_mov_b32 s52, 5\n\t" \
  ".p2align 3\n\t" \
  "1:\n\t" \
  "v_mfma_f32_16x16x32_bf16 v[0:3], v[160:163], v[128:131], v[0:3]\n\t" \
  "global_load_dword v254, v[250:251], off\n\t" \
  "v_lshl_add_u64 v[250:251], v[252:253], 0, v[250:251]\n\t" \
  "s_add_u32 s53, s53, 0x8000\n\t" \
  "s_cmp_eq_u32 s53, 0x18000\n\t" \
  "s_cselect_b32 s53, 0, s53\n\t" \
  "ds_read_b64_tr_b16 v[208:209], v240 offset:8192\n\t" \
  "v_mfma_f32_16x16x32_bf16 v[16:19], v[160:163], v[132:135], v[16:19]\n\t" \
  "s_add_u32 m0, s51, s58\n\t" \
  "ds_read_b128 v[176:179], v225 offset:0\n\t" \
  "v_mfma_f32_16x16x32_bf16 v[32:35], v[160:163], v[136:139], v[32:35]\n\t" \
  "buffer_load_dwordx4 v236, s[44:47], s49 offen offset:0 lds\n\t" \
  "ds_read_b64_tr_b16 v[210:211], v240 offset:9216\n\t" \
  "v_mfma_f32_16x16x32_bf16 v[48:51], v[160:163], v[140:143], v[48:51]\n\t" \
  "ds_read_b128 v[180:183], v225 offset:2048\n\t" \
  "v_mfma_f32_16x16x32_bf16 v[64:67], v[160:163], v[144:147], v[64:67]\n\t" \
  "ds_read_b64_tr_b16 v[212:213], v241 offset:8192\n\t" \
  "v_mfma_f32_16x16x32_bf16 v[80:83], v[160:163], v[148:151], v[80:83]\n\t" \
  "ds_read_b128 v[184:187], v225 offset:4096\n\t" \
  "v_mfma_f32_16x16x32_bf16 v[96:99], v[160:163], v[152:155], v[96:99]\n\t" \
  "ds_read_b64_tr_b16 v[214:215], v241 offset:9216\n\t" \
  "v_mfma_f32_16x16x32_bf16 v[112:115], v[160:163], v[156:159], v[112:115]\n\t" \
  "ds_read_b128 v[188:191], v225 offset:6144\n\t" \
  "v_mfma_f32_16x16x32_bf16 v[4:7], v[164:167], v[128:131], v[4:7]\n\t" \
  "ds_read_b64_tr_b16 v[216:217], v240 offset:24576\n\t" \
  "v_mfma_f32_16x16x32_bf16 v[20:23], v[164:167], v[132:135], v[20:23]\n\t" \
  "buffer_load_dwordx4 v237, s[44:47], s49 offen offset:1024 lds\n\t" \
  "ds_read_b128 v[192:195], v225 offset:8192\n\t" \
  "v_mfma_f32_16x16x32_bf16 v[36:39], v[164:167], v[136:139], v[36:39]\n\t" \
  "ds_read_b64_tr_b16 v[218:219], v240 offset:25600\n\t" \
  "v_mfma_f32_16x16x32_bf16 v[52:55], v[164:167], v[140:143], v[52:55]\n\t" \
  "ds_read_b128 v[196:199], v225 offset:10240\n\t" \
  "v_mfma_f32_16x16x32_bf16 v[68:71], v[164:167], v[144:147], v[68:71]\n\t" \
  "ds_read_b64_tr_b16 v[220:221], v241 offset:24576\n\t" \
  "v_mfma_f32_16x16x32_bf16 v[84:87], v[164:167], v[148:151], v[84:87]\n\t" \
  "ds_read_b128 v[200:203], v225 offset:12288\n\t" \
  "v_mfma_f32_16x16x32_bf16 v[100:103], v[164:167], v[152:155], v[100:103]\n\t" \
  "ds_read_b64_tr_b16 v[222:223], v241 offset:25600\n\t" \
  "v_mfma_f32_16x16x32_bf16 v[116:119], v[164:167], v[156:159], v[116:119]\n\t" \
  "ds_read_b128 v[204:207], v225 offset:14336\n\t" \
  "v_mfma_f32_16x16x32_bf16 v[8:11], v[168:171], v[128:131], v[8:11]\n\t" \
  "buffer_load_dwordx4 v238, s[44:47], s49 offen offset:2048 lds\n\t" \
  "v_mfma_f32_16x16x32_bf16 v[24:27], v[168:171], v[132:135], v[24:27]\n\t" \
  "v_mfma_f32_16x16x32_bf16 v[40:43], v[168:171], v[136:139], v[40:43]\n\t" \
  "v_mfma_f32_16x16x32_bf16 v[56:59], v[168:171], v[140:143], v[56:59]\n\t" \
  "v_mfma_f32_16x16x32_bf16 v[72:75], v[168:171], v[144:147], v[72:75]\n\t" \
  "v_mfma_f32_16x16x32_bf16 v[88:91], v[168:171], v[148:151], v[88:91]\n\t" \
  "v_mfma_f32_16x16x32_bf16 v[104:107], v[168:171], v[152:155], v[104:107]\n\t" \
  "v_mfma_f32_16x16x32_bf16 v[120:123], v[168:171], v[156:159], v[120:123]\n\t" \
  "buffer_load_dwordx4 v239, s[44:47], s49 offen offset:3072 lds\n\t" \
  "v_mfma_f32_16x16x32_bf16 v[12:15], v[172:175], v[128:131], v[12:15]\n\t" \
  "s_add_u32 s49, s49, 458752\n\t" \
  "s_add_u32 s58, s58, 0x8000\n\t" \
  "s_cmp_eq_u32 s58, 0x18000\n\t" \
  "s_cselect_b32 s58, 0, s58\n\t" \
  "v_xor_b32 v224, 0x8000, v224\n\t" \
  "v_mfma_f32_16x16x32_bf16 v[28:31], v[172:175], v[132:135], v[28:31]\n\t" \
  "v_add_u32 v226, s53, v228\n\t" \
  "v_mfma_f32_16x16x32_bf16 v[44:47], v[172:175], v[136:139], v[44:47]\n\t" \
  "v_add_u32 v227, s53, v229\n\t" \
  "v_mfma_f32_16x16x32_bf16 v[60:63], v[172:175], v[140:143], v[60:63]\n\t" \
  "v_mfma_f32_16x16x32_bf16 v[76:79], v[172:175], v[144:147], v[76:79]\n\t" \
  "v_mfma_f32_16x16x32_bf16 v[92:95], v[172:175], v[148:151], v[92:95]\n\t" \
  "v_mfma_f32_16x16x32_bf16 v[108:111], v[172:175], v[152:155], v[108:111]\n\t" \
  "v_mfma_f32_16x16x32_bf16 v[124:127], v[172:175], v[156:159], v[124:127]\n\t" \
  "s_waitcnt vmcnt(5) lgkmcnt(0)\n\t" \
  "s_barrier\n\t" \
  "v_mfma_f32_16x16x32_bf16 v[0:3], v[208:211], v[176:179], v[0:3]\n\t" \
  "ds_read_b64_tr_b16 v[160:161], v226 offset:0\n\t" \
  "v_mfma_f32_16x16x32_bf16 v[16:19], v[208:211], v[180:183], v[16:19]\n\t" \
  "s_add_u32 m0, s50, s59\n\t" \
  "ds_read_b128 v[128:131], v224 offset:0\n\t" \
  "v_mfma_f32_16x16x32_bf16 v[32:35], v[208:211], v[184:187], v[32:35]\n\t" \
  "buffer_load_dwordx4 v232, s[40:43], s48 offen offset:0 lds\n\t" \
  "ds_read_b64_tr_b16 v[162:163], v226 offset:1024\n\t" \
  "v_mfma_f32_16x16x32_bf16 v[48:51], v[208:211], v[188:191], v[48:51]\n\t" \
  "ds_read_b128 v[132:135], v224 offset:2048\n\t" \
  "v_mfma_f32_16x16x32_bf16 v[64:67], v[208:211], v[192:195], v[64:67]\n\t" \
  "ds_read_b64_tr_b16 v[164:165], v227 offset:0\n\t" \
  "v_mfma_f32_16x16x32_bf16 v[80:83], v[208:211], v[196:199], v[80:83]\n\t" \
  "ds_read_b128 v[136:139], v224 offset:4096\n\t" \
  "v_mfma_f32_16x16x32_bf16 v[96:99], v[208:211], v[200:203], v[96:99]\n\t" \
  "ds_read_b64_tr_b16 v[166:167], v227 offset:1024\n\t" \
  "v_mfma_f32_16x16x32_bf16 v[112:115], v[208:211], v[204:207], v[112:115]\n\t" \
  "ds_read_b128 v[140:143], v224 offset:6144\n\t" \
  "v_mfma_f32_16x16x32_bf16 v[4:7], v[212:215], v[176:179], v[4:7]\n\t" \
  "ds_read_b64_tr_b16 v[168:169], v226 offset:16384\n\t" \
  "v_mfma_f32_16x16x32_bf16 v[20:23], v[212:215], v[180:183], v[20:23]\n\t" \
  "buffer_load_dwordx4 v233, s[40:43], s48 offen offset:1024 lds\n\t" \
  "ds_read_b128 v[144:147], v224 offset:8192\n\t" \
  "v_mfma_f32_16x16x32_bf16 v[36:39], v[212:215], v[184:187], v[36:39]\n\t" \
  "ds_read_b64_tr_b16 v[170:171], v226 offset:17408\n\t" \
  "v_mfma_f32_16x16x32_bf16 v[52:55], v[212:215], v[188:191], v[52:55]\n\t" \
  "ds_read_b128 v[148:151], v224 offset:10240\n\t" \
  "v_mfma_f32_16x16x32_bf16 v[68:71], v[212:215], v[192:195], v[68:71]\n\t" \
  "ds_read_b64_tr_b16 v[172:173], v227 offset:16384\n\t" \
  "v_mfma_f32_16x16x32_bf16 v[84:87], v[212:215], v[196:199], v[84:87]\n\t" \
  "ds_read_b128 v[152:155], v224 offset:12288\n\t" \
  "v_mfma_f32_16x16x32_bf16 v[100:103], v[212:215], v[200:203], v[100:103]\n\t" \
  "ds_read_b64_tr_b16 v[174:175], v227 offset:17408\n\t" \
  "v_mfma_f32_16x16x32_bf16 v[116:119], v[212:215], v[204:207], v[116:119]\n\t" \
  "ds_read_b128 v[156:159], v224 offset:14336\n\t" \
  "v_mfma_f32_16x16x32_bf16 v[8:11], v[216:219], v[176:179], v[8:11]\n\t" \
  "buffer_load_dwordx4 v234, s[40:43], s48 offen offset:2048 lds\n\t" \
  "v_mfma_f32_16x16x32_bf16 v[24:27], v[216:219], v[180:183], v[24:27]\n\t" \
  "v_mfma_f32_16x16x32_bf16 v[40:43], v[216:219], v[184:187], v[40:43]\n\t" \
  "v_mfma_f32_16x16x32_bf16 v[56:59], v[216:219], v[188:191], v[56:59]\n\t" \
  "v_mfma_f32_16x16x32_bf16 v[72:75], v[216:219], v[192:195], v[72:75]\n\t" \
  "v_mfma_f32_16x16x32_bf16 v[88:91], v[216:219], v[196:199], v[88:91]\n\t" \
  "v_mfma_f32_16x16x32_bf16 v[104:107], v[216:219], v[200:203], v[104:107]\n\t" \
  "v_mfma_f32_16x16x32_bf16 v[120:123], v[216:219], v[204:207], v[120:123]\n\t" \
  "buffer_load_dwordx4 v235, s[40:43], s48 offen offset:3072 lds\n\t" \
  "v_mfma_f32_16x16x32_bf16 v[12:15], v[220:223], v[176:179], v[12:15]\n\t" \
  "s_add_u32 s48, s48, 128\n\t" \
  "s_xor_b32 s59, s59, 0x8000\n\t" \
  "v_xor_b32 v225, 0x8000, v225\n\t" \
  "v_mfma_f32_16x16x32_bf16 v[28:31], v[220:223], v[180:183], v[28:31]\n\t" \
  "v_add_u32 v240, s53, v228\n\t" \
  "v_mfma_f32_16x16x32_bf16 v[44:47], v[220:223], v[184:187], v[44:47]\n\t" \
  "v_add_u32 v241, s53, v229\n\t" \
  "v_mfma_f32_16x16x32_bf16 v[60:63], v[220:223], v[188:191], v[60:63]\n\t" \
  "v_mfma_f32_16x16x32_bf16 v[76:79], v[220:223], v[192:195], v[76:79]\n\t" \
  "v_mfma_f32_16x16x32_bf16 v[92:95], v[220:223], v[196:199], v[92:95]\n\t" \
  "v_mfma_f32_16x16x32_bf16 v[108:111], v[220:223], v[200:203], v[108:111]\n\t" \
  "v_mfma_f32_16x16x32_bf16 v[124:127], v[220:223], v[204:207], v[124:127]\n\t" \
  "s_waitcnt lgkmcnt(0)\n\t" \
  "v_mfma_f32_16x16x32_bf16 v[0:3], v[160:163], v[128:131], v[0:3]\n\t" \
  "global_load_dword v254, v[250:251], off\n\t" \
  "v_lshl_add_u64 v[250:251], v[252:253], 0, v[250:251]\n\t" \
  "s_add_u32 s53, s53, 0x8000\n\t" \
  "s_cmp_eq_u32 s53, 0x18000\n\t" \
  "s_cselect_b32 s53, 0, s53\n\t" \
  "ds_read_b64_tr_b16 v[208:209], v240 offset:8192\n\t" \
  "v_mfma_f32_16x16x32_bf16 v[16:19], v[160:163], v[132:135], v[16:19]\n\t" \
  "s_add_u32 m0, s51, s58\n\t" \
  "ds_read_b128 v[176:179], v225 offset:0\n\t" \
  "v_mfma_f32_16x16x32_bf16 v[32:35], v[160:163], v[136:139], v[32:35]\n\t" \
  "buffer_load_dwordx4 v236, s[44:47], s49 offen offset:0 lds\n\t" \
  "ds_read_b64_tr_b16 v[210:211], v240 offset:9216\n\t" \
  "v_mfma_f32_16x16x32_bf16 v[48:51], v[160:163], v[140:143], v[48:51]\n\t" \
  "ds_read_b128 v[180:183], v225 offset:2048\n\t" \
  "v_mfma_f32_16x16x32_bf16 v[64:67], v[160:163], v[144:147], v[64:67]\n\t" \
  "ds_read_b64_tr_b16 v[212:213], v241 offset:8192\n\t" \
  "v_mfma_f32_16x16x32_bf16 v[80:83], v[160:163], v[148:151], v[80:83]\n\t" \
  "ds_read_b128 v[184:187], v225 offset:4096\n\t" \
  "v_mfma_f32_16x16x32_bf16 v[96:99], v[160:163], v[152:155], v[96:99]\n\t" \
  "ds_read_b64_tr_b16 v[214:215], v241 offset:9216\n\t" \
  "v_mfma_f32_16x16x32_bf16 v[112:115], v[160:163], v[156:159], v[112:115]\n\t" \
  "ds_read_b128 v[188:191], v225 offset:6144\n\t" \
  "v_mfma_f32_16x16x32_bf16 v[4:7], v[164:167], v[128:131], v[4:7]\n\t" \
  "ds_read_b64_tr_b16 v[216:217], v240 offset:24576\n\t" \
  "v_mfma_f32_16x16x32_bf16 v[20:23], v[164:167], v[132:135], v[20:23]\n\t" \
  "buffer_load_dwordx4 v237, s[44:47], s49 offen offset:1024 lds\n\t" \
  "ds_read_b128 v[192:195], v225 offset:8192\n\t" \
  "v_mfma_f32_16x16x32_bf16 v[36:39], v[164:167], v[136:139], v[36:39]\n\t" \
  "ds_read_b64_tr_b16 v[218:219], v240 offset:25600\n\t" \
  "v_mfma_f32_16x16x32_bf16 v[52:55], v[164:167], v[140:143], v[52:55]\n\t" \
  "ds_read_b128 v[196:199], v225 offset:10240\n\t" \
  "v_mfma_f32_16x16x32_bf16 v[68:71], v[164:167], v[144:147], v[68:71]\n\t" \
  "ds_read_b64_tr_b16 v[220:221], v241 offset:24576\n\t" \
  "v_mfma_f32_16x16x32_bf16 v[84:87], v[164:167], v[148:151], v[84:87]\n\t" \
  "ds_read_b128 v[200:203], v225 offset:12288\n\t" \
  "v_mfma_f32_16x16x32_bf16 v[100:103], v[164:167], v[152:155], v[100:103]\n\t" \
  "ds_read_b64_tr_b16 v[222:223], v241 offset:25600\n\t" \
  "v_mfma_f32_16x16x32_bf16 v[116:119], v[164:167], v[156:159], v[116:119]\n\t" \
  "ds_read_b128 v[204:207], v225 offset:14336\n\t" \
  "v_mfma_f32_16x16x32_bf16 v[8:11], v[168:171], v[128:131], v[8:11]\n\t" \
  "buffer_load_dwordx4 v238, s[44:47], s49 offen offset:2048 lds\n\t" \
  "v_mfma_f32_16x16x32_bf16 v[24:27], v[168:171], v[132:135], v[24:27]\n\t" \
  "v_mfma_f32_16x16x32_bf16 v[40:43], v[168:171], v[136:139], v[40:43]\n\t" \
  "v_mfma_f32_16x16x32_bf16 v[56:59], v[168:171], v[140:143], v[56:59]\n\t" \
  "v_mfma_f32_16x16x32_bf16 v[72:75], v[168:171], v[144:147], v[72:75]\n\t" \
  "v_mfma_f32_16x16x32_bf16 v[88:91], v[168:171], v[148:151], v[88:91]\n\t" \
  "v_mfma_f32_16x16x32_bf16 v[104:107], v[168:171], v[152:155], v[104:107]\n\t" \
  "v_mfma_f32_16x16x32_bf16 v[120:123], v[168:171], v[156:159], v[120:123]\n\t" \
  "buffer_load_dwordx4 v239, s[44:47], s49 offen offset:3072 lds\n\t" \
  "v_mfma_f32_16x16x32_bf16 v[12:15], v[172:175], v[128:131], v[12:15]\n\t" \
  "s_add_u32 s49, s49, 458752\n\t" \
  "s_add_u32 s58, s58, 0x8000\n\t" \
  "s_cmp_eq_u32 s58, 0x18000\n\t" \
  "s_cselect_b32 s58, 0, s58\n\t" \
  "v_xor_b32 v224, 0x8000, v224\n\t" \
  "v_mfma_f32_16x16x32_bf16 v[28:31], v[172:175], v[132:135], v[28:31]\n\t" \
  "v_add_u32 v226, s53, v228\n\t" \
  "v_mfma_f32_16x16x32_bf16 v[44:47], v[172:175], v[136:139], v[44:47]\n\t" \
  "v_add_u32 v227, s53, v229\n\t" \
  "v_mfma_f32_16x16x32_bf16 v[60:63], v[172:175], v[140:143], v[60:63]\n\t" \
  "v_mfma_f32_16x16x32_bf16 v[76:79], v[172:175], v[144:147], v[76:79]\n\t" \
  "v_mfma_f32_16x16x32_bf16 v[92:95], v[172:175], v[148:151], v[92:95]\n\t" \
  "v_mfma_f32_16x16x32_bf16 v[108:111], v[172:175], v[152:155], v[108:111]\n\t" \
  "v_mfma_f32_16x16x32_bf16 v[124:127], v[172:175], v[156:159], v[124:127]\n\t" \
  "s_waitcnt vmcnt(5) lgkmcnt(0)\n\t" \
  "s_barrier\n\t" \
  "v_mfma_f32_16x16x32_bf16 v[0:3], v[208:211], v[176:179], v[0:3]\n\t" \
  "ds_read_b64_tr_b16 v[160:161], v226 offset:0\n\t" \
  "v_mfma_f32_16x16x32_bf16 v[16:19], v[208:211], v[180:183], v[16:19]\n\t" \
  "s_add_u32 m0, s50, s59\n\t" \
  "ds_read_b128 v[128:131], v224 offset:0\n\t" \
  "v_mfma_f32_16x16x32_bf16 v[32:35], v[208:211], v[184:187], v[32:35]\n\t" \
  "buffer_load_dwordx4 v232, s[40:43], s48 offen offset:0 lds\n\t" \
  "ds_read_b64_tr_b16 v[162:163], v226 offset:1024\n\t" \
  "v_mfma_f32_16x16x32_bf16 v[48:51], v[208:211], v[188:191], v[48:51]\n\t" \
  "ds_read_b128 v[132:135], v224 offset:2048\n\t" \
  "v_mfma_f32_16x16x32_bf16 v[64:67], v[208:211], v[192:195], v[64:67]\n\t" \
  "ds_read_b64_tr_b16 v[164:165], v227 offset:0\n\t" \
  "v_mfma_f32_16x16x32_bf16 v[80:83], v[208:211], v[196:199], v[80:83]\n\t" \
  "ds_read_b128 v[136:139], v224 offset:4096\n\t" \
  "v_mfma_f32_16x16x32_bf16 v[96:99], v[208:211], v[200:203], v[96:99]\n\t" \
  "ds_read_b64_tr_b16 v[166:167], v227 offset:1024\n\t" \
  "v_mfma_f32_16x16x32_bf16 v[112:115], v[208:211], v[204:207], v[112:115]\n\t" \
  "ds_read_b128 v[140:143], v224 offset:6144\n\t" \
  "v_mfma_f32_16x16x32_bf16 v[4:7], v[212:215], v[176:179], v[4:7]\n\t" \
  "ds_read_b64_tr_b16 v[168:169], v226 offset:16384\n\t" \
  "v_mfma_f32_16x16x32_bf16 v[20:23], v[212:215], v[180:183], v[20:23]\n\t" \
  "buffer_load_dwordx4 v233, s[40:43], s48 offen offset:1024 lds\n\t" \
  "ds_read_b128 v[144:147], v224 offset:8192\n\t" \
  "v_mfma_f32_16x16x32_bf16 v[36:39], v[212:215], v[184:187], v[36:39]\n\t" \
  "ds_read_b64_tr_b16 v[170:171], v226 offset:17408\n\t" \
  "v_mfma_f32_16x16x32_bf16 v[52:55], v[212:215], v[188:191], v[52:55]\n\t" \
  "ds_read_b128 v[148:151], v224 offset:10240\n\t" \
  "v_mfma_f32_16x16x32_bf16 v[68:71], v[212:215], v[192:195], v[68:71]\n\t" \
  "ds_read_b64_tr_b16 v[172:173], v227 offset:16384\n\t" \
  "v_mfma_f32_16x16x32_bf16 v[84:87], v[212:215], v[196:199], v[84:87]\n\t" \
  "ds_read_b128 v[152:155], v224 offset:12288\n\t" \
  "v_mfma_f32_16x16x32_bf16 v[100:103], v[212:215], v[200:203], v[100:103]\n\t" \
  "ds_read_b64_tr_b16 v[174:175], v227 offset:17408\n\t" \
  "v_mfma_f32_16x16x32_bf16 v[116:119], v[212:215], v[204:207], v[116:119]\n\t" \
  "ds_read_b128 v[156:159], v224 offset:14336\n\t" \
  "v_mfma_f32_16x16x32_bf16 v[8:11], v[216:219], v[176:179], v[8:11]\n\t" \
  "buffer_load_dwordx4 v234, s[40:43], s48 offen offset:2048 lds\n\t" \
  "v_mfma_f32_16x16x32_bf16 v[24:27], v[216:219], v[180:183], v[24:27]\n\t" \
  "v_mfma_f32_16x16x32_bf16 v[40:43], v[216:219], v[184:187], v[40:43]\n\t" \
  "v_mfma_f32_16x16x32_bf16 v[56:59], v[216:219], v[188:191], v[56:59]\n\t" \
  "v_mfma_f32_16x16x32_bf16 v[72:75], v[216:219], v[192:195], v[72:75]\n\t" \
  "v_mfma_f32_16x16x32_bf16 v[88:91], v[216:219], v[196:199], v[88:91]\n\t" \
  "v_mfma_f32_16x16x32_bf16 v[104:107], v[216:219], v[200:203], v[104:107]\n\t" \
  "v_mfma_f32_16x16x32_bf16 v[120:123], v[216:219], v[204:207], v[120:123]\n\t" \
  "buffer_load_dwordx4 v235, s[40:43], s48 offen offset:3072 lds\n\t" \
  "v_mfma_f32_16x16x32_bf16 v[12:15], v[220:223], v[176:179], v[12:15]\n\t" \
  "s_add_u32 s48, s48, 128\n\t" \
  "s_xor_b32 s59, s59, 0x8000\n\t" \
  "v_xor_b32 v225, 0x8000, v225\n\t" \
  "v_mfma_f32_16x16x32_bf16 v[28:31], v[220:223], v[180:183], v[28:31]\n\t" \
  "v_add_u32 v240, s53, v228\n\t" \
  "v_mfma_f32_16x16x32_bf16 v[44:47], v[220:223], v[184:187], v[44:47]\n\t" \
  "v_add_u32 v241, s53, v229\n\t" \
  "v_mfma_f32_16x16x32_bf16 v[60:63], v[220:223], v[188:191], v[60:63]\n\t" \
  "s_sub_u32 s52, s52, 1\n\t" \
  "v_mfma_f32_16x16x32_bf16 v[76:79], v[220:223], v[192:195], v[76:79]\n\t" \
  "s_cmp_lg_u32 s52, 0\n\t" \
  "v_mfma_f32_16x16x32_bf16 v[92:95], v[220:223], v[196:199], v[92:95]\n\t" \
  "v_mfma_f32_16x16x32_bf16 v[108:111], v[220:223], v[200:203], v[108:111]\n\t" \
  "v_mfma_f32_16x16x32_bf16 v[124:127], v[220:223], v[204:207], v[124:127]\n\t" \
  "s_waitcnt lgkmcnt(0)\n\t" \
  "s_cbranch_scc1 1b\n\t" \
  "4:\n\t" \
  "v_mfma_f32_16x16x32_bf16 v[0:3], v[160:163], v[128:131], v[0:3]\n\t" \
  "s_getpc_b64 s[54:55]\n\t" \
  "s_add_u32 s54, s54, s83\n\t" \
  "s_addc_u32 s55, s55, 0\n\t" \
  "global_load_dword v254, v253, s[54:55]\n\t" \
  "s_add_u32 s53, s53, 0x8000\n\t" \
  "s_cmp_eq_u32 s53, 0x18000\n\t" \
  "s_cselect_b32 s53, 0, s53\n\t" \
  "ds_read_b64_tr_b16 v[208:209], v240 offset:8192\n\t" \
  "v_mfma_f32_16x16x32_bf16 v[16:19], v[160:163], v[132:135], v[16:19]\n\t" \
  "s_add_u32 m0, s51, s58\n\t" \
  "ds_read_b128 v[176:179], v225 offset:0\n\t" \
  "v_mfma_f32_16x16x32_bf16 v[32:35], v[160:163], v[136:139], v[32:35]\n\t" \
  "buffer_load_dwordx4 v236, s[44:47], s49 offen offset:0 lds\n\t" \
  "ds_read_b64_tr_b16 v[210:211], v240 offset:9216\n\t" \
  "v_mfma_f32_16x16x32_bf16 v[48:51], v[160:163], v[140:143], v[48:51]\n\t" \
  "ds_read_b128 v[180:183], v225 offset:2048\n\t" \
  "v_mfma_f32_16x16x32_bf16 v[64:67], v[160:163], v[144:147], v[64:67]\n\t" \
  "ds_read_b64_tr_b16 v[212:213], v241 offset:8192\n\t" \
  "v_mfma_f32_16x16x32_bf16 v[80:83], v[160:163], v[148:151], v[80:83]\n\t" \
  "ds_read_b128 v[184:187], v225 offset:4096\n\t" \
  "v_mfma_f32_16x16x32_bf16 v[96:99], v[160:163], v[152:155], v[96:99]\n\t" \
  "ds_read_b64_tr_b16 v[214:215], v241 offset:9216\n\t" \
  "v_mfma_f32_16x16x32_bf16 v[112:115], v[160:163], v[156:159], v[112:115]\n\t" \
  "ds_read_b128 v[188:191], v225 offset:6144\n\t" \
  "v_mfma_f32_16x16x32_bf16 v[4:7], v[164:167], v[128:131], v[4:7]\n\t" \
  "ds_read_b64_tr_b16 v[216:217], v240 offset:24576\n\t" \
  "v_mfma_f32_16x16x32_bf16 v[20:23], v[164:167], v[132:135], v[20:23]\n\t" \
  "buffer_load_dwordx4 v237, s[44:47], s49 offen offset:1024 lds\n\t" \
  "ds_read_b128 v[192:195], v225 offset:8192\n\t" \
  "v_mfma_f32_16x16x32_bf16 v[36:39], v[164:167], v[136:139], v[36:39]\n\t" \
  "ds_read_b64_tr_b16 v[218:219], v240 offset:25600\n\t" \
  "v_mfma_f32_16x16x32_bf16 v[52:55], v[164:167], v[140:143], v[52:55]\n\t" \
  "ds_read_b128 v[196:199], v225 offset:10240\n\t" \
  "v_mfma_f32_16x16x32_bf16 v[68:71], v[164:167], v[144:147], v[68:71]\n\t" \
  "ds_read_b64_tr_b16 v[220:221], v241 offset:24576\n\t" \
  "v_mfma_f32_16x16x32_bf16 v[84:87], v[164:167], v[148:151], v[84:87]\n\t" \
  "ds_read_b128 v[200:203], v225 offset:12288\n\t" \
  "v_mfma_f32_16x16x32_bf16 v[100:103], v[164:167], v[152:155], v[100:103]\n\t" \
  "ds_read_b64_tr_b16 v[222:223], v241 offset:25600\n\t" \
  "v_mfma_f32_16x16x32_bf16 v[116:119], v[164:167], v[156:159], v[116:119]\n\t" \
  "ds_read_b128 v[204:207], v225 offset:14336\n\t" \
  "v_mfma_f32_16x16x32_bf16 v[8:11], v[168:171], v[128:131], v[8:11]\n\t" \
  "buffer_load_dwordx4 v238, s[44:47], s49 offen offset:2048 lds\n\t" \
  "v_mfma_f32_16x16x32_bf16 v[24:27], v[168:171], v[132:135], v[24:27]\n\t" \
  "v_mfma_f32_16x16x32_bf16 v[40:43], v[168:171], v[136:139], v[40:43]\n\t" \
  "v_mfma_f32_16x16x32_bf16 v[56:59], v[168:171], v[140:143], v[56:59]\n\t" \
  "v_mfma_f32_16x16x32_bf16 v[72:75], v[168:171], v[144:147], v[72:75]\n\t" \
  "v_mfma_f32_16x16x32_bf16 v[88:91], v[168:171], v[148:151], v[88:91]\n\t" \
  "v_mfma_f32_16x16x32_bf16 v[104:107], v[168:171], v[152:155], v[104:107]\n\t" \
  "v_mfma_f32_16x16x32_bf16 v[120:123], v[168:171], v[156:159], v[120:123]\n\t" \
  "buffer_load_dwordx4 v239, s[44:47], s49 offen offset:3072 lds\n\t" \
  "v_mfma_f32_16x16x32_bf16 v[12:15], v[172:175], v[128:131], v[12:15]\n\t" \
  "s_add_u32 s49, s49, 458752\n\t" \
  "s_add_u32 s58, s58, 0x8000\n\t" \
  "s_cmp_eq_u32 s58, 0x18000\n\t" \
  "s_cselect_b32 s58, 0, s58\n\t" \
  "v_xor_b32 v224, 0x8000, v224\n\t" \
  "v_mfma_f32_16x16x32_bf16 v[28:31], v[172:175], v[132:135], v[28:31]\n\t" \
  "v_add_u32 v226, s53, v228\n\t" \
  "v_mfma_f32_16x16x32_bf16 v[44:47], v[172:175], v[136:139], v[44:47]\n\t" \
  "v_add_u32 v227, s53, v229\n\t" \
  "v_mfma_f32_16x16x32_bf16 v[60:63], v[172:175], v[140:143], v[60:63]\n\t" \
  "v_mfma_f32_16x16x32_bf16 v[76:79], v[172:175], v[144:147], v[76:79]\n\t" \
  "v_mfma_f32_16x16x32_bf16 v[92:95], v[172:175], v[148:151], v[92:95]\n\t" \
  "v_mfma_f32_16x16x32_bf16 v[108:111], v[172:175], v[152:155], v[108:111]\n\t" \
  "v_mfma_f32_16x16x32_bf16 v[124:127], v[172:175], v[156:159], v[124:127]\n\t" \
  "s_waitcnt vmcnt(5) lgkmcnt(0)\n\t" \
  "s_barrier\n\t" \
  "v_mfma_f32_16x16x32_bf16 v[0:3], v[208:211], v[176:179], v[0:3]\n\t" \
  "ds_read_b64_tr_b16 v[160:161], v226 offset:0\n\t" \
  "v_mfma_f32_16x16x32_bf16 v[16:19], v[208:211], v[180:183], v[16:19]\n\t" \
  "s_add_u32 m0, s50, s59\n\t" \
  "ds_read_b128 v[128:131], v224 offset:0\n\t" \
  "v_mfma_f32_16x16x32_bf16 v[32:35], v[208:211], v[184:187], v[32:35]\n\t" \
  "buffer_load_dwordx4 v232, s[40:43], s48 offen offset:0 lds\n\t" \
  "ds_read_b64_tr_b16 v[162:163], v226 offset:1024\n\t" \
  "v_mfma_f32_16x16x32_bf16 v[48:51], v[208:211], v[188:191], v[48:51]\n\t" \
  "ds_read_b128 v[132:135], v224 offset:2048\n\t" \
  "v_mfma_f32_16x16x32_bf16 v[64:67], v[208:211], v[192:195], v[64:67]\n\t" \
  "ds_read_b64_tr_b16 v[164:165], v227 offset:0\n\t" \
  "v_mfma_f32_16x16x32_bf16 v[80:83], v[208:211], v[196:199], v[80:83]\n\t" \
  "ds_read_b128 v[136:139], v224 offset:4096\n\t" \
  "v_mfma_f32_16x16x32_bf16 v[96:99], v[208:211], v[200:203], v[96:99]\n\t" \
  "ds_read_b64_tr_b16 v[166:167], v227 offset:1024\n\t" \
  "v_mfma_f32_16x16x32_bf16 v[112:115], v[208:211], v[204:207], v[112:115]\n\t" \
  "ds_read_b128 v[140:143], v224 offset:6144\n\t" \
  "v_mfma_f32_16x16x32_bf16 v[4:7], v[212:215], v[176:179], v[4:7]\n\t" \
  "ds_read_b64_tr_b16 v[168:169], v226 offset:16384\n\t" \
  "v_mfma_f32_16x16x32_bf16 v[20:23], v[212:215], v[180:183], v[20:23]\n\t" \
  "buffer_load_dwordx4 v233, s[40:43], s48 offen offset:1024 lds\n\t" \
  "ds_read_b128 v[144:147], v224 offset:8192\n\t" \
  "v_mfma_f32_16x16x32_bf16 v[36:39], v[212:215], v[184:187], v[36:39]\n\t" \
  "ds_read_b64_tr_b16 v[170:171], v226 offset:17408\n\t" \
  "v_mfma_f32_16x16x32_bf16 v[52:55], v[212:215], v[188:191], v[52:55]\n\t" \
  "ds_read_b128 v[148:151], v224 offset:10240\n\t" \
  "v_mfma_f32_16x16x32_bf16 v[68:71], v[212:215], v[192:195], v[68:71]\n\t" \
  "ds_read_b64_tr_b16 v[172:173], v227 offset:16384\n\t" \
  "v_mfma_f32_16x16x32_bf16 v[84:87], v[212:215], v[196:199], v[84:87]\n\t" \
  "ds_read_b128 v[152:155], v224 offset:12288\n\t" \
  "v_mfma_f32_16x16x32_bf16 v[100:103], v[212:215], v[200:203], v[100:103]\n\t" \
  "ds_read_b64_tr_b16 v[174:175], v227 offset:17408\n\t" \
  "v_mfma_f32_16x16x32_bf16 v[116:119], v[212:215], v[204:207], v[116:119]\n\t" \
  "ds_read_b128 v[156:159], v224 offset:14336\n\t" \
  "v_mfma_f32_16x16x32_bf16 v[8:11], v[216:219], v[176:179], v[8:11]\n\t" \
  "buffer_load_dwordx4 v234, s[40:43], s48 offen offset:2048 lds\n\t" \
  "v_mfma_f32_16x16x32_bf16 v[24:27], v[216:219], v[180:183], v[24:27]\n\t" \
  "v_mfma_f32_16x16x32_bf16 v[40:43], v[216:219], v[184:187], v[40:43]\n\t" \
  "v_mfma_f32_16x16x32_bf16 v[56:59], v[216:219], v[188:191], v[56:59]\n\t" \
  "v_mfma_f32_16x16x32_bf16 v[72:75], v[216:219], v[192:195], v[72:75]\n\t" \
  "v_mfma_f32_16x16x32_bf16 v[88:91], v[216:219], v[196:199], v[88:91]\n\t" \
  "v_mfma_f32_16x16x32_bf16 v[104:107], v[216:219], v[200:203], v[104:107]\n\t" \
  "v_mfma_f32_16x16x32_bf16 v[120:123], v[216:219], v[204:207], v[120:123]\n\t" \
  "buffer_load_dwordx4 v235, s[40:43], s48 offen offset:3072 lds\n\t" \
  "v_mfma_f32_16x16x32_bf16 v[12:15], v[220:223], v[176:179], v[12:15]\n\t" \
  "s_add_u32 s48, s48, 128\n\t" \
  "s_xor_b32 s59, s59, 0x8000\n\t" \
  "v_xor_b32 v225, 0x8000, v225\n\t" \
  "v_mfma_f32_16x16x32_bf16 v[28:31], v[220:223], v[180:183], v[28:31]\n\t" \
  "v_add_u32 v240, s53, v228\n\t" \
  "v_mfma_f32_16x16x32_bf16 v[44:47], v[220:223], v[184:187], v[44:47]\n\t" \
  "v_add_u32 v241, s53, v229\n\t" \
  "v_mfma_f32_16x16x32_bf16 v[60:63], v[220:223], v[188:191], v[60:63]\n\t" \
  "v_mfma_f32_16x16x32_bf16 v[76:79], v[220:223], v[192:195], v[76:79]\n\t" \
  "v_mfma_f32_16x16x32_bf16 v[92:95], v[220:223], v[196:199], v[92:95]\n\t" \
  "v_mfma_f32_16x16x32_bf16 v[108:111], v[220:223], v[200:203], v[108:111]\n\t" \
  "v_mfma_f32_16x16x32_bf16 v[124:127], v[220:223], v[204:207], v[124:127]\n\t" \
  "s_waitcnt lgkmcnt(0)\n\t" \
  "v_mfma_f32_16x16x32_bf16 v[0:3], v[160:163], v[128:131], v[0:3]\n\t" \
  "s_add_u32 s53, s53, 0x8000\n\t" \
  "s_cmp_eq_u32 s53, 0x18000\n\t" \
  "s_cselect_b32 s53, 0, s53\n\t" \
  "ds_read_b64_tr_b16 v[208:209], v240 offset:8192\n\t" \
  "v_mfma_f32_16x16x32_bf16 v[16:19], v[160:163], v[132:135], v[16:19]\n\t" \
  "s_add_u32 m0, s51, s58\n\t" \
  "ds_read_b128 v[176:179], v225 offset:0\n\t" \
  "v_mfma_f32_16x16x32_bf16 v[32:35], v[160:163], v[136:139], v[32:35]\n\t" \
  "buffer_load_dwordx4 v236, s[44:47], s49 offen offset:0 lds\n\t" \
  "ds_read_b64_tr_b16 v[210:211], v240 offset:9216\n\t" \
  "v_mfma_f32_16x16x32_bf16 v[48:51], v[160:163], v[140:143], v[48:51]\n\t" \
  "ds_read_b128 v[180:183], v225 offset:2048\n\t" \
  "v_mfma_f32_16x16x32_bf16 v[64:67], v[160:163], v[144:147], v[64:67]\n\t" \
  "ds_read_b64_tr_b16 v[212:213], v241 offset:8192\n\t" \
  "v_mfma_f32_16x16x32_bf16 v[80:83], v[160:163], v[148:151], v[80:83]\n\t" \
  "ds_read_b128 v[184:187], v225 offset:4096\n\t" \
  "v_mfma_f32_16x16x32_bf16 v[96:99], v[160:163], v[152:155], v[96:99]\n\t" \
  "ds_read_b64_tr_b16 v[214:215], v241 offset:9216\n\t" \
  "v_mfma_f32_16x16x32_bf16 v[112:115], v[160:163], v[156:159], v[112:115]\n\t" \
  "ds_read_b128 v[188:191], v225 offset:6144\n\t" \
  "v_mfma_f32_16x16x32_bf16 v[4:7], v[164:167], v[128:131], v[4:7]\n\t" \
  "ds_read_b64_tr_b16 v[216:217], v240 offset:24576\n\t" \
  "v_mfma_f32_16x16x32_bf16 v[20:23], v[164:167], v[132:135], v[20:23]\n\t" \
  "buffer_load_dwordx4 v237, s[44:47], s49 offen offset:1024 lds\n\t" \
  "ds_read_b128 v[192:195], v225 offset:8192\n\t" \
  "v_mfma_f32_16x16x32_bf16 v[36:39], v[164:167], v[136:139], v[36:39]\n\t" \
  "ds_read_b64_tr_b16 v[218:219], v240 offset:25600\n\t" \
  "v_mfma_f32_16x16x32_bf16 v[52:55], v[164:167], v[140:143], v[52:55]\n\t" \
  "ds_read_b128 v[196:199], v225 offset:10240\n\t" \
  "v_mfma_f32_16x16x32_bf16 v[68:71], v[164:167], v[144:147], v[68:71]\n\t" \
  "ds_read_b64_tr_b16 v[220:221], v241 offset:24576\n\t" \
  "v_mfma_f32_16x16x32_bf16 v[84:87], v[164:167], v[148:151], v[84:87]\n\t" \
  "ds_read_b128 v[200:203], v225 offset:12288\n\t" \
  "v_mfma_f32_16x16x32_bf16 v[100:103], v[164:167], v[152:155], v[100:103]\n\t" \
  "ds_read_b64_tr_b16 v[222:223], v241 offset:25600\n\t" \
  "v_mfma_f32_16x16x32_bf16 v[116:119], v[164:167], v[156:159], v[116:119]\n\t" \
  "ds_read_b128 v[204:207], v225 offset:14336\n\t" \
  "v_mfma_f32_16x16x32_bf16 v[8:11], v[168:171], v[128:131], v[8:11]\n\t" \
  "buffer_load_dwordx4 v238, s[44:47], s49 offen offset:2048 lds\n\t" \
  "v_mfma_f32_16x16x32_bf16 v[24:27], v[168:171], v[132:135], v[24:27]\n\t" \
  "v_mfma_f32_16x16x32_bf16 v[40:43], v[168:171], v[136:139], v[40:43]\n\t" \
  "v_mfma_f32_16x16x32_bf16 v[56:59], v[168:171], v[140:143], v[56:59]\n\t" \
  "v_mfma_f32_16x16x32_bf16 v[72:75], v[168:171], v[144:147], v[72:75]\n\t" \
  "v_mfma_f32_16x16x32_bf16 v[88:91], v[168:171], v[148:151], v[88:91]\n\t" \
  "v_mfma_f32_16x16x32_bf16 v[104:107], v[168:171], v[152:155], v[104:107]\n\t" \
  "v_mfma_f32_16x16x32_bf16 v[120:123], v[168:171], v[156:159], v[120:123]\n\t" \
  "buffer_load_dwordx4 v239, s[44:47], s49 offen offset:3072 lds\n\t" \
  "v_mfma_f32_16x16x32_bf16 v[12:15], v[172:175], v[128:131], v[12:15]\n\t" \
  "s_add_u32 s49, s49, 458752\n\t" \
  "s_add_u32 s58, s58, 0x8000\n\t" \
  "s_cmp_eq_u32 s58, 0x18000\n\t" \
  "s_cselect_b32 s58, 0, s58\n\t" \
  "v_xor_b32 v224, 0x8000, v224\n\t" \
  "v_mfma_f32_16x16x32_bf16 v[28:31], v[172:175], v[132:135], v[28:31]\n\t" \
  "v_add_u32 v226, s53, v228\n\t" \
  "v_mfma_f32_16x16x32_bf16 v[44:47], v[172:175], v[136:139], v[44:47]\n\t" \
  "v_add_u32 v227, s53, v229\n\t" \
  "v_mfma_f32_16x16x32_bf16 v[60:63], v[172:175], v[140:143], v[60:63]\n\t" \
  "v_mfma_f32_16x16x32_bf16 v[76:79], v[172:175], v[144:147], v[76:79]\n\t" \
  "v_mfma_f32_16x16x32_bf16 v[92:95], v[172:175], v[148:151], v[92:95]\n\t" \
  "v_mfma_f32_16x16x32_bf16 v[108:111], v[172:175], v[152:155], v[108:111]\n\t" \
  "v_mfma_f32_16x16x32_bf16 v[124:127], v[172:175], v[156:159], v[124:127]\n\t" \
  "s_waitcnt vmcnt(4) lgkmcnt(0)\n\t" \
  "s_barrier\n\t" \
  "v_mfma_f32_16x16x32_bf16 v[0:3], v[208:211], v[176:179], v[0:3]\n\t" \
  "ds_read_b64_tr_b16 v[160:161], v226 offset:0\n\t" \
  "v_mfma_f32_16x16x32_bf16 v[16:19], v[208:211], v[180:183], v[16:19]\n\t" \
  "s_add_u32 m0, s50, s59\n\t" \
  "ds_read_b128 v[128:131], v224 offset:0\n\t" \
  "v_mfma_f32_16x16x32_bf16 v[32:35], v[208:211], v[184:187], v[32:35]\n\t" \
  "buffer_load_dwordx4 v232, s[40:43], s48 offen offset:0 lds\n\t" \
  "ds_read_b64_tr_b16 v[162:163], v226 offset:1024\n\t" \
  "v_mfma_f32_16x16x32_bf16 v[48:51], v[208:211], v[188:191], v[48:51]\n\t" \
  "ds_read_b128 v[132:135], v224 offset:2048\n\t" \
  "v_mfma_f32_16x16x32_bf16 v[64:67], v[208:211], v[192:195], v[64:67]\n\t" \
  "ds_read_b64_tr_b16 v[164:165], v227 offset:0\n\t" \
  "v_mfma_f32_16x16x32_bf16 v[80:83], v[208:211], v[196:199], v[80:83]\n\t" \
  "buffer_load_dwordx4 v233, s[40:43], s48 offen offset:1024 lds\n\t" \
  "ds_read_b128 v[136:139], v224 offset:4096\n\t" \
  "v_mfma_f32_16x16x32_bf16 v[96:99], v[208:211], v[200:203], v[96:99]\n\t" \
  "ds_read_b64_tr_b16 v[166:167], v227 offset:1024\n\t" \
  "v_mfma_f32_16x16x32_bf16 v[112:115], v[208:211], v[204:207], v[112:115]\n\t" \
  "ds_read_b128 v[140:143], v224 offset:6144\n\t" \
  "v_mfma_f32_16x16x32_bf16 v[4:7], v[212:215], v[176:179], v[4:7]\n\t" \
  "buffer_load_dwordx4 v234, s[40:43], s48 offen offset:2048 lds\n\t" \
  "ds_read_b64_tr_b16 v[168:169], v226 offset:16384\n\t" \
  "v_mfma_f32_16x16x32_bf16 v[20:23], v[212:215], v[180:183], v[20:23]\n\t" \
  "ds_read_b128 v[144:147], v224 offset:8192\n\t" \
  "v_mfma_f32_16x16x32_bf16 v[36:39], v[212:215], v[184:187], v[36:39]\n\t" \
  "ds_read_b64_tr_b16 v[170:171], v226 offset:17408\n\t" \
  "v_mfma_f32_16x16x32_bf16 v[52:55], v[212:215], v[188:191], v[52:55]\n\t" \
  "buffer_load_dwordx4 v235, s[40:43], s48 offen offset:3072 lds\n\t" \
  "ds_read_b128 v[148:151], v224 offset:10240\n\t" \
  "v_mfma_f32_16x16x32_bf16 v[68:71], v[212:215], v[192:195], v[68:71]\n\t" \
  "s_add_u32 s48, s48, 128\n\t" \
  "s_xor_b32 s59, s59, 0x8000\n\t" \
  "ds_read_b64_tr_b16 v[172:173], v227 offset:16384\n\t" \
  "v_mfma_f32_16x16x32_bf16 v[84:87], v[212:215], v[196:199], v[84:87]\n\t" \
  "ds_read_b128 v[152:155], v224 offset:12288\n\t" \
  "v_mfma_f32_16x16x32_bf16 v[100:103], v[212:215], v[200:203], v[100:103]\n\t" \
  "ds_read_b64_tr_b16 v[174:175], v227 offset:17408\n\t" \
  "v_mfma_f32_16x16x32_bf16 v[116:119], v[212:215], v[204:207], v[116:119]\n\t" \
  "ds_read_b128 v[156:159], v224 offset:14336\n\t" \
  "v_mfma_f32_16x16x32_bf16 v[8:11], v[216:219], v[176:179], v[8:11]\n\t" \
  "v_mfma_f32_16x16x32_bf16 v[24:27], v[216:219], v[180:183], v[24:27]\n\t" \
  "v_mfma_f32_16x16x32_bf16 v[40:43], v[216:219], v[184:187], v[40:43]\n\t" \
  "v_mfma_f32_16x16x32_bf16 v[56:59], v[216:219], v[188:191], v[56:59]\n\t" \
  "v_mfma_f32_16x16x32_bf16 v[72:75], v[216:219], v[192:195], v[72:75]\n\t" \
  "v_mfma_f32_16x16x32_bf16 v[88:91], v[216:219], v[196:199], v[88:91]\n\t" \
  "v_mfma_f32_16x16x32_bf16 v[104:107], v[216:219], v[200:203], v[104:107]\n\t" \
  "v_mfma_f32_16x16x32_bf16 v[120:123], v[216:219], v[204:207], v[120:123]\n\t" \
  "v_mfma_f32_16x16x32_bf16 v[12:15], v[220:223], v[176:179], v[12:15]\n\t" \
  "v_xor_b32 v225, 0x8000, v225\n\t" \
  "v_mfma_f32_16x16x32_bf16 v[28:31], v[220:223], v[180:183], v[28:31]\n\t" \
  "v_add_u32 v240, s53, v228\n\t" \
  "v_mfma_f32_16x16x32_bf16 v[44:47], v[220:223], v[184:187], v[44:47]\n\t" \
  "v_add_u32 v241, s53, v229\n\t" \
  "v_mfma_f32_16x16x32_bf16 v[60:63], v[220:223], v[188:191], v[60:63]\n\t" \
  "v_mfma_f32_16x16x32_bf16 v[76:79], v[220:223], v[192:195], v[76:79]\n\t" \
  "v_mfma_f32_16x16x32_bf16 v[92:95], v[220:223], v[196:199], v[92:95]\n\t" \
  "v_mfma_f32_16x16x32_bf16 v[108:111], v[220:223], v[200:203], v[108:111]\n\t" \
  "v_mfma_f32_16x16x32_bf16 v[124:127], v[220:223], v[204:207], v[124:127]\n\t" \
  "s_waitcnt lgkmcnt(0)\n\t" \
  "v_mfma_f32_16x16x32_bf16 v[0:3], v[160:163], v[128:131], v[0:3]\n\t" \
  "s_add_u32 s53, s53, 0x8000\n\t" \
  "s_cmp_eq_u32 s53, 0x18000\n\t" \
  "s_cselect_b32 s53, 0, s53\n\t" \
  "ds_read_b64_tr_b16 v[208:209], v240 offset:8192\n\t" \
  "v_mfma_f32_16x16x32_bf16 v[16:19], v[160:163], v[132:135], v[16:19]\n\t" \
  "ds_read_b128 v[176:179], v225 offset:0\n\t" \
  "v_mfma_f32_16x16x32_bf16 v[32:35], v[160:163], v[136:139], v[32:35]\n\t" \
  "ds_read_b64_tr_b16 v[210:211], v240 offset:9216\n\t" \
  "v_mfma_f32_16x16x32_bf16 v[48:51], v[160:163], v[140:143], v[48:51]\n\t" \
  "ds_read_b128 v[180:183], v225 offset:2048\n\t" \
  "v_mfma_f32_16x16x32_bf16 v[64:67], v[160:163], v[144:147], v[64:67]\n\t" \
  "ds_read_b64_tr_b16 v[212:213], v241 offset:8192\n\t" \
  "v_mfma_f32_16x16x32_bf16 v[80:83], v[160:163], v[148:151], v[80:83]\n\t" \
  "ds_read_b128 v[184:187], v225 offset:4096\n\t" \
  "v_mfma_f32_16x16x32_bf16 v[96:99], v[160:163], v[152:155], v[96:99]\n\t" \
  "ds_read_b64_tr_b16 v[214:215], v241 offset:9216\n\t" \
  "v_mfma_f32_16x16x32_bf16 v[112:115], v[160:163], v[156:159], v[112:115]\n\t" \
  "ds_read_b128 v[188:191], v225 offset:6144\n\t" \
  "v_mfma_f32_16x16x32_bf16 v[4:7], v[164:167], v[128:131], v[4:7]\n\t" \
  "ds_read_b64_tr_b16 v[216:217], v240 offset:24576\n\t" \
  "v_mfma_f32_16x16x32_bf16 v[20:23], v[164:167], v[132:135], v[20:23]\n\t" \
  "ds_read_b128 v[192:195], v225 offset:8192\n\t" \
  "v_mfma_f32_16x16x32_bf16 v[36:39], v[164:167], v[136:139], v[36:39]\n\t" \
  "ds_read_b64_tr_b16 v[218:219], v240 offset:25600\n\t" \
  "v_mfma_f32_16x16x32_bf16 v[52:55], v[164:167], v[140:143], v[52:55]\n\t" \
  "ds_read_b128 v[196:199], v225 offset:10240\n\t" \
  "v_mfma_f32_16x16x32_bf16 v[68:71], v[164:167], v[144:147], v[68:71]\n\t" \
  "ds_read_b64_tr_b16 v[220:221], v241 offset:24576\n\t" \
  "v_mfma_f32_16x16x32_bf16 v[84:87], v[164:167], v[148:151], v[84:87]\n\t" \
  "ds_read_b128 v[200:203], v225 offset:12288\n\t" \
  "v_mfma_f32_16x16x32_bf16 v[100:103], v[164:167], v[152:155], v[100:103]\n\t" \
  "ds_read_b64_tr_b16 v[222:223], v241 offset:25600\n\t" \
  "v_mfma_f32_16x16x32_bf16 v[116:119], v[164:167], v[156:159], v[116:119]\n\t" \
  "ds_read_b128 v[204:207], v225 offset:14336\n\t" \
  "v_mfma_f32_16x16x32_bf16 v[8:11], v[168:171], v[128:131], v[8:11]\n\t" \
  "v_mfma_f32_16x16x32_bf16 v[24:27], v[168:171], v[132:135], v[24:27]\n\t" \
  "v_mfma_f32_16x16x32_bf16 v[40:43], v[168:171], v[136:139], v[40:43]\n\t" \
  "v_mfma_f32_16x16x32_bf16 v[56:59], v[168:171], v[140:143], v[56:59]\n\t" \
  "v_mfma_f32_16x16x32_bf16 v[72:75], v[168:171], v[144:147], v[72:75]\n\t" \
  "v_mfma_f32_16x16x32_bf16 v[88:91], v[168:171], v[148:151], v[88:91]\n\t" \
  "v_mfma_f32_16x16x32_bf16 v[104:107], v[168:171], v[152:155], v[104:107]\n\t" \
  "v_mfma_f32_16x16x32_bf16 v[120:123], v[168:171], v[156:159], v[120:123]\n\t" \
  "v_mfma_f32_16x16x32_bf16 v[12:15], v[172:175], v[128:131], v[12:15]\n\t" \
  "v_xor_b32 v224, 0x8000, v224\n\t" \
  "v_mfma_f32_16x16x32_bf16 v[28:31], v[172:175], v[132:135], v[28:31]\n\t" \
  "v_add_u32 v226, s53, v228\n\t" \
  "v_mfma_f32_16x16x32_bf16 v[44:47], v[172:175], v[136:139], v[44:47]\n\t" \
  "v_add_u32 v227, s53, v229\n\t" \
  "v_mfma_f32_16x16x32_bf16 v[60:63], v[172:175], v[140:143], v[60:63]\n\t" \
  "v_xor_b32 v225, 0x8000, v225\n\t" \
  "v_mfma_f32_16x16x32_bf16 v[76:79], v[172:175], v[144:147], v[76:79]\n\t" \
  "v_add_u32 v240, s53, v228\n\t" \
  "v_mfma_f32_16x16x32_bf16 v[92:95], v[172:175], v[148:151], v[92:95]\n\t" \
  "v_add_u32 v241, s53, v229\n\t" \
  "v_mfma_f32_16x16x32_bf16 v[108:111], v[172:175], v[152:155], v[108:111]\n\t" \
  "v_mfma_f32_16x16x32_bf16 v[124:127], v[172:175], v[156:159], v[124:127]\n\t" \
  "s_waitcnt vmcnt(0) lgkmcnt(0)\n\t" \
  "s_barrier\n\t" \
  ".p2align 3\n\t" \
  "s_mov_b32 s54, 4096\n\t" \
  "s_mov_b32 s48, 0xbfb8aa3b\n\t" \
  "s_mov_b32 s49, 0xbfb8aa3b\n\t" \
  "v_mfma_f32_16x16x32_bf16 v[0:3], v[208:211], v[176:179], v[0:3]\n\t" \
  "ds_read_b64_tr_b16 v[128:129], v226 offset:0\n\t" \
  "ds_read_b64_tr_b16 v[130:131], v226 offset:1024\n\t" \
  "v_mfma_f32_16x16x32_bf16 v[4:7], v[212:215], v[176:179], v[4:7]\n\t" \
  "ds_read_b64_tr_b16 v[132:133], v227 offset:0\n\t" \
  "ds_read_b64_tr_b16 v[134:135], v227 offset:1024\n\t" \
  "ds_read_b64_tr_b16 v[136:137], v226 offset:16384\n\t" \
  "v_mfma_f32_16x16x32_bf16 v[8:11], v[216:219], v[176:179], v[8:11]\n\t" \
  "ds_read_b64_tr_b16 v[138:139], v226 offset:17408\n\t" \
  "ds_read_b64_tr_b16 v[140:141], v227 offset:16384\n\t" \
  "v_mfma_f32_16x16x32_bf16 v[12:15], v[220:223], v[176:179], v[12:15]\n\t" \
  "ds_read_b64_tr_b16 v[142:143], v227 offset:17408\n\t" \
  "ds_read_b128 v[160:163], v224 offset:0\n\t" \
  "ds_read_b128 v[164:167], v225 offset:0\n\t" \
  "v_mfma_f32_16x16x32_bf16 v[16:19], v[208:211], v[180:183], v[16:19]\n\t" \
  "ds_read_b64_tr_b16 v[144:145], v240 offset:8192\n\t" \
  "ds_read_b64_tr_b16 v[146:147], v240 offset:9216\n\t" \
  "v_mfma_f32_16x16x32_bf16 v[20:23], v[212:215], v[180:183], v[20:23]\n\t" \
  "ds_read_b64_tr_b16 v[148:149], v241 offset:8192\n\t" \
  "ds_read_b64_tr_b16 v[150:151], v241 offset:9216\n\t" \
  "ds_read_b64_tr_b16 v[152:153], v240 offset:24576\n\t" \
  "v_mfma_f32_16x16x32_bf16 v[24:27], v[216:219], v[180:183], v[24:27]\n\t" \
  "ds_read_b64_tr_b16 v[154:155], v240 offset:25600\n\t" \
  "ds_read_b64_tr_b16 v[156:157], v241 offset:24576\n\t" \
  "v_mfma_f32_16x16x32_bf16 v[28:31], v[220:223], v[180:183], v[28:31]\n\t" \
  "ds_read_b64_tr_b16 v[158:159], v241 offset:25600\n\t" \
  "ds_read_b128 v[168:171], v224 offset:2048\n\t" \
  "ds_read_b128 v[172:175], v225 offset:2048\n\t" \
  "s_waitcnt lgkmcnt(11)\n\t" \
  "s_nop 0\n\t" \
  "v_mfma_f32_16x16x32_bf16 v[0:3], v[128:131], v[160:163], v[0:3]\n\t" \
  "v_mfma_f32_16x16x32_bf16 v[4:7], v[132:135], v[160:163], v[4:7]\n\t" \
  "buffer_load_dword v242, v230, s[72:75], 0 offen offset:4 sc1\n\t" \
  "v_mfma_f32_16x16x32_bf16 v[8:11], v[136:139], v[160:163], v[8:11]\n\t" \
  "v_mfma_f32_16x16x32_bf16 v[12:15], v[140:143], v[160:163], v[12:15]\n\t" \
  "s_mov_b32 s81, 0\n\t" \
  "s_waitcnt lgkmcnt(8)\n\t" \
  "v_mfma_f32_16x16x32_bf16 v[0:3], v[144:147], v[164:167], v[0:3]\n\t" \
  "s_add_u32 m0, s80, 32768\n\t" \
  "s_waitcnt lgkmcnt(6)\n\t" \
  "v_mfma_f32_16x16x32_bf16 v[4:7], v[148:151], v[164:167], v[4:7]\n\t" \
  "s_waitcnt lgkmcnt(4)\n\t" \
  "v_mfma_f32_16x16x32_bf16 v[8:11], v[152:155], v[164:167], v[8:11]\n\t" \
  "buffer_load_dwordx4 v244, s[76:79], s81 offen offset:0 lds\n\t" \
  "s_waitcnt lgkmcnt(2)\n\t" \
  "v_mfma_f32_16x16x32_bf16 v[12:15], v[156:159], v[164:167], v[12:15]\n\t" \
  "buffer_load_dwordx4 v245, s[76:79], s81 offen offset:1024 lds\n\t" \
  "s_waitcnt lgkmcnt(0)\n\t" \
  "s_barrier\n\t" \
  "s_nop 0\n\t" \
  "ds_read_b128 v[160:163], v224 offset:4096\n\t" \
  "ds_read_b128 v[164:167], v225 offset:4096\n\t" \
  "v_mfma_f32_16x16x32_bf16 v[32:35], v[208:211], v[184:187], v[32:35]\n\t" \
  "v_mfma_f32_16x16x32_bf16 v[36:39], v[212:215], v[184:187], v[36:39]\n\t" \
  "v_mfma_f32_16x16x32_bf16 v[40:43], v[216:219], v[184:187], v[40:43]\n\t" \
  "v_mul_f32 v226, s48, v0\n\t" \
  "v_mul_f32 v227, s48, v1\n\t" \
  "v_mul_f32 v228, s48, v2\n\t" \
  "v_mul_f32 v229, s48, v3\n\t" \
  "v_exp_f32_e64 v226, v226\n\t" \
  "v_mfma_f32_16x16x32_bf16 v[44:47], v[220:223], v[184:187], v[44:47]\n\t" \
  "v_exp_f32 v227, v227\n\t" \
  "v_exp_f32 v228, v228\n\t" \
  "v_exp_f32 v229, v229\n\t" \
  "v_mul_f32 v0, v0, v8\n\t" \
  "v_mul_f32 v1, v1, v9\n\t" \
  "v_mul_f32 v2, v2, v10\n\t" \
  "v_mfma_f32_16x16x32_bf16 v[16:19], v[128:131], v[168:171], v[16:19]\n\t" \
  "v_mul_f32 v3, v3, v11\n\t" \
  "v_add_f32 v226, 1.0, v226\n\t" \
  "v_add_f32 v227, 1.0, v227\n\t" \
  "v_add_f32 v228, 1.0, v228\n\t" \
  "v_add_f32 v229, 1.0, v229\n\t" \
  "v_rcp_f32 v226, v226\n\t" \
  "v_mfma_f32_16x16x32_bf16 v[20:23], v[132:135], v[168:171], v[20:23]\n\t" \
  "s_mov_b32 s81, 131072\n\t" \
  "s_add_u32 m0, s80, 49152\n\t" \
  "s_nop 0\n\t" \
  "buffer_load_dwordx4 v244, s[76:79], s81 offen offset:0 lds\n\t" \
  "buffer_load_dwordx4 v245, s[76:79], s81 offen offset:1024 lds\n\t" \
  "v_rcp_f32 v227, v227\n\t" \
  "v_mfma_f32_16x16x32_bf16 v[24:27], v[136:139], v[168:171], v[24:27]\n\t" \
  "v_rcp_f32 v228, v228\n\t" \
  "v_rcp_f32 v229, v229\n\t" \
  "v_mul_f32 v0, v0, v226\n\t" \
  "v_mul_f32 v1, v1, v227\n\t" \
  "v_mul_f32 v2, v2, v228\n\t" \
  "v_mul_f32 v3, v3, v229\n\t" \
  "v_mfma_f32_16x16x32_bf16 v[28:31], v[140:143], v[168:171], v[28:31]\n\t" \
  "v_cvt_pk_bf16_f32 v232, v0, v1\n\t" \
  "v_cvt_pk_bf16_f32 v233, v2, v3\n\t" \
  "v_mul_f32 v240, s48, v4\n\t" \
  "v_mul_f32 v241, s48, v5\n\t" \
  "v_mul_f32 v248, s48, v6\n\t" \
  "v_mul_f32 v249, s48, v7\n\t" \
  "v_mfma_f32_16x16x32_bf16 v[16:19], v[144:147], v[172:175], v[16:19]\n\t" \
  "v_exp_f32 v240, v240\n\t" \
  "v_exp_f32 v241, v241\n\t" \
  "v_exp_f32 v248, v248\n\t" \
  "v_exp_f32 v249, v249\n\t" \
  "v_mul_f32_e64 v4, v4, v12\n\t" \
  "v_mfma_f32_16x16x32_bf16 v[20:23], v[148:151], v[172:175], v[20:23]\n\t" \
  "v_mul_f32 v5, v5, v13\n\t" \
  "v_mul_f32 v6, v6, v14\n\t" \
  "v_mul_f32 v7, v7, v15\n\t" \
  "v_add_f32 v240, 1.0, v240\n\t" \
  "v_add_f32 v241, 1.0, v241\n\t" \
  "v_add_f32 v248, 1.0, v248\n\t" \
  "v_mfma_f32_16x16x32_bf16 v[24:27], v[152:155], v[172:175], v[24:27]\n\t" \
  "v_add_f32 v249, 1.0, v249\n\t" \
  "v_rcp_f32 v240, v240\n\t" \
  "v_rcp_f32 v241, v241\n\t" \
  "v_rcp_f32 v248, v248\n\t" \
  "v_rcp_f32 v249, v249\n\t" \
  "v_mul_f32 v4, v4, v240\n\t" \
  "v_mfma_f32_16x16x32_bf16 v[28:31], v[156:159], v[172:175], v[28:31]\n\t" \
  "v_mul_f32 v5, v5, v241\n\t" \
  "v_mul_f32 v6, v6, v248\n\t" \
  "v_mul_f32_e64 v7, v7, v249\n\t" \
  "v_cvt_pk_bf16_f32 v234, v4, v5\n\t" \
  "v_cvt_pk_bf16_f32 v235, v6, v7\n\t" \
  "buffer_store_dwordx4 v[232:235], v243, s[64:67], 0 offen offset:0 sc1\n\t" \
  "ds_read_b128 v[168:171], v224 offset:6144\n\t" \
  "ds_read_b128 v[172:175], v225 offset:6144\n\t" \
  "v_mfma_f32_16x16x32_bf16 v[48:51], v[208:211], v[188:191], v[48:51]\n\t" \
  "v_mfma_f32_16x16x32_bf16 v[52:55], v[212:215], v[188:191], v[52:55]\n\t" \
  "v_mfma_f32_16x16x32_bf16 v[56:59], v[216:219], v[188:191], v[56:59]\n\t" \
  "v_mul_f32 v226, s48, v16\n\t" \
  "v_mul_f32 v227, s48, v17\n\t" \
  "v_mul_f32 v228, s48, v18\n\t" \
  "v_mul_f32 v229, s48, v19\n\t" \
  "v_exp_f32_e64 v226, v226\n\t" \
  "v_mfma_f32_16x16x32_bf16 v[60:63], v[220:223], v[188:191], v[60:63]\n\t" \
  "v_exp_f32 v227, v227\n\t" \
  "v_exp_f32 v228, v228\n\t" \
  "v_exp_f32 v229, v229\n\t" \
  "v_mul_f32 v16, v16, v24\n\t" \
  "v_mul_f32 v17, v17, v25\n\t" \
  "v_mul_f32 v18, v18, v26\n\t" \
  "s_waitcnt lgkmcnt(3)\n\t" \
  "v_mfma_f32_16x16x32_bf16 v[32:35], v[128:131], v[160:163], v[32:35]\n\t" \
  "v_mul_f32 v19, v19, v27\n\t" \
  "v_add_f32 v226, 1.0, v226\n\t" \
  "v_add_f32 v227, 1.0, v227\n\t" \
  "v_add_f32 v228, 1.0, v228\n\t" \
  "v_add_f32 v229, 1.0, v229\n\t" \
  "v_rcp_f32_e64 v226, v226\n\t" \
  "v_mfma_f32_16x16x32_bf16 v[36:39], v[132:135], v[160:163], v[36:39]\n\t" \
  "s_mov_b32 s81, 262144\n\t" \
  "s_add_u32 m0, s80, 65536\n\t" \
  "s_nop 0\n\t" \
  "buffer_load_dwordx4 v244, s[76:79], s81 offen offset:0 lds\n\t" \
  "buffer_load_dwordx4 v245, s[76:79], s81 offen offset:1024 lds\n\t" \
  "v_rcp_f32 v227, v227\n\t" \
  "v_mfma_f32_16x16x32_bf16 v[40:43], v[136:139], v[160:163], v[40:43]\n\t" \
  "v_rcp_f32 v228, v228\n\t" \
  "v_rcp_f32 v229, v229\n\t" \
  "v_mul_f32 v16, v16, v226\n\t" \
  "v_mul_f32 v17, v17, v227\n\t" \
  "v_mul_f32 v18, v18, v228\n\t" \
  "v_mul_f32 v19, v19, v229\n\t" \
  "v_mfma_f32_16x16x32_bf16 v[44:47], v[140:143], v[160:163], v[44:47]\n\t" \
  "v_cvt_pk_bf16_f32 v236, v16, v17\n\t" \
  "v_cvt_pk_bf16_f32 v237, v18, v19\n\t" \
  "v_mul_f32 v240, s48, v20\n\t" \
  "v_mul_f32 v241, s48, v21\n\t" \
  "v_mul_f32 v248, s48, v22\n\t" \
  "v_mul_f32 v249, s48, v23\n\t" \
  "s_waitcnt lgkmcnt(2)\n\t" \
  "v_mfma_f32_16x16x32_bf16 v[32:35], v[144:147], v[164:167], v[32:35]\n\t" \
  "v_exp_f32 v240, v240\n\t" \
  "v_exp_f32 v241, v241\n\t" \
  "v_exp_f32 v248, v248\n\t" \
  "v_exp_f32 v249, v249\n\t" \
  "v_mul_f32 v20, v20, v28\n\t" \
  "v_mfma_f32_16x16x32_bf16 v[36:39], v[148:151], v[164:167], v[36:39]\n\t" \
  "v_mul_f32 v21, v21, v29\n\t" \
  "v_mul_f32 v22, v22, v30\n\t" \
  "v_mul_f32 v23, v23, v31\n\t" \
  "v_add_f32 v240, 1.0, v240\n\t" \
  "v_add_f32 v241, 1.0, v241\n\t" \
  "v_add_f32 v248, 1.0, v248\n\t" \
  "v_mfma_f32_16x16x32_bf16 v[40:43], v[152:155], v[164:167], v[40:43]\n\t" \
  "v_add_f32 v249, 1.0, v249\n\t" \
  "v_rcp_f32 v240, v240\n\t" \
  "v_rcp_f32 v241, v241\n\t" \
  "v_rcp_f32 v248, v248\n\t" \
  "v_rcp_f32 v249, v249\n\t" \
  "v_mul_f32 v20, v20, v240\n\t" \
  "v_mfma_f32_16x16x32_bf16 v[44:47], v[156:159], v[164:167], v[44:47]\n\t" \
  "v_mul_f32 v21, v21, v241\n\t" \
  "v_mul_f32 v22, v22, v248\n\t" \
  "v_mul_f32_e64 v23, v23, v249\n\t" \
  "v_cvt_pk_bf16_f32 v238, v20, v21\n\t" \
  "v_cvt_pk_bf16_f32 v239, v22, v23\n\t" \
  "buffer_store_dwordx4 v[236:239], v243, s[64:67], 0 offen offset:1024 sc1\n\t" \
  "ds_read_b128 v[160:163], v224 offset:8192\n\t" \
  "ds_read_b128 v[164:167], v225 offset:8192\n\t" \
  "v_mfma_f32_16x16x32_bf16 v[64:67], v[208:211], v[192:195], v[64:67]\n\t" \
  "v_mfma_f32_16x16x32_bf16 v[68:71], v[212:215], v[192:195], v[68:71]\n\t" \
  "v_mfma_f32_16x16x32_bf16 v[72:75], v[216:219], v[192:195], v[72:75]\n\t" \
  "v_mul_f32 v226, s48, v32\n\t" \
  "v_mul_f32 v227, s48, v33\n\t" \
  "v_mul_f32 v228, s48, v34\n\t" \
  "v_mul_f32 v229, s48, v35\n\t" \
  "v_exp_f32_e64 v226, v226\n\t" \
  "v_mfma_f32_16x16x32_bf16 v[76:79], v[220:223], v[192:195], v[76:79]\n\t" \
  "v_exp_f32 v227, v227\n\t" \
  "v_exp_f32 v228, v228\n\t" \
  "v_exp_f32 v229, v229\n\t" \
  "v_mul_f32 v32, v32, v40\n\t" \
  "v_mul_f32 v33, v33, v41\n\t" \
  "v_mul_f32 v34, v34, v42\n\t" \
  "s_waitcnt lgkmcnt(3)\n\t" \
  "v_mfma_f32_16x16x32_bf16 v[48:51], v[128:131], v[168:171], v[48:51]\n\t" \
  "v_mul_f32 v35, v35, v43\n\t" \
  "v_add_f32 v226, 1.0, v226\n\t" \
  "v_add_f32 v227, 1.0, v227\n\t" \
  "v_add_f32 v228, 1.0, v228\n\t" \
  "v_add_f32 v229, 1.0, v229\n\t" \
  "v_rcp_f32_e64 v226, v226\n\t" \
  "v_mfma_f32_16x16x32_bf16 v[52:55], v[132:135], v[168:171], v[52:55]\n\t" \
  "s_mov_b32 s81, 393216\n\t" \
  "s_add_u32 m0, s80, 81920\n\t" \
  "s_nop 0\n\t" \
  "buffer_load_dwordx4 v244, s[76:79], s81 offen offset:0 lds\n\t" \
  "buffer_load_dwordx4 v245, s[76:79], s81 offen offset:1024 lds\n\t" \
  "v_rcp_f32 v227, v227\n\t" \
  "v_mfma_f32_16x16x32_bf16 v[56:59], v[136:139], v[168:171], v[56:59]\n\t" \
  "v_rcp_f32 v228, v228\n\t" \
  "v_rcp_f32 v229, v229\n\t" \
  "v_mul_f32 v32, v32, v226\n\t" \
  "v_mul_f32 v33, v33, v227\n\t" \
  "v_mul_f32 v34, v34, v228\n\t" \
  "v_mul_f32 v35, v35, v229\n\t" \
  "v_mfma_f32_16x16x32_bf16 v[60:63], v[140:143], v[168:171], v[60:63]\n\t" \
  "v_cvt_pk_bf16_f32 v232, v32, v33\n\t" \
  "v_cvt_pk_bf16_f32 v233, v34, v35\n\t" \
  "v_mul_f32 v240, s48, v36\n\t" \
  "v_mul_f32 v241, s48, v37\n\t" \
  "v_mul_f32 v248, s48, v38\n\t" \
  "v_mul_f32 v249, s48, v39\n\t" \
  "s_waitcnt lgkmcnt(2)\n\t" \
  "v_mfma_f32_16x16x32_bf16 v[48:51], v[144:147], v[172:175], v[48:51]\n\t" \
  "v_exp_f32 v240, v240\n\t" \
  "v_exp_f32 v241, v241\n\t" \
  "v_exp_f32 v248, v248\n\t" \
  "v_exp_f32 v249, v249\n\t" \
  "v_mul_f32 v36, v36, v44\n\t" \
  "v_mfma_f32_16x16x32_bf16 v[52:55], v[148:151], v[172:175], v[52:55]\n\t" \
  "v_mul_f32 v37, v37, v45\n\t" \
  "v_mul_f32 v38, v38, v46\n\t" \
  "v_mul_f32 v39, v39, v47\n\t" \
  "v_add_f32 v240, 1.0, v240\n\t" \
  "v_add_f32 v241, 1.0, v241\n\t" \
  "v_add_f32 v248, 1.0, v248\n\t" \
  "v_mfma_f32_16x16x32_bf16 v[56:59], v[152:155], v[172:175], v[56:59]\n\t" \
  "v_add_f32 v249, 1.0, v249\n\t" \
  "v_rcp_f32 v240, v240\n\t" \
  "v_rcp_f32 v241, v241\n\t" \
  "v_rcp_f32 v248, v248\n\t" \
  "v_rcp_f32 v249, v249\n\t" \
  "v_mul_f32 v36, v36, v240\n\t" \
  "v_mfma_f32_16x16x32_bf16 v[60:63], v[156:159], v[172:175], v[60:63]\n\t" \
  "v_mul_f32 v37, v37, v241\n\t" \
  "v_mul_f32 v38, v38, v248\n\t" \
  "v_mul_f32_e64 v39, v39, v249\n\t" \
  "v_cvt_pk_bf16_f32 v234, v36, v37\n\t" \
  "v_cvt_pk_bf16_f32 v235, v38, v39\n\t" \
  "buffer_store_dwordx4 v[232:235], v243, s[64:67], 0 offen offset:2048 sc1\n\t" \
  "ds_read_b128 v[168:171], v224 offset:10240\n\t" \
  "ds_read_b128 v[172:175], v225 offset:10240\n\t" \
  "v_mfma_f32_16x16x32_bf16 v[80:83], v[208:211], v[196:199], v[80:83]\n\t" \
  "v_mfma_f32_16x16x32_bf16 v[84:87], v[212:215], v[196:199], v[84:87]\n\t" \
  "v_mfma_f32_16x16x32_bf16 v[88:91], v[216:219], v[196:199], v[88:91]\n\t" \
  "v_mul_f32 v226, s48, v48\n\t" \
  "v_mul_f32 v227, s48, v49\n\t" \
  "v_mul_f32 v228, s48, v50\n\t" \
  "v_mul_f32 v229, s48, v51\n\t" \
  "v_exp_f32_e64 v226, v226\n\t" \
  "v_mfma_f32_16x16x32_bf16 v[92:95], v[220:223], v[196:199], v[92:95]\n\t" \
  "v_exp_f32 v227, v227\n\t" \
  "v_exp_f32 v228, v228\n\t" \
  "v_exp_f32 v229, v229\n\t" \
  "v_mul_f32 v48, v48, v56\n\t" \
  "v_mul_f32 v49, v49, v57\n\t" \
  "v_mul_f32 v50, v50, v58\n\t" \
  "s_waitcnt lgkmcnt(3)\n\t" \
  "v_mfma_f32_16x16x32_bf16 v[64:67], v[128:131], v[160:163], v[64:67]\n\t" \
  "v_mul_f32 v51, v51, v59\n\t" \
  "v_add_f32 v226, 1.0, v226\n\t" \
  "v_add_f32 v227, 1.0, v227\n\t" \
  "v_add_f32 v228, 1.0, v228\n\t" \
  "v_add_f32 v229, 1.0, v229\n\t" \
  "v_rcp_f32_e64 v226, v226\n\t" \
  "v_mfma_f32_16x16x32_bf16 v[68:71], v[132:135], v[160:163], v[68:71]\n\t" \
  "s_mov_b32 s81, 524288\n\t" \
  "s_add_u32 m0, s80, 0\n\t" \
  "s_nop 0\n\t" \
  "buffer_load_dwordx4 v244, s[76:79], s81 offen offset:0 lds\n\t" \
  "buffer_load_dwordx4 v245, s[76:79], s81 offen offset:1024 lds\n\t" \
  "v_rcp_f32_e64 v227, v227\n\t" \
  "v_mfma_f32_16x16x32_bf16 v[72:75], v[136:139], v[160:163], v[72:75]\n\t" \
  "v_rcp_f32 v228, v228\n\t" \
  "v_rcp_f32 v229, v229\n\t" \
  "v_mul_f32 v48, v48, v226\n\t" \
  "v_mul_f32 v49, v49, v227\n\t" \
  "v_mul_f32 v50, v50, v228\n\t" \
  "v_mul_f32 v51, v51, v229\n\t" \
  "v_mfma_f32_16x16x32_bf16 v[76:79], v[140:143], v[160:163], v[76:79]\n\t" \
  "v_cvt_pk_bf16_f32 v236, v48, v49\n\t" \
  "v_cvt_pk_bf16_f32 v237, v50, v51\n\t" \
  "v_mul_f32 v240, s48, v52\n\t" \
  "v_mul_f32 v241, s48, v53\n\t" \
  "v_mul_f32 v248, s48, v54\n\t" \
  "v_mul_f32 v249, s48, v55\n\t" \
  "s_waitcnt lgkmcnt(2)\n\t" \
  "v_mfma_f32_16x16x32_bf16 v[64:67], v[144:147], v[164:167], v[64:67]\n\t" \
  "v_exp_f32 v240, v240\n\t" \
  "v_exp_f32 v241, v241\n\t" \
  "v_exp_f32 v248, v248\n\t" \
  "v_exp_f32 v249, v249\n\t" \
  "v_mul_f32 v52, v52, v60\n\t" \
  "v_mfma_f32_16x16x32_bf16 v[68:71], v[148:151], v[164:167], v[68:71]\n\t" \
  "v_mul_f32 v53, v53, v61\n\t" \
  "v_mul_f32 v54, v54, v62\n\t" \
  "v_mul_f32 v55, v55, v63\n\t" \
  "v_add_f32 v240, 1.0, v240\n\t" \
  "v_add_f32 v241, 1.0, v241\n\t" \
  "v_add_f32 v248, 1.0, v248\n\t" \
  "v_mfma_f32_16x16x32_bf16 v[72:75], v[152:155], v[164:167], v[72:75]\n\t" \
  "v_add_f32 v249, 1.0, v249\n\t" \
  "v_rcp_f32 v240, v240\n\t" \
  "v_rcp_f32 v241, v241\n\t" \
  "v_rcp_f32 v248, v248\n\t" \
  "v_rcp_f32 v249, v249\n\t" \
  "v_mul_f32 v52, v52, v240\n\t" \
  "v_mfma_f32_16x16x32_bf16 v[76:79], v[156:159], v[164:167], v[76:79]\n\t" \
  "v_mul_f32 v53, v53, v241\n\t" \
  "v_mul_f32 v54, v54, v248\n\t" \
  "v_mul_f32_e64 v55, v55, v249\n\t" \
  "v_cvt_pk_bf16_f32 v238, v52, v53\n\t" \
  "v_cvt_pk_bf16_f32 v239, v54, v55\n\t" \
  "buffer_store_dwordx4 v[236:239], v243, s[64:67], 0 offen offset:3072 sc1\n\t" \
  "ds_read_b128 v[160:163], v224 offset:12288\n\t" \
  "ds_read_b128 v[164:167], v225 offset:12288\n\t" \
  "v_mfma_f32_16x16x32_bf16 v[96:99], v[208:211], v[200:203], v[96:99]\n\t" \
  "v_mfma_f32_16x16x32_bf16 v[100:103], v[212:215], v[200:203], v[100:103]\n\t" \
  "v_mfma_f32_16x16x32_bf16 v[104:107], v[216:219], v[200:203], v[104:107]\n\t" \
  "v_mul_f32 v226, s48, v64\n\t" \
  "v_mul_f32 v227, s48, v65\n\t" \
  "v_mul_f32 v228, s48, v66\n\t" \
  "v_mul_f32 v229, s48, v67\n\t" \
  "v_exp_f32_e64 v226, v226\n\t" \
  "v_mfma_f32_16x16x32_bf16 v[108:111], v[220:223], v[200:203], v[108:111]\n\t" \
  "v_exp_f32 v227, v227\n\t" \
  "v_exp_f32 v228, v228\n\t" \
  "v_exp_f32 v229, v229\n\t" \
  "v_mul_f32 v64, v64, v72\n\t" \
  "v_mul_f32 v65, v65, v73\n\t" \
  "v_mul_f32 v66, v66, v74\n\t" \
  "s_waitcnt lgkmcnt(3)\n\t" \
  "v_mfma_f32_16x16x32_bf16 v[80:83], v[128:131], v[168:171], v[80:83]\n\t" \
  "v_mul_f32 v67, v67, v75\n\t" \
  "v_add_f32 v226, 1.0, v226\n\t" \
  "v_add_f32 v227, 1.0, v227\n\t" \
  "v_add_f32 v228, 1.0, v228\n\t" \
  "v_add_f32 v229, 1.0, v229\n\t" \
  "v_rcp_f32_e64 v226, v226\n\t" \
  "v_mfma_f32_16x16x32_bf16 v[84:87], v[132:135], v[168:171], v[84:87]\n\t" \
  "s_mov_b32 s81, 655360\n\t" \
  "s_add_u32 m0, s80, 16384\n\t" \
  "s_nop 0\n\t" \
  "buffer_load_dwordx4 v244, s[76:79], s81 offen offset:0 lds\n\t" \
  "buffer_load_dwordx4 v245, s[76:79], s81 offen offset:1024 lds\n\t" \
  "v_rcp_f32 v227, v227\n\t" \
  "v_mfma_f32_16x16x32_bf16 v[88:91], v[136:139], v[168:171], v[88:91]\n\t" \
  "v_rcp_f32 v228, v228\n\t" \
  "v_rcp_f32 v229, v229\n\t" \
  "v_mul_f32 v64, v64, v226\n\t" \
  "v_mul_f32 v65, v65, v227\n\t" \
  "v_mul_f32 v66, v66, v228\n\t" \
  "v_mul_f32 v67, v67, v229\n\t" \
  "v_mfma_f32_16x16x32_bf16 v[92:95], v[140:143], v[168:171], v[92:95]\n\t" \
  "v_cvt_pk_bf16_f32 v232, v64, v65\n\t" \
  "v_cvt_pk_bf16_f32 v233, v66, v67\n\t" \
  "v_mul_f32 v240, s48, v68\n\t" \
  "v_mul_f32 v241, s48, v69\n\t" \
  "v_mul_f32 v248, s48, v70\n\t" \
  "v_mul_f32 v249, s48, v71\n\t" \
  "s_waitcnt lgkmcnt(2)\n\t" \
  "v_mfma_f32_16x16x32_bf16 v[80:83], v[144:147], v[172:175], v[80:83]\n\t" \
  "v_exp_f32 v240, v240\n\t" \
  "v_exp_f32 v241, v241\n\t" \
  "v_exp_f32 v248, v248\n\t" \
  "v_exp_f32 v249, v249\n\t" \
  "v_mul_f32 v68, v68, v76\n\t" \
  "v_mfma_f32_16x16x32_bf16 v[84:87], v[148:151], v[172:175], v[84:87]\n\t" \
  "v_mul_f32 v69, v69, v77\n\t" \
  "v_mul_f32 v70, v70, v78\n\t" \
  "v_mul_f32 v71, v71, v79\n\t" \
  "v_add_f32 v240, 1.0, v240\n\t" \
  "v_add_f32 v241, 1.0, v241\n\t" \
  "v_add_f32 v248, 1.0, v248\n\t" \
  "v_mfma_f32_16x16x32_bf16 v[88:91], v[152:155], v[172:175], v[88:91]\n\t" \
  "v_add_f32 v249, 1.0, v249\n\t" \
  "v_rcp_f32 v240, v240\n\t" \
  "v_rcp_f32 v241, v241\n\t" \
  "v_rcp_f32 v248, v248\n\t" \
  "v_rcp_f32 v249, v249\n\t" \
  "v_mul_f32 v68, v68, v240\n\t" \
  "v_mfma_f32_16x16x32_bf16 v[92:95], v[156:159], v[172:175], v[92:95]\n\t" \
  "v_mul_f32 v69, v69, v241\n\t" \
  "v_mul_f32 v70, v70, v248\n\t" \
  "v_mul_f32_e64 v71, v71, v249\n\t" \
  "v_cvt_pk_bf16_f32 v234, v68, v69\n\t" \
  "v_cvt_pk_bf16_f32 v235, v70, v71\n\t" \
  "buffer_store_dwordx4 v[232:235], v243, s[64:67], s54 offen offset:0 sc1\n\t" \
  "ds_read_b128 v[168:171], v224 offset:14336\n\t" \
  "ds_read_b128 v[172:175], v225 offset:14336\n\t" \
  "v_mfma_f32_16x16x32_bf16 v[112:115], v[208:211], v[204:207], v[112:115]\n\t" \
  "v_mfma_f32_16x16x32_bf16 v[116:119], v[212:215], v[204:207], v[116:119]\n\t" \
  "v_mfma_f32_16x16x32_bf16 v[120:123], v[216:219], v[204:207], v[120:123]\n\t" \
  "buffer_load_dwordx4 v[0:3], v243, s[60:63], 0 offen offset:0\n\t" \
  "buffer_load_dwordx4 v[4:7], v243, s[60:63], 0 offen offset:1024\n\t" \
  "buffer_load_dwordx4 v[8:11], v243, s[68:71], 0 offen offset:0\n\t" \
  "buffer_load_dwordx4 v[12:15], v243, s[68:71], 0 offen offset:1024\n\t" \
  "v_mul_f32_e64 v226, s48, v80\n\t" \
  "v_mfma_f32_16x16x32_bf16 v[124:127], v[220:223], v[204:207], v[124:127]\n\t" \
  "v_mul_f32 v227, s48, v81\n\t" \
  "v_mul_f32 v228, s48, v82\n\t" \
  "v_mul_f32 v229, s48, v83\n\t" \
  "v_exp_f32 v226, v226\n\t" \
  "v_exp_f32 v227, v227\n\t" \
  "v_exp_f32 v228, v228\n\t" \
  "s_waitcnt lgkmcnt(3)\n\t" \
  "v_mfma_f32_16x16x32_bf16 v[96:99], v[128:131], v[160:163], v[96:99]\n\t" \
  "v_exp_f32 v229, v229\n\t" \
  "v_mul_f32 v80, v80, v88\n\t" \
  "v_mul_f32 v81, v81, v89\n\t" \
  "v_mul_f32 v82, v82, v90\n\t" \
  "v_mul_f32 v83, v83, v91\n\t" \
  "v_add_f32_e64 v226, 1.0, v226\n\t" \
  "v_mfma_f32_16x16x32_bf16 v[100:103], v[132:135], v[160:163], v[100:103]\n\t" \
  "v_add_f32 v227, 1.0, v227\n\t" \
  "v_add_f32 v228, 1.0, v228\n\t" \
  "v_add_f32 v229, 1.0, v229\n\t" \
  "v_rcp_f32 v226, v226\n\t" \
  "v_rcp_f32_e64 v227, v227\n\t" \
  "v_mfma_f32_16x16x32_bf16 v[104:107], v[136:139], v[160:163], v[104:107]\n\t" \
  "v_rcp_f32 v228, v228\n\t" \
  "v_rcp_f32 v229, v229\n\t" \
  "v_mul_f32 v80, v80, v226\n\t" \
  "v_mul_f32 v81, v81, v227\n\t" \
  "v_mul_f32 v82, v82, v228\n\t" \
  "v_mul_f32 v83, v83, v229\n\t" \
  "v_mfma_f32_16x16x32_bf16 v[108:111], v[140:143], v[160:163], v[108:111]\n\t" \
  "v_cvt_pk_bf16_f32 v236, v80, v81\n\t" \
  "v_cvt_pk_bf16_f32 v237, v82, v83\n\t" \
  "v_mul_f32 v240, s48, v84\n\t" \
  "v_mul_f32 v241, s48, v85\n\t" \
  "v_mul_f32 v248, s48, v86\n\t" \
  "v_mul_f32 v249, s48, v87\n\t" \
  "s_waitcnt lgkmcnt(2)\n\t" \
  "v_mfma_f32_16x16x32_bf16 v[96:99], v[144:147], v[164:167], v[96:99]\n\t" \
  "v_exp_f32 v240, v240\n\t" \
  "v_exp_f32 v241, v241\n\t" \
  "v_exp_f32 v248, v248\n\t" \
  "v_exp_f32 v249, v249\n\t" \
  "v_mul_f32 v84, v84, v92\n\t" \
  "v_mfma_f32_16x16x32_bf16 v[100:103], v[148:151], v[164:167], v[100:103]\n\t" \
  "v_mul_f32 v85, v85, v93\n\t" \
  "v_mul_f32 v86, v86, v94\n\t" \
  "v_mul_f32 v87, v87, v95\n\t" \
  "v_add_f32 v240, 1.0, v240\n\t" \
  "v_add_f32 v241, 1.0, v241\n\t" \
  "v_add_f32 v248, 1.0, v248\n\t" \
  "v_mfma_f32_16x16x32_bf16 v[104:107], v[152:155], v[164:167], v[104:107]\n\t" \
  "v_add_f32 v249, 1.0, v249\n\t" \
  "v_rcp_f32 v240, v240\n\t" \
  "v_rcp_f32 v241, v241\n\t" \
  "v_rcp_f32 v248, v248\n\t" \
  "v_rcp_f32 v249, v249\n\t" \
  "v_mul_f32 v84, v84, v240\n\t" \
  "v_mfma_f32_16x16x32_bf16 v[108:111], v[156:159], v[164:167], v[108:111]\n\t" \
  "v_mul_f32 v85, v85, v241\n\t" \
  "v_mul_f32 v86, v86, v248\n\t" \
  "v_mul_f32_e64 v87, v87, v249\n\t" \
  "v_cvt_pk_bf16_f32 v238, v84, v85\n\t" \
  "v_cvt_pk_bf16_f32 v239, v86, v87\n\t" \
  "buffer_store_dwordx4 v[236:239], v243, s[64:67], s54 offen offset:1024 sc1\n\t" \
  "s_waitcnt lgkmcnt(1)\n\t" \
  "v_mfma_f32_16x16x32_bf16 v[112:115], v[128:131], v[168:171], v[112:115]\n\t" \
  "v_mfma_f32_16x16x32_bf16 v[116:119], v[132:135], v[168:171], v[116:119]\n\t" \
  "v_mfma_f32_16x16x32_bf16 v[120:123], v[136:139], v[168:171], v[120:123]\n\t" \
  "v_mul_f32 v226, s48, v96\n\t" \
  "v_mul_f32 v227, s48, v97\n\t" \
  "v_mul_f32 v228, s48, v98\n\t" \
  "v_mul_f32 v229, s48, v99\n\t" \
  "v_exp_f32 v226, v226\n\t" \
  "v_exp_f32 v227, v227\n\t" \
  "v_exp_f32 v228, v228\n\t" \
  "v_exp_f32_e64 v229, v229\n\t" \
  "v_mfma_f32_16x16x32_bf16 v[124:127], v[140:143], v[168:171], v[124:127]\n\t" \
  "v_mul_f32 v96, v96, v104\n\t" \
  "v_mul_f32 v97, v97, v105\n\t" \
  "v_mul_f32 v98, v98, v106\n\t" \
  "v_mul_f32 v99, v99, v107\n\t" \
  "v_add_f32 v226, 1.0, v226\n\t" \
  "v_add_f32 v227, 1.0, v227\n\t" \
  "v_add_f32 v228, 1.0, v228\n\t" \
  "v_add_f32 v229, 1.0, v229\n\t" \
  "v_rcp_f32 v226, v226\n\t" \
  "s_waitcnt lgkmcnt(0)\n\t" \
  "v_mfma_f32_16x16x32_bf16 v[112:115], v[144:147], v[172:175], v[112:115]\n\t" \
  "v_rcp_f32 v227, v227\n\t" \
  "v_rcp_f32 v228, v228\n\t" \
  "v_rcp_f32 v229, v229\n\t" \
  "v_mul_f32 v96, v96, v226\n\t" \
  "v_mul_f32 v97, v97, v227\n\t" \
  "v_mul_f32 v98, v98, v228\n\t" \
  "v_mul_f32_e64 v99, v99, v229\n\t" \
  "v_cvt_pk_bf16_f32 v232, v96, v97\n\t" \
  "v_cvt_pk_bf16_f32 v233, v98, v99\n\t" \
  "v_mfma_f32_16x16x32_bf16 v[116:119], v[148:151], v[172:175], v[116:119]\n\t" \
  "v_mul_f32 v240, s48, v100\n\t" \
  "v_mul_f32 v241, s48, v101\n\t" \
  "v_mul_f32 v248, s48, v102\n\t" \
  "v_mul_f32 v249, s48, v103\n\t" \
  "v_exp_f32 v240, v240\n\t" \
  "v_exp_f32 v241, v241\n\t" \
  "v_exp_f32 v248, v248\n\t" \
  "v_exp_f32 v249, v249\n\t" \
  "v_mul_f32_e64 v100, v100, v108\n\t" \
  "v_mfma_f32_16x16x32_bf16 v[120:123], v[152:155], v[172:175], v[120:123]\n\t" \
  "v_mul_f32 v101, v101, v109\n\t" \
  "v_mul_f32 v102, v102, v110\n\t" \
  "v_mul_f32 v103, v103, v111\n\t" \
  "v_add_f32 v240, 1.0, v240\n\t" \
  "v_add_f32 v241, 1.0, v241\n\t" \
  "v_add_f32 v248, 1.0, v248\n\t" \
  "v_add_f32 v249, 1.0, v249\n\t" \
  "v_rcp_f32 v240, v240\n\t" \
  "v_rcp_f32_e64 v241, v241\n\t" \
  "v_mfma_f32_16x16x32_bf16 v[124:127], v[156:159], v[172:175], v[124:127]\n\t" \
  "v_rcp_f32 v248, v248\n\t" \
  "v_rcp_f32 v249, v249\n\t" \
  "v_mul_f32 v100, v100, v240\n\t" \
  "v_mul_f32 v101, v101, v241\n\t" \
  "v_mul_f32 v102, v102, v248\n\t" \
  "v_mul_f32 v103, v103, v249\n\t" \
  "v_cvt_pk_bf16_f32 v234, v100, v101\n\t" \
  "v_cvt_pk_bf16_f32 v235, v102, v103\n\t" \
  "buffer_store_dwordx4 v[232:235], v243, s[64:67], s54 offen offset:2048 sc1\n\t" \
  "v_pk_mul_f32 v[226:227], v[112:113], s[48:49]\n\t" \
  "v_pk_mul_f32 v[228:229], v[114:115], s[48:49]\n\t" \
  "v_exp_f32 v226, v226\n\t" \
  "v_exp_f32 v227, v227\n\t" \
  "v_exp_f32 v228, v228\n\t" \
  "v_exp_f32 v229, v229\n\t" \
  "v_pk_mul_f32 v[112:113], v[112:113], v[120:121]\n\t" \
  "v_pk_mul_f32 v[114:115], v[114:115], v[122:123]\n\t" \
  "v_pk_add_f32 v[226:227], v[226:227], 1.0 op_sel_hi:[1,0]\n\t" \
  "v_pk_add_f32 v[228:229], v[228:229], 1.0 op_sel_hi:[1,0]\n\t" \
  "v_rcp_f32 v226, v226\n\t" \
  "v_rcp_f32 v227, v227\n\t" \
  "v_rcp_f32 v228, v228\n\t" \
  "v_rcp_f32 v229, v229\n\t" \
  "v_pk_mul_f32 v[112:113], v[112:113], v[226:227]\n\t" \
  "v_pk_mul_f32 v[114:115], v[114:115], v[228:229]\n\t" \
  "v_cvt_pk_bf16_f32 v236, v112, v113\n\t" \
  "v_cvt_pk_bf16_f32 v237, v114, v115\n\t" \
  "v_pk_mul_f32 v[240:241], v[116:117], s[48:49]\n\t" \
  "v_pk_mul_f32 v[248:249], v[118:119], s[48:49]\n\t" \
  "v_exp_f32 v240, v240\n\t" \
  "v_exp_f32 v241, v241\n\t" \
  "v_exp_f32 v248, v248\n\t" \
  "v_exp_f32 v249, v249\n\t" \
  "v_pk_mul_f32 v[116:117], v[116:117], v[124:125]\n\t" \
  "v_pk_mul_f32 v[118:119], v[118:119], v[126:127]\n\t" \
  "v_pk_add_f32 v[240:241], v[240:241], 1.0 op_sel_hi:[1,0]\n\t" \
  "v_pk_add_f32 v[248:249], v[248:249], 1.0 op_sel_hi:[1,0]\n\t" \
  "v_rcp_f32 v240, v240\n\t" \
  "v_rcp_f32 v241, v241\n\t" \
  "v_rcp_f32 v248, v248\n\t" \
  "v_rcp_f32 v249, v249\n\t" \
  "v_pk_mul_f32 v[116:117], v[116:117], v[240:241]\n\t" \
  "v_pk_mul_f32 v[118:119], v[118:119], v[248:249]\n\t" \
  "v_cvt_pk_bf16_f32 v238, v116, v117\n\t" \
  "v_cvt_pk_bf16_f32 v239, v118, v119\n\t" \
  "buffer_store_dwordx4 v[236:239], v243, s[64:67], s54 offen offset:3072 sc1\n\t" \
  "s_waitcnt vmcnt(3)\n\t" \
  "s_mov_b32 s55, 0x3f803f80\n\t" \
  "v_mov_b32 v30, 0\n\t" \
  "v_cmp_eq_u32 vcc, 1008, v243\n\t" \
  "v_mov_b32 v31, 0x40000000\n\t" \
  "v_mov_b32 v29, 0\n\t" \
  "v_lshlrev_b32 v16, 16, v0\n\t" \
  "v_and_b32 v17, 0xffff0000, v0\n\t" \
  "v_lshlrev_b32_e64 v18, 16, v8\n\t" \
  "v_and_b32 v19, 0xffff0000, v8\n\t" \
  "v_mul_f32 v16, v16, v18\n\t" \
  "v_mul_f32 v17, v17, v19\n\t" \
  "v_cvt_pk_bf16_f32 v16, v16, v17\n\t" \
  "v_dot2_f32_bf16 v30, v16, s55, v30\n\t" \
  "v_lshlrev_b32_e64 v20, 16, v1\n\t" \
  "v_and_b32 v21, 0xffff0000, v1\n\t" \
  "v_lshlrev_b32_e64 v22, 16, v9\n\t" \
  "v_and_b32 v23, 0xffff0000, v9\n\t" \
  "v_mul_f32 v20, v20, v22\n\t" \
  "v_mul_f32 v21, v21, v23\n\t" \
  "v_cvt_pk_bf16_f32 v20, v20, v21\n\t" \
  "v_dot2_f32_bf16 v30, v20, s55, v30\n\t" \
  "v_lshlrev_b32_e64 v16, 16, v2\n\t" \
  "v_and_b32 v17, 0xffff0000, v2\n\t" \
  "v_lshlrev_b32_e64 v18, 16, v10\n\t" \
  "v_and_b32 v19, 0xffff0000, v10\n\t" \
  "v_mul_f32 v16, v16, v18\n\t" \
  "v_mul_f32 v17, v17, v19\n\t" \
  "v_cvt_pk_bf16_f32 v16, v16, v17\n\t" \
  "v_dot2_f32_bf16 v30, v16, s55, v30\n\t" \
  "v_lshlrev_b32_e64 v20, 16, v3\n\t" \
  "v_and_b32 v21, 0xffff0000, v3\n\t" \
  "v_lshlrev_b32_e64 v22, 16, v11\n\t" \
  "v_and_b32 v23, 0xffff0000, v11\n\t" \
  "v_mul_f32 v20, v20, v22\n\t" \
  "v_mul_f32 v21, v21, v23\n\t" \
  "v_cvt_pk_bf16_f32 v20, v20, v21\n\t" \
  "v_dot2_f32_bf16 v30, v20, s55, v30\n\t" \
  "v_lshlrev_b32_e64 v16, 16, v4\n\t" \
  "v_and_b32 v17, 0xffff0000, v4\n\t" \
  "v_lshlrev_b32_e64 v18, 16, v12\n\t" \
  "v_and_b32 v19, 0xffff0000, v12\n\t" \
  "v_mul_f32 v16, v16, v18\n\t" \
  "v_mul_f32 v17, v17, v19\n\t" \
  "v_cvt_pk_bf16_f32 v16, v16, v17\n\t" \
  "v_dot2_f32_bf16 v30, v16, s55, v30\n\t" \
  "v_lshlrev_b32_e64 v20, 16, v5\n\t" \
  "v_and_b32 v21, 0xffff0000, v5\n\t" \
  "v_lshlrev_b32_e64 v22, 16, v13\n\t" \
  "v_and_b32 v23, 0xffff0000, v13\n\t" \
  "v_mul_f32 v20, v20, v22\n\t" \
  "v_mul_f32 v21, v21, v23\n\t" \
  "v_cvt_pk_bf16_f32 v20, v20, v21\n\t" \
  "v_dot2_f32_bf16 v30, v20, s55, v30\n\t" \
  "v_lshlrev_b32_e64 v16, 16, v6\n\t" \
  "v_and_b32 v17, 0xffff0000, v6\n\t" \
  "v_lshlrev_b32_e64 v18, 16, v14\n\t" \
  "v_and_b32 v19, 0xffff0000, v14\n\t" \
  "v_mul_f32 v16, v16, v18\n\t" \
  "v_mul_f32 v17, v17, v19\n\t" \
  "v_cvt_pk_bf16_f32 v16, v16, v17\n\t" \
  "v_dot2_f32_bf16 v30, v16, s55, v30\n\t" \
  "v_lshlrev_b32_e64 v20, 16, v7\n\t" \
  "v_and_b32 v21, 0xffff0000, v7\n\t" \
  "v_lshlrev_b32_e64 v22, 16, v15\n\t" \
  "v_and_b32 v23, 0xffff0000, v15\n\t" \
  "v_mul_f32 v20, v20, v22\n\t" \
  "v_mul_f32 v21, v21, v23\n\t" \
  "v_cvt_pk_bf16_f32 v20, v20, v21\n\t" \
  "v_dot2_f32_bf16 v30, v20, s55, v30\n\t" \
  "v_cndmask_b32 v31, v31, v29, vcc\n\t" \
  "buffer_store_dword v29, v31, s[72:75], s57 offen sc1\n\t" \
  "s_nop 1\n\t" \
  "v_add_f32_dpp v30, v30, v30 row_shr:1 row_mask:0xf bank_mask:0xf bound_ctrl:1\n\t" \
  "s_nop 1\n\t" \
  "v_add_f32_dpp v30, v30, v30 row_shr:2 row_mask:0xf bank_mask:0xf bound_ctrl:1\n\t" \
  "s_nop 1\n\t" \
  "v_add_f32_dpp v30, v30, v30 row_shr:4 row_mask:0xf bank_mask:0xf bound_ctrl:1\n\t" \
  "s_nop 1\n\t" \
  "v_add_f32_dpp v30, v30, v30 row_shr:8 row_mask:0xf bank_mask:0xf bound_ctrl:1\n\t" \
  "s_nop 1\n\t" \
  "v_add_f32_dpp v30, v30, v30 row_bcast:15 row_mask:0xa bank_mask:0xf\n\t" \
  "s_nop 1\n\t" \
  "v_add_f32_dpp v30, v30, v30 row_bcast:31 row_mask:0xc bank_mask:0xf\n\t" \
  "v_cvt_pk_bf16_f32 v30, v30, v30\n\t" \
  "v_and_b32 v30, 0xffff0000, v30\n\t" \
  "v_mul_f32 v30, 0xbfb8aa3b, v30\n\t" \
  "v_exp_f32 v30, v30\n\t" \
  "s_nop 0\n\t" \
  "v_add_f32 v30, 1.0, v30\n\t" \
  "v_rcp_f32 v30, v30\n\t" \
  "s_nop 0\n\t" \
  "v_cvt_pk_bf16_f32 v30, v30, v30\n\t" \
  "v_and_b32 v30, 0xffff0000, v30\n\t" \
  "buffer_store_dword v30, v31, s[72:75], s56 offen sc1\n\t" \
  "s_waitcnt vmcnt(0)\n\t" \
  "s_barrier\n\t"

#define G2_HEAD \
  "s_bfe_u32 s83, s93, 0x4000c\n\t" \
  "s_mov_b32 s82, s80\n\t" \
  "s_and_b32 s80, s84, 1\n\t" \
  "s_cmp_lg_u32 s85, 0\n\t" \
  "s_cbranch_scc1 70f\n\t" \
  "s_mov_b64 exec, 1\n\t" \
  "v_readfirstlane_b32 s53, v231\n\t" \
  "s_lshl_b32 s48, s83, 8\n\t" \
  "s_add_u32 s48, s48, 4096\n\t" \
  "v_cmp_ne_u32 vcc, s53, v242\n\t" \
  "v_mov_b32 v16, 1\n\t" \
  "s_mov_b32 s52, 0\n\t" \
  "s_nop 3\n\t" \
  "s_cbranch_vccz 73f\n\t" \
  "72:\n\t" \
  "s_sleep 1\n\t" \
  "buffer_load_dword v242, v230, s[72:75], 0 offen offset:4 sc1\n\t" \
  "s_add_u32 s52, s52, 1\n\t" \
  "s_waitcnt vmcnt(0)\n\t" \
  "v_cmp_ne_u32 vcc, s53, v242\n\t" \
  "s_cmp_ge_u32 s52, 0x8000\n\t" \
  "s_nop 2\n\t" \
  "s_cbranch_scc1 73f\n\t" \
  "s_nop 1\n\t" \
  "s_cbranch_vccnz 72b\n\t" \
  "73:\n\t" \
  "buffer_atomic_add v16, off, s[72:75], s48\n\t" \
  "s_mov_b64 exec, -1\n\t" \
  "s_nop 0\n\t" \
  "70:\n\t" \
  "s_mul_i32 s48, s84, 25600\n\t" \
  "s_mul_i32 s52, s85, 3200\n\t" \
  "s_add_u32 s52, s52, 29360128\n\t" \
  "s_add_u32 s48, s48, s52\n\t" \
  "s_add_u32 s56, s72, s48\n\t" \
  "s_addc_u32 s57, s73, 0\n\t" \
  "s_and_b32 s57, s57, 0xffff\n\t" \
  "s_mov_b32 s58, 3200\n\t" \
  "s_mov_b32 s59, 0x20000\n\t" \
  "s_xor_b32 s48, s84, 1\n\t" \
  "s_mul_i32 s48, s48, 25600\n\t" \
  "s_add_u32 s48, s48, s52\n\t" \
  "s_add_u32 s60, s72, s48\n\t" \
  "s_addc_u32 s61, s73, 0\n\t" \
  "s_and_b32 s61, s61, 0xffff\n\t" \
  "s_mov_b32 s62, 3200\n\t" \
  "s_mov_b32 s63, 0x20000\n\t" \
  "s_add_u32 s64, s72, 0\n\t" \
  "s_addc_u32 s65, s73, 0\n\t" \
  "s_and_b32 s65, s65, 0xffff\n\t" \
  "s_mov_b32 s66, 13631488\n\t" \
  "s_mov_b32 s67, 0x20000\n\t" \
  "s_add_u32 s68, s86, 0\n\t" \
  "s_addc_u32 s69, s87, 0\n\t" \
  "s_and_b32 s69, s69, 0xffff\n\t" \
  "s_mov_b32 s70, 4194304\n\t" \
  "s_mov_b32 s71, 0x20000\n\t" \
  "s_add_u32 s44, s72, 4096\n\t" \
  "s_addc_u32 s45, s73, 0\n\t" \
  "s_and_b32 s45, s45, 0xffff\n\t" \
  "s_mov_b32 s46, 8192\n\t" \
  "s_mov_b32 s47, 0x20000\n\t" \
  "s_and_b32 s54, s93, 0xff\n\t" \
  "s_mul_i32 s48, s80, 7168\n\t" \
  "s_add_u32 s48, s48, s54\n\t" \
  "s_lshl_b32 s48, s48, 10\n\t" \
  "s_add_u32 s48, s48, 13628416\n\t" \
  "s_add_u32 s40, s72, s48\n\t" \
  "s_addc_u32 s41, s73, 0\n\t" \
  "s_and_b32 s41, s41, 0xffff\n\t" \
  "s_mov_b32 s42, 7222272\n\t" \
  "s_mov_b32 s43, 0x20000\n\t" \
  "s_mov_b32 s72, s85\n\t" \
  "s_lshl_b32 s48, s85, 12\n\t" \
  "s_sub_u32 s73, s50, s48\n\t" \
  "s_mov_b32 s74, s54\n\t" \
  "s_bfe_u32 s75, s93, 0x40008\n\t" \
  "s_lshr_b32 s48, s93, 15\n\t" \
  "s_and_b32 s48, s48, 2\n\t" \
  "s_or_b32 s80, s80, s48\n\t" \
  "s_lshr_b32 s83, s93, 20\n\t" \
  "s_and_b32 s88, s72, 3\n\t" \
  "s_lshr_b32 s89, s72, 2\n\t" \
  "s_and_b32 s90, s72, 1\n\t" \
  "s_lshr_b32 s91, s80, 1\n\t" \
  "s_and_b32 s54, s80, 1\n\t" \
  "s_and_b32 s49, s89, s91\n\t" \
  "s_add_u32 s49, s49, s89\n\t" \
  "s_xor_b32 s55, s89, 1\n\t" \
  "s_or_b32 s55, s55, s91\n\t" \
  "s_lshl_b32 s48, s88, 12\n\t" \
  "s_add_u32 s50, s48, s73\n\t" \
  "s_add_u32 s53, s50, 65536\n\t" \
  "s_add_u32 s50, s50, 4096\n\t" \
  "s_lshl_b32 s48, s88, 10\n\t" \
  "s_add_u32 s51, s48, s73\n\t" \
  "s_add_u32 s51, s51, 20480\n\t" \
  "s_lshl_b32 s48, s88, 11\n\t" \
  "s_add_u32 s48, s48, 3072\n\t" \
  "v_add_u32 v236, s48, v243\n\t" \
  "v_add_u32 v237, 130048, v236\n\t" \
  "v_add_u32 v238, -1024, v236\n\t" \
  "v_add_u32 v239, 129024, v236\n\t" \
  "s_lshr_b32 s48, s88, 1\n\t" \
  "s_lshl_b32 s48, s48, 10\n\t" \
  "s_lshl_b32 s52, s90, 17\n\t" \
  "s_add_u32 s48, s48, s52\n\t" \
  "s_add_u32 s48, s48, 11264\n\t" \
  "v_add_u32 v226, s48, v243\n\t" \
  "s_lshl_b32 s48, s88, 15\n\t" \
  "s_lshl_b32 s52, s72, 14\n\t" \
  "s_sub_u32 s48, s48, s52\n\t" \
  "v_add_u32 v248, s48, v244\n\t" \
  "v_add_u32 v249, s48, v245\n\t" \
  "v_add_u32 v250, 14336, v248\n\t" \
  "v_add_u32 v251, 14336, v249\n\t" \
  "s_lshl_b32 s48, s90, 10\n\t" \
  "s_lshl_b32 s52, s89, 13\n\t" \
  "s_add_u32 s48, s48, s52\n\t" \
  "s_add_u32 s48, s48, s73\n\t" \
  "s_add_u32 s48, s48, 4096\n\t" \
  "v_add_u32 v227, s48, v243\n\t" \
  "s_lshl_b32 s48, s90, 13\n\t" \
  "s_add_u32 s48, s48, s73\n\t" \
  "s_add_u32 s48, s48, 65536\n\t" \
  "v_lshrrev_b32 v232, 6, v243\n\t" \
  "v_lshrrev_b32 v233, 1, v243\n\t" \
  "v_lshl_add_u32 v232, v232, 8, s48\n\t" \
  "v_and_b32 v234, 24, v233\n\t" \
  "v_and_b32 v233, 0xe0, v233\n\t" \
  "s_lshl_b32 s48, s88, 6\n\t" \
  "v_add_u32 v232, v232, v234\n\t" \
  "v_xor_b32 v233, s48, v233\n\t" \
  "s_movk_i32 s52, 0x60\n\t" \
  "v_add_u32 v228, v233, v232\n\t" \
  "v_xad_u32 v229, v233, 32, v232\n\t" \
  "v_xad_u32 v230, v233, 64, v232\n\t" \
  "v_xad_u32 v231, v233, s52, v232\n\t" \
  "v_add_u32 v232, 49152, v228\n\t" \
  "v_add_u32 v233, 49152, v229\n\t" \
  "v_add_u32 v234, 49152, v230\n\t" \
  "v_add_u32 v235, 49152, v231\n\t" \
  "s_cmp_lg_u32 s72, 0\n\t" \
  "s_cbranch_scc1 74f\n\t" \
  "s_lshr_b32 s48, s74, 4\n\t" \
  "s_lshl_b32 s48, s48, 1\n\t" \
  "s_or_b32 s48, s48, s54\n\t" \
  "s_lshl_b32 s48, s48, 8\n\t" \
  "v_mov_b32 v16, s48\n\t" \
  "s_add_u32 s48, s74, 8\n\t" \
  "s_add_u32 s48, s48, s91\n\t" \
  "s_lshr_b32 s48, s48, 4\n\t" \
  "s_lshl_b32 s48, s48, 1\n\t" \
  "s_or_b32 s48, s48, s54\n\t" \
  "s_lshl_b32 s48, s48, 8\n\t" \
  "v_writelane_b32 v16, s48, 1\n\t" \
  "buffer_load_dword v17, v16, s[44:47], 0 offen sc1\n\t" \
  "s_waitcnt vmcnt(0)\n\t" \
  "v_cmp_gt_u32 vcc, 14, v17\n\t" \
  "s_mov_b32 s52, 0\n\t" \
  "s_nop 3\n\t" \
  "s_cbranch_vccz 77f\n\t" \
  "76:\n\t" \
  "s_sleep 1\n\t" \
  "buffer_load_dword v17, v16, s[44:47], 0 offen sc1\n\t" \
  "s_add_u32 s52, s52, 1\n\t" \
  "s_waitcnt vmcnt(0)\n\t" \
  "v_cmp_gt_u32 vcc, 14, v17\n\t" \
  "s_cmp_ge_u32 s52, 0x8000\n\t" \
  "s_nop 2\n\t" \
  "s_cbranch_scc1 77f\n\t" \
  "s_nop 1\n\t" \
  "s_cbranch_vccnz 76b\n\t" \
  "77:\n\t" \
  "74:\n\t" \
  "s_barrier\n\t"
#define G2_LOOP \
  "s_cmp_lt_u32 s88, 2\n\t" \
  "s_cselect_b32 s52, 1, s91\n\t" \
  "s_sub_u32 vcc_lo, 0, s52\n\t" \
  "s_sub_u32 vcc_hi, 0, s52\n\t" \
  "s_mov_b32 s48, 0\n\t" \
  "s_mov_b32 s81, 786432\n\t" \
  "s_cmp_eq_u32 s49, 1\n\t" \
  "s_cbranch_scc1 41f\n\t" \
  "s_cmp_eq_u32 s49, 2\n\t" \
  "s_cbranch_scc1 42f\n\t" \
  "s_add_u32 m0, s50, 40960\n\t" \
  "s_nop 0\n\t" \
  "buffer_load_dwordx4 v236, s[40:43], s48 offen offset:0 sc1 lds\n\t" \
  "buffer_load_dwordx4 v237, s[40:43], s48 offen offset:1024 sc1 lds\n\t" \
  "buffer_load_dwordx4 v238, s[40:43], s48 offen offset:2048 sc1 lds\n\t" \
  "buffer_load_dwordx4 v239, s[40:43], s48 offen offset:3072 sc1 lds\n\t" \
  "s_add_u32 m0, s51, 40960\n\t" \
  "s_nop 0\n\t" \
  "s_cbranch_vccz 11f\n\t" \
  "buffer_load_dwordx4 v226, s[40:43], s48 offen sc1 lds\n\t" \
  "11:\n\t" \
  "s_add_u32 s48, s48, 262144\n\t" \
  "s_add_u32 m0, s50, 0\n\t" \
  "s_nop 0\n\t" \
  "buffer_load_dwordx4 v236, s[40:43], s48 offen offset:0 sc1 lds\n\t" \
  "buffer_load_dwordx4 v237, s[40:43], s48 offen offset:1024 sc1 lds\n\t" \
  "buffer_load_dwordx4 v238, s[40:43], s48 offen offset:2048 sc1 lds\n\t" \
  "buffer_load_dwordx4 v239, s[40:43], s48 offen offset:3072 sc1 lds\n\t" \
  "s_add_u32 m0, s51, 0\n\t" \
  "s_nop 0\n\t" \
  "s_cbranch_vccz 11f\n\t" \
  "buffer_load_dwordx4 v226, s[40:43], s48 offen sc1 lds\n\t" \
  "11:\n\t" \
  "s_add_u32 s48, s48, 262144\n\t" \
  "buffer_load_dword v240, off, s[76:79], 0 offset:3072\n\t" \
  "buffer_load_dword v240, off, s[76:79], 0 offset:3072\n\t" \
  "buffer_load_dword v240, off, s[76:79], 0 offset:3072\n\t" \
  "buffer_load_dword v240, off, s[76:79], 0 offset:3072\n\t" \
  "s_add_u32 m0, s50, 20480\n\t" \
  "s_nop 0\n\t" \
  "buffer_load_dwordx4 v236, s[40:43], s48 offen offset:0 sc1 lds\n\t" \
  "buffer_load_dwordx4 v237, s[40:43], s48 offen offset:1024 sc1 lds\n\t" \
  "buffer_load_dwordx4 v238, s[40:43], s48 offen offset:2048 sc1 lds\n\t" \
  "buffer_load_dwordx4 v239, s[40:43], s48 offen offset:3072 sc1 lds\n\t" \
  "s_add_u32 m0, s51, 20480\n\t" \
  "s_nop 0\n\t" \
  "s_cbranch_vccz 11f\n\t" \
  "buffer_load_dwordx4 v226, s[40:43], s48 offen sc1 lds\n\t" \
  "11:\n\t" \
  "s_add_u32 s48, s48, 262144\n\t" \
  "buffer_load_dword v240, off, s[76:79], 0 offset:3072\n\t" \
  "buffer_load_dword v240, off, s[76:79], 0 offset:3072\n\t" \
  "buffer_load_dword v240, off, s[76:79], 0 offset:3072\n\t" \
  "buffer_load_dword v240, off, s[76:79], 0 offset:3072\n\t" \
  "v_mov_b32 v0, 0\n\t" \
  "v_mov_b32 v1, 0\n\t" \
  "v_mov_b32 v2, 0\n\t" \
  "v_mov_b32 v3, 0\n\t" \
  "v_mov_b32 v4, 0\n\t" \
  "v_mov_b32 v5, 0\n\t" \
  "v_mov_b32 v6, 0\n\t" \
  "v_mov_b32 v7, 0\n\t" \
  "v_mov_b32 v8, 0\n\t" \
  "v_mov_b32 v9, 0\n\t" \
  "v_mov_b32 v10, 0\n\t" \
  "v_mov_b32 v11, 0\n\t" \
  "v_mov_b32 v12, 0\n\t" \
  "v_mov_b32 v13, 0\n\t" \
  "v_mov_b32 v14, 0\n\t" \
  "v_mov_b32 v15, 0\n\t" \
  "v_mov_b32 v16, 0\n\t" \
  "v_mov_b32 v17, 0\n\t" \
  "v_mov_b32 v18, 0\n\t" \
  "v_mov_b32 v19, 0\n\t" \
  "v_mov_b32 v20, 0\n\t" \
  "v_mov_b32 v21, 0\n\t" \
  "v_mov_b32 v22, 0\n\t" \
  "v_mov_b32 v23, 0\n\t" \
  "v_mov_b32 v24, 0\n\t" \
  "v_mov_b32 v25, 0\n\t" \
  "v_mov_b32 v26, 0\n\t" \
  "v_mov_b32 v27, 0\n\t" \
  "v_mov_b32 v28, 0\n\t" \
  "v_mov_b32 v29, 0\n\t" \
  "v_mov_b32 v30, 0\n\t" \
  "v_mov_b32 v31, 0\n\t" \
  "v_mov_b32 v32, 0\n\t" \
  "v_mov_b32 v33, 0\n\t" \
  "v_mov_b32 v34, 0\n\t" \
  "v_mov_b32 v35, 0\n\t" \
  "v_mov_b32 v36, 0\n\t" \
  "v_mov_b32 v37, 0\n\t" \
  "v_mov_b32 v38, 0\n\t" \
  "v_mov_b32 v39, 0\n\t" \
  "v_mov_b32 v40, 0\n\t" \
  "v_mov_b32 v41, 0\n\t" \
  "v_mov_b32 v42, 0\n\t" \
  "v_mov_b32 v43, 0\n\t" \
  "v_mov_b32 v44, 0\n\t" \
  "v_mov_b32 v45, 0\n\t" \
  "v_mov_b32 v46, 0\n\t" \
  "v_mov_b32 v47, 0\n\t" \
  "v_mov_b32 v48, 0\n\t" \
  "v_mov_b32 v49, 0\n\t" \
  "v_mov_b32 v50, 0\n\t" \
  "v_mov_b32 v51, 0\n\t" \
  "v_mov_b32 v52, 0\n\t" \
  "v_mov_b32 v53, 0\n\t" \
  "v_mov_b32 v54, 0\n\t" \
  "v_mov_b32 v55, 0\n\t" \
  "v_mov_b32 v56, 0\n\t" \
  "v_mov_b32 v57, 0\n\t" \
  "v_mov_b32 v58, 0\n\t" \
  "v_mov_b32 v59, 0\n\t" \
  "v_mov_b32 v60, 0\n\t" \
  "v_mov_b32 v61, 0\n\t" \
  "v_mov_b32 v62, 0\n\t" \
  "v_mov_b32 v63, 0\n\t" \
  "v_mov_b32 v64, 0\n\t" \
  "v_mov_b32 v65, 0\n\t" \
  "v_mov_b32 v66, 0\n\t" \
  "v_mov_b32 v67, 0\n\t" \
  "v_mov_b32 v68, 0\n\t" \
  "v_mov_b32 v69, 0\n\t" \
  "v_mov_b32 v70, 0\n\t" \
  "v_mov_b32 v71, 0\n\t" \
  "v_mov_b32 v72, 0\n\t" \
  "v_mov_b32 v73, 0\n\t" \
  "v_mov_b32 v74, 0\n\t" \
  "v_mov_b32 v75, 0\n\t" \
  "v_mov_b32 v76, 0\n\t" \
  "v_mov_b32 v77, 0\n\t" \
  "v_mov_b32 v78, 0\n\t" \
  "v_mov_b32 v79, 0\n\t" \
  "s_waitcnt vmcnt(16)\n\t" \
  "s_barrier\n\t" \
  "ds_read_b64_tr_b16 v[100:101], v228 offset:32768\n\t" \
  "ds_read_b64_tr_b16 v[102:103], v228 offset:36864\n\t" \
  "ds_read_b128 v[80:83], v227 offset:40960\n\t" \
  "ds_read_b128 v[84:87], v227 offset:43008\n\t" \
  "ds_read_b128 v[88:91], v227 offset:45056\n\t" \
  "ds_read_b128 v[92:95], v227 offset:47104\n\t" \
  "ds_read_b128 v[96:99], v227 offset:57344\n\t" \
  "ds_read_b64_tr_b16 v[104:105], v229 offset:32768\n\t" \
  "ds_read_b64_tr_b16 v[106:107], v229 offset:36864\n\t" \
  "ds_read_b64_tr_b16 v[108:109], v230 offset:32768\n\t" \
  "ds_read_b64_tr_b16 v[110:111], v230 offset:36864\n\t" \
  "ds_read_b64_tr_b16 v[112:113], v231 offset:32768\n\t" \
  "ds_read_b64_tr_b16 v[114:115], v231 offset:36864\n\t" \
  "s_mov_b32 s52, 4\n\t" \
  "s_branch 12f\n\t" \
  ".p2align 3\n\t" \
  "10:\n\t" \
  "s_waitcnt vmcnt(12) lgkmcnt(0)\n\t" \
  "s_barrier\n\t" \
  "v_mfma_f32_16x16x32_bf16 v[0:3], v[100:103], v[80:83], v[0:3]\n\t" \
  "ds_read_b64_tr_b16 v[136:137], v228 offset:16384\n\t" \
  "s_add_u32 m0, s50, 0\n\t" \
  "v_mfma_f32_16x16x32_bf16 v[16:19], v[100:103], v[84:87], v[16:19]\n\t" \
  "ds_read_b64_tr_b16 v[138:139], v228 offset:20480\n\t" \
  "buffer_load_dwordx4 v236, s[40:43], s48 offen offset:0 sc1 lds\n\t" \
  "v_mfma_f32_16x16x32_bf16 v[32:35], v[100:103], v[88:91], v[32:35]\n\t" \
  "ds_read_b128 v[116:119], v227 offset:20480\n\t" \
  "v_mfma_f32_16x16x32_bf16 v[48:51], v[100:103], v[92:95], v[48:51]\n\t" \
  "ds_read_b128 v[120:123], v227 offset:22528\n\t" \
  "buffer_load_dwordx4 v237, s[40:43], s48 offen offset:1024 sc1 lds\n\t" \
  "v_mfma_f32_16x16x32_bf16 v[64:67], v[100:103], v[96:99], v[64:67]\n\t" \
  "ds_read_b128 v[124:127], v227 offset:24576\n\t" \
  "v_mfma_f32_16x16x32_bf16 v[4:7], v[104:107], v[80:83], v[4:7]\n\t" \
  "ds_read_b128 v[128:131], v227 offset:26624\n\t" \
  "buffer_load_dwordx4 v238, s[40:43], s48 offen offset:2048 sc1 lds\n\t" \
  "v_mfma_f32_16x16x32_bf16 v[20:23], v[104:107], v[84:87], v[20:23]\n\t" \
  "ds_read_b128 v[132:135], v227 offset:36864\n\t" \
  "v_mfma_f32_16x16x32_bf16 v[36:39], v[104:107], v[88:91], v[36:39]\n\t" \
  "ds_read_b64_tr_b16 v[140:141], v229 offset:16384\n\t" \
  "buffer_load_dwordx4 v239, s[40:43], s48 offen offset:3072 sc1 lds\n\t" \
  "v_mfma_f32_16x16x32_bf16 v[52:55], v[104:107], v[92:95], v[52:55]\n\t" \
  "ds_read_b64_tr_b16 v[142:143], v229 offset:20480\n\t" \
  "s_add_u32 m0, s51, 0\n\t" \
  "v_mfma_f32_16x16x32_bf16 v[68:71], v[104:107], v[96:99], v[68:71]\n\t" \
  "ds_read_b64_tr_b16 v[144:145], v230 offset:16384\n\t" \
  "s_cbranch_vccz 11f\n\t" \
  "buffer_load_dwordx4 v226, s[40:43], s48 offen sc1 lds\n\t" \
  "11:\n\t" \
  "v_mfma_f32_16x16x32_bf16 v[8:11], v[108:111], v[80:83], v[8:11]\n\t" \
  "ds_read_b64_tr_b16 v[146:147], v230 offset:20480\n\t" \
  "s_add_u32 s48, s48, 262144\n\t" \
  "s_add_u32 m0, s53, 0\n\t" \
  "v_mfma_f32_16x16x32_bf16 v[24:27], v[108:111], v[84:87], v[24:27]\n\t" \
  "ds_read_b64_tr_b16 v[148:149], v231 offset:16384\n\t" \
  "buffer_load_dwordx4 v248, s[76:79], s81 offen offset:0 lds\n\t" \
  "v_mfma_f32_16x16x32_bf16 v[40:43], v[108:111], v[88:91], v[40:43]\n\t" \
  "ds_read_b64_tr_b16 v[150:151], v231 offset:20480\n\t" \
  "v_mfma_f32_16x16x32_bf16 v[56:59], v[108:111], v[92:95], v[56:59]\n\t" \
  "buffer_load_dwordx4 v249, s[76:79], s81 offen offset:1024 lds\n\t" \
  "v_mfma_f32_16x16x32_bf16 v[72:75], v[108:111], v[96:99], v[72:75]\n\t" \
  "v_mfma_f32_16x16x32_bf16 v[12:15], v[112:115], v[80:83], v[12:15]\n\t" \
  "buffer_load_dwordx4 v250, s[76:79], s81 offen offset:2048 lds\n\t" \
  "v_mfma_f32_16x16x32_bf16 v[28:31], v[112:115], v[84:87], v[28:31]\n\t" \
  "v_mfma_f32_16x16x32_bf16 v[44:47], v[112:115], v[88:91], v[44:47]\n\t" \
  "buffer_load_dwordx4 v251, s[76:79], s81 offen offset:3072 lds\n\t" \
  "v_mfma_f32_16x16x32_bf16 v[60:63], v[112:115], v[92:95], v[60:63]\n\t" \
  "s_add_u32 s81, s81, 131072\n\t" \
  "v_mfma_f32_16x16x32_bf16 v[76:79], v[112:115], v[96:99], v[76:79]\n\t" \
  "s_waitcnt vmcnt(12) lgkmcnt(0)\n\t" \
  "s_barrier\n\t" \
  "v_mfma_f32_16x16x32_bf16 v[0:3], v[136:139], v[116:119], v[0:3]\n\t" \
  "ds_read_b64_tr_b16 v[100:101], v228 offset:32768\n\t" \
  "s_add_u32 m0, s50, 20480\n\t" \
  "v_mfma_f32_16x16x32_bf16 v[16:19], v[136:139], v[120:123], v[16:19]\n\t" \
  "ds_read_b64_tr_b16 v[102:103], v228 offset:36864\n\t" \
  "buffer_load_dwordx4 v236, s[40:43], s48 offen offset:0 sc1 lds\n\t" \
  "v_mfma_f32_16x16x32_bf16 v[32:35], v[136:139], v[124:127], v[32:35]\n\t" \
  "ds_read_b128 v[80:83], v227 offset:40960\n\t" \
  "v_mfma_f32_16x16x32_bf16 v[48:51], v[136:139], v[128:131], v[48:51]\n\t" \
  "ds_read_b128 v[84:87], v227 offset:43008\n\t" \
  "buffer_load_dwordx4 v237, s[40:43], s48 offen offset:1024 sc1 lds\n\t" \
  "v_mfma_f32_16x16x32_bf16 v[64:67], v[136:139], v[132:135], v[64:67]\n\t" \
  "ds_read_b128 v[88:91], v227 offset:45056\n\t" \
  "v_mfma_f32_16x16x32_bf16 v[4:7], v[140:143], v[116:119], v[4:7]\n\t" \
  "ds_read_b128 v[92:95], v227 offset:47104\n\t" \
  "buffer_load_dwordx4 v238, s[40:43], s48 offen offset:2048 sc1 lds\n\t" \
  "v_mfma_f32_16x16x32_bf16 v[20:23], v[140:143], v[120:123], v[20:23]\n\t" \
  "ds_read_b128 v[96:99], v227 offset:57344\n\t" \
  "v_mfma_f32_16x16x32_bf16 v[36:39], v[140:143], v[124:127], v[36:39]\n\t" \
  "ds_read_b64_tr_b16 v[104:105], v229 offset:32768\n\t" \
  "buffer_load_dwordx4 v239, s[40:43], s48 offen offset:3072 sc1 lds\n\t" \
  "v_mfma_f32_16x16x32_bf16 v[52:55], v[140:143], v[128:131], v[52:55]\n\t" \
  "ds_read_b64_tr_b16 v[106:107], v229 offset:36864\n\t" \
  "s_add_u32 m0, s51, 20480\n\t" \
  "v_mfma_f32_16x16x32_bf16 v[68:71], v[140:143], v[132:135], v[68:71]\n\t" \
  "ds_read_b64_tr_b16 v[108:109], v230 offset:32768\n\t" \
  "s_cbranch_vccz 11f\n\t" \
  "buffer_load_dwordx4 v226, s[40:43], s48 offen sc1 lds\n\t" \
  "11:\n\t" \
  "v_mfma_f32_16x16x32_bf16 v[8:11], v[144:147], v[116:119], v[8:11]\n\t" \
  "ds_read_b64_tr_b16 v[110:111], v230 offset:36864\n\t" \
  "s_add_u32 s48, s48, 262144\n\t" \
  "s_add_u32 m0, s53, 16384\n\t" \
  "v_mfma_f32_16x16x32_bf16 v[24:27], v[144:147], v[120:123], v[24:27]\n\t" \
  "ds_read_b64_tr_b16 v[112:113], v231 offset:32768\n\t" \
  "buffer_load_dwordx4 v248, s[76:79], s81 offen offset:0 lds\n\t" \
  "v_mfma_f32_16x16x32_bf16 v[40:43], v[144:147], v[124:127], v[40:43]\n\t" \
  "ds_read_b64_tr_b16 v[114:115], v231 offset:36864\n\t" \
  "v_mfma_f32_16x16x32_bf16 v[56:59], v[144:147], v[128:131], v[56:59]\n\t" \
  "buffer_load_dwordx4 v249, s[76:79], s81 offen offset:1024 lds\n\t" \
  "v_mfma_f32_16x16x32_bf16 v[72:75], v[144:147], v[132:135], v[72:75]\n\t" \
  "v_mfma_f32_16x16x32_bf16 v[12:15], v[148:151], v[116:119], v[12:15]\n\t" \
  "buffer_load_dwordx4 v250, s[76:79], s81 offen offset:2048 lds\n\t" \
  "v_mfma_f32_16x16x32_bf16 v[28:31], v[148:151], v[120:123], v[28:31]\n\t" \
  "v_mfma_f32_16x16x32_bf16 v[44:47], v[148:151], v[124:127], v[44:47]\n\t" \
  "buffer_load_dwordx4 v251, s[76:79], s81 offen offset:3072 lds\n\t" \
  "v_mfma_f32_16x16x32_bf16 v[60:63], v[148:151], v[128:131], v[60:63]\n\t" \
  "s_add_u32 s81, s81, 131072\n\t" \
  "v_mfma_f32_16x16x32_bf16 v[76:79], v[148:151], v[132:135], v[76:79]\n\t" \
  "12:\n\t" \
  "s_waitcnt vmcnt(12) lgkmcnt(0)\n\t" \
  "s_barrier\n\t" \
  "v_mfma_f32_16x16x32_bf16 v[0:3], v[100:103], v[80:83], v[0:3]\n\t" \
  "ds_read_b64_tr_b16 v[136:137], v232 offset:0\n\t" \
  "s_add_u32 m0, s50, 40960\n\t" \
  "v_mfma_f32_16x16x32_bf16 v[16:19], v[100:103], v[84:87], v[16:19]\n\t" \
  "ds_read_b64_tr_b16 v[138:139], v232 offset:4096\n\t" \
  "buffer_load_dwordx4 v236, s[40:43], s48 offen offset:0 sc1 lds\n\t" \
  "v_mfma_f32_16x16x32_bf16 v[32:35], v[100:103], v[88:91], v[32:35]\n\t" \
  "ds_read_b128 v[116:119], v227 offset:0\n\t" \
  "v_mfma_f32_16x16x32_bf16 v[48:51], v[100:103], v[92:95], v[48:51]\n\t" \
  "ds_read_b128 v[120:123], v227 offset:2048\n\t" \
  "buffer_load_dwordx4 v237, s[40:43], s48 offen offset:1024 sc1 lds\n\t" \
  "v_mfma_f32_16x16x32_bf16 v[64:67], v[100:103], v[96:99], v[64:67]\n\t" \
  "ds_read_b128 v[124:127], v227 offset:4096\n\t" \
  "v_mfma_f32_16x16x32_bf16 v[4:7], v[104:107], v[80:83], v[4:7]\n\t" \
  "ds_read_b128 v[128:131], v227 offset:6144\n\t" \
  "buffer_load_dwordx4 v238, s[40:43], s48 offen offset:2048 sc1 lds\n\t" \
  "v_mfma_f32_16x16x32_bf16 v[20:23], v[104:107], v[84:87], v[20:23]\n\t" \
  "ds_read_b128 v[132:135], v227 offset:16384\n\t" \
  "v_mfma_f32_16x16x32_bf16 v[36:39], v[104:107], v[88:91], v[36:39]\n\t" \
  "ds_read_b64_tr_b16 v[140:141], v233 offset:0\n\t" \
  "buffer_load_dwordx4 v239, s[40:43], s48 offen offset:3072 sc1 lds\n\t" \
  "v_mfma_f32_16x16x32_bf16 v[52:55], v[104:107], v[92:95], v[52:55]\n\t" \
  "ds_read_b64_tr_b16 v[142:143], v233 offset:4096\n\t" \
  "s_add_u32 m0, s51, 40960\n\t" \
  "v_mfma_f32_16x16x32_bf16 v[68:71], v[104:107], v[96:99], v[68:71]\n\t" \
  "ds_read_b64_tr_b16 v[144:145], v234 offset:0\n\t" \
  "s_cbranch_vccz 11f\n\t" \
  "buffer_load_dwordx4 v226, s[40:43], s48 offen sc1 lds\n\t" \
  "11:\n\t" \
  "v_mfma_f32_16x16x32_bf16 v[8:11], v[108:111], v[80:83], v[8:11]\n\t" \
  "ds_read_b64_tr_b16 v[146:147], v234 offset:4096\n\t" \
  "s_add_u32 s48, s48, 262144\n\t" \
  "s_add_u32 m0, s53, 32768\n\t" \
  "v_mfma_f32_16x16x32_bf16 v[24:27], v[108:111], v[84:87], v[24:27]\n\t" \
  "ds_read_b64_tr_b16 v[148:149], v235 offset:0\n\t" \
  "buffer_load_dwordx4 v248, s[76:79], s81 offen offset:0 lds\n\t" \
  "v_mfma_f32_16x16x32_bf16 v[40:43], v[108:111], v[88:91], v[40:43]\n\t" \
  "ds_read_b64_tr_b16 v[150:151], v235 offset:4096\n\t" \
  "v_mfma_f32_16x16x32_bf16 v[56:59], v[108:111], v[92:95], v[56:59]\n\t" \
  "buffer_load_dwordx4 v249, s[76:79], s81 offen offset:1024 lds\n\t" \
  "v_mfma_f32_16x16x32_bf16 v[72:75], v[108:111], v[96:99], v[72:75]\n\t" \
  "v_mfma_f32_16x16x32_bf16 v[12:15], v[112:115], v[80:83], v[12:15]\n\t" \
  "buffer_load_dwordx4 v250, s[76:79], s81 offen offset:2048 lds\n\t" \
  "v_mfma_f32_16x16x32_bf16 v[28:31], v[112:115], v[84:87], v[28:31]\n\t" \
  "v_mfma_f32_16x16x32_bf16 v[44:47], v[112:115], v[88:91], v[44:47]\n\t" \
  "buffer_load_dwordx4 v251, s[76:79], s81 offen offset:3072 lds\n\t" \
  "v_mfma_f32_16x16x32_bf16 v[60:63], v[112:115], v[92:95], v[60:63]\n\t" \
  "s_add_u32 s81, s81, 131072\n\t" \
  "v_mfma_f32_16x16x32_bf16 v[76:79], v[112:115], v[96:99], v[76:79]\n\t" \
  "s_waitcnt vmcnt(12) lgkmcnt(0)\n\t" \
  "s_barrier\n\t" \
  "v_mfma_f32_16x16x32_bf16 v[0:3], v[136:139], v[116:119], v[0:3]\n\t" \
  "ds_read_b64_tr_b16 v[100:101], v232 offset:16384\n\t" \
  "s_add_u32 m0, s50, 0\n\t" \
  "v_mfma_f32_16x16x32_bf16 v[16:19], v[136:139], v[120:123], v[16:19]\n\t" \
  "ds_read_b64_tr_b16 v[102:103], v232 offset:20480\n\t" \
  "buffer_load_dwordx4 v236, s[40:43], s48 offen offset:0 sc1 lds\n\t" \
  "v_mfma_f32_16x16x32_bf16 v[32:35], v[136:139], v[124:127], v[32:35]\n\t" \
  "ds_read_b128 v[80:83], v227 offset:20480\n\t" \
  "v_mfma_f32_16x16x32_bf16 v[48:51], v[136:139], v[128:131], v[48:51]\n\t" \
  "ds_read_b128 v[84:87], v227 offset:22528\n\t" \
  "buffer_load_dwordx4 v237, s[40:43], s48 offen offset:1024 sc1 lds\n\t" \
  "v_mfma_f32_16x16x32_bf16 v[64:67], v[136:139], v[132:135], v[64:67]\n\t" \
  "ds_read_b128 v[88:91], v227 offset:24576\n\t" \
  "v_mfma_f32_16x16x32_bf16 v[4:7], v[140:143], v[116:119], v[4:7]\n\t" \
  "ds_read_b128 v[92:95], v227 offset:26624\n\t" \
  "buffer_load_dwordx4 v238, s[40:43], s48 offen offset:2048 sc1 lds\n\t" \
  "v_mfma_f32_16x16x32_bf16 v[20:23], v[140:143], v[120:123], v[20:23]\n\t" \
  "ds_read_b128 v[96:99], v227 offset:36864\n\t" \
  "v_mfma_f32_16x16x32_bf16 v[36:39], v[140:143], v[124:127], v[36:39]\n\t" \
  "ds_read_b64_tr_b16 v[104:105], v233 offset:16384\n\t" \
  "buffer_load_dwordx4 v239, s[40:43], s48 offen offset:3072 sc1 lds\n\t" \
  "v_mfma_f32_16x16x32_bf16 v[52:55], v[140:143], v[128:131], v[52:55]\n\t" \
  "ds_read_b64_tr_b16 v[106:107], v233 offset:20480\n\t" \
  "s_add_u32 m0, s51, 0\n\t" \
  "v_mfma_f32_16x16x32_bf16 v[68:71], v[140:143], v[132:135], v[68:71]\n\t" \
  "ds_read_b64_tr_b16 v[108:109], v234 offset:16384\n\t" \
  "s_cbranch_vccz 11f\n\t" \
  "buffer_load_dwordx4 v226, s[40:43], s48 offen sc1 lds\n\t" \
  "11:\n\t" \
  "v_mfma_f32_16x16x32_bf16 v[8:11], v[144:147], v[116:119], v[8:11]\n\t" \
  "ds_read_b64_tr_b16 v[110:111], v234 offset:20480\n\t" \
  "s_add_u32 s48, s48, 262144\n\t" \
  "s_add_u32 m0, s53, 49152\n\t" \
  "v_mfma_f32_16x16x32_bf16 v[24:27], v[144:147], v[120:123], v[24:27]\n\t" \
  "ds_read_b64_tr_b16 v[112:113], v235 offset:16384\n\t" \
  "buffer_load_dwordx4 v248, s[76:79], s81 offen offset:0 lds\n\t" \
  "v_mfma_f32_16x16x32_bf16 v[40:43], v[144:147], v[124:127], v[40:43]\n\t" \
  "ds_read_b64_tr_b16 v[114:115], v235 offset:20480\n\t" \
  "v_mfma_f32_16x16x32_bf16 v[56:59], v[144:147], v[128:131], v[56:59]\n\t" \
  "buffer_load_dwordx4 v249, s[76:79], s81 offen offset:1024 lds\n\t" \
  "v_mfma_f32_16x16x32_bf16 v[72:75], v[144:147], v[132:135], v[72:75]\n\t" \
  "v_mfma_f32_16x16x32_bf16 v[12:15], v[148:151], v[116:119], v[12:15]\n\t" \
  "buffer_load_dwordx4 v250, s[76:79], s81 offen offset:2048 lds\n\t" \
  "v_mfma_f32_16x16x32_bf16 v[28:31], v[148:151], v[120:123], v[28:31]\n\t" \
  "v_mfma_f32_16x16x32_bf16 v[44:47], v[148:151], v[124:127], v[44:47]\n\t" \
  "buffer_load_dwordx4 v251, s[76:79], s81 offen offset:3072 lds\n\t" \
  "v_mfma_f32_16x16x32_bf16 v[60:63], v[148:151], v[128:131], v[60:63]\n\t" \
  "s_add_u32 s81, s81, 131072\n\t" \
  "v_mfma_f32_16x16x32_bf16 v[76:79], v[148:151], v[132:135], v[76:79]\n\t" \
  "s_waitcnt vmcnt(12) lgkmcnt(0)\n\t" \
  "s_barrier\n\t" \
  "v_mfma_f32_16x16x32_bf16 v[0:3], v[100:103], v[80:83], v[0:3]\n\t" \
  "ds_read_b64_tr_b16 v[136:137], v232 offset:32768\n\t" \
  "s_add_u32 m0, s50, 20480\n\t" \
  "v_mfma_f32_16x16x32_bf16 v[16:19], v[100:103], v[84:87], v[16:19]\n\t" \
  "ds_read_b64_tr_b16 v[138:139], v232 offset:36864\n\t" \
  "buffer_load_dwordx4 v236, s[40:43], s48 offen offset:0 sc1 lds\n\t" \
  "v_mfma_f32_16x16x32_bf16 v[32:35], v[100:103], v[88:91], v[32:35]\n\t" \
  "ds_read_b128 v[116:119], v227 offset:40960\n\t" \
  "v_mfma_f32_16x16x32_bf16 v[48:51], v[100:103], v[92:95], v[48:51]\n\t" \
  "ds_read_b128 v[120:123], v227 offset:43008\n\t" \
  "buffer_load_dwordx4 v237, s[40:43], s48 offen offset:1024 sc1 lds\n\t" \
  "v_mfma_f32_16x16x32_bf16 v[64:67], v[100:103], v[96:99], v[64:67]\n\t" \
  "ds_read_b128 v[124:127], v227 offset:45056\n\t" \
  "v_mfma_f32_16x16x32_bf16 v[4:7], v[104:107], v[80:83], v[4:7]\n\t" \
  "ds_read_b128 v[128:131], v227 offset:47104\n\t" \
  "buffer_load_dwordx4 v238, s[40:43], s48 offen offset:2048 sc1 lds\n\t" \
  "v_mfma_f32_16x16x32_bf16 v[20:23], v[104:107], v[84:87], v[20:23]\n\t" \
  "ds_read_b128 v[132:135], v227 offset:57344\n\t" \
  "v_mfma_f32_16x16x32_bf16 v[36:39], v[104:107], v[88:91], v[36:39]\n\t" \
  "ds_read_b64_tr_b16 v[140:141], v233 offset:32768\n\t" \
  "buffer_load_dwordx4 v239, s[40:43], s48 offen offset:3072 sc1 lds\n\t" \
  "v_mfma_f32_16x16x32_bf16 v[52:55], v[104:107], v[92:95], v[52:55]\n\t" \
  "ds_read_b64_tr_b16 v[142:143], v233 offset:36864\n\t" \
  "s_add_u32 m0, s51, 20480\n\t" \
  "v_mfma_f32_16x16x32_bf16 v[68:71], v[104:107], v[96:99], v[68:71]\n\t" \
  "ds_read_b64_tr_b16 v[144:145], v234 offset:32768\n\t" \
  "s_cbranch_vccz 11f\n\t" \
  "buffer_load_dwordx4 v226, s[40:43], s48 offen sc1 lds\n\t" \
  "11:\n\t" \
  "v_mfma_f32_16x16x32_bf16 v[8:11], v[108:111], v[80:83], v[8:11]\n\t" \
  "ds_read_b64_tr_b16 v[146:147], v234 offset:36864\n\t" \
  "s_add_u32 s48, s48, 262144\n\t" \
  "s_add_u32 m0, s53, 65536\n\t" \
  "v_mfma_f32_16x16x32_bf16 v[24:27], v[108:111], v[84:87], v[24:27]\n\t" \
  "ds_read_b64_tr_b16 v[148:149], v235 offset:32768\n\t" \
  "buffer_load_dwordx4 v248, s[76:79], s81 offen offset:0 lds\n\t" \
  "v_mfma_f32_16x16x32_bf16 v[40:43], v[108:111], v[88:91], v[40:43]\n\t" \
  "ds_read_b64_tr_b16 v[150:151], v235 offset:36864\n\t" \
  "v_mfma_f32_16x16x32_bf16 v[56:59], v[108:111], v[92:95], v[56:59]\n\t" \
  "buffer_load_dwordx4 v249, s[76:79], s81 offen offset:1024 lds\n\t" \
  "v_mfma_f32_16x16x32_bf16 v[72:75], v[108:111], v[96:99], v[72:75]\n\t" \
  "v_mfma_f32_16x16x32_bf16 v[12:15], v[112:115], v[80:83], v[12:15]\n\t" \
  "buffer_load_dwordx4 v250, s[76:79], s81 offen offset:2048 lds\n\t" \
  "v_mfma_f32_16x16x32_bf16 v[28:31], v[112:115], v[84:87], v[28:31]\n\t" \
  "v_mfma_f32_16x16x32_bf16 v[44:47], v[112:115], v[88:91], v[44:47]\n\t" \
  "buffer_load_dwordx4 v251, s[76:79], s81 offen offset:3072 lds\n\t" \
  "v_mfma_f32_16x16x32_bf16 v[60:63], v[112:115], v[92:95], v[60:63]\n\t" \
  "s_add_u32 s81, s81, 131072\n\t" \
  "v_mfma_f32_16x16x32_bf16 v[76:79], v[112:115], v[96:99], v[76:79]\n\t" \
  "s_waitcnt vmcnt(12) lgkmcnt(0)\n\t" \
  "s_barrier\n\t" \
  "v_mfma_f32_16x16x32_bf16 v[0:3], v[136:139], v[116:119], v[0:3]\n\t" \
  "ds_read_b64_tr_b16 v[100:101], v228 offset:0\n\t" \
  "s_add_u32 m0, s50, 40960\n\t" \
  "v_mfma_f32_16x16x32_bf16 v[16:19], v[136:139], v[120:123], v[16:19]\n\t" \
  "ds_read_b64_tr_b16 v[102:103], v228 offset:4096\n\t" \
  "buffer_load_dwordx4 v236, s[40:43], s48 offen offset:0 sc1 lds\n\t" \
  "v_mfma_f32_16x16x32_bf16 v[32:35], v[136:139], v[124:127], v[32:35]\n\t" \
  "ds_read_b128 v[80:83], v227 offset:0\n\t" \
  "v_mfma_f32_16x16x32_bf16 v[48:51], v[136:139], v[128:131], v[48:51]\n\t" \
  "ds_read_b128 v[84:87], v227 offset:2048\n\t" \
  "buffer_load_dwordx4 v237, s[40:43], s48 offen offset:1024 sc1 lds\n\t" \
  "v_mfma_f32_16x16x32_bf16 v[64:67], v[136:139], v[132:135], v[64:67]\n\t" \
  "ds_read_b128 v[88:91], v227 offset:4096\n\t" \
  "v_mfma_f32_16x16x32_bf16 v[4:7], v[140:143], v[116:119], v[4:7]\n\t" \
  "ds_read_b128 v[92:95], v227 offset:6144\n\t" \
  "buffer_load_dwordx4 v238, s[40:43], s48 offen offset:2048 sc1 lds\n\t" \
  "v_mfma_f32_16x16x32_bf16 v[20:23], v[140:143], v[120:123], v[20:23]\n\t" \
  "ds_read_b128 v[96:99], v227 offset:16384\n\t" \
  "v_mfma_f32_16x16x32_bf16 v[36:39], v[140:143], v[124:127], v[36:39]\n\t" \
  "ds_read_b64_tr_b16 v[104:105], v229 offset:0\n\t" \
  "buffer_load_dwordx4 v239, s[40:43], s48 offen offset:3072 sc1 lds\n\t" \
  "v_mfma_f32_16x16x32_bf16 v[52:55], v[140:143], v[128:131], v[52:55]\n\t" \
  "ds_read_b64_tr_b16 v[106:107], v229 offset:4096\n\t" \
  "s_add_u32 m0, s51, 40960\n\t" \
  "v_mfma_f32_16x16x32_bf16 v[68:71], v[140:143], v[132:135], v[68:71]\n\t" \
  "ds_read_b64_tr_b16 v[108:109], v230 offset:0\n\t" \
  "s_cbranch_vccz 11f\n\t" \
  "buffer_load_dwordx4 v226, s[40:43], s48 offen sc1 lds\n\t" \
  "11:\n\t" \
  "v_mfma_f32_16x16x32_bf16 v[8:11], v[144:147], v[116:119], v[8:11]\n\t" \
  "ds_read_b64_tr_b16 v[110:111], v230 offset:4096\n\t" \
  "s_add_u32 s48, s48, 262144\n\t" \
  "s_add_u32 m0, s53, 81920\n\t" \
  "v_mfma_f32_16x16x32_bf16 v[24:27], v[144:147], v[120:123], v[24:27]\n\t" \
  "ds_read_b64_tr_b16 v[112:113], v231 offset:0\n\t" \
  "buffer_load_dwordx4 v248, s[76:79], s81 offen offset:0 lds\n\t" \
  "v_mfma_f32_16x16x32_bf16 v[40:43], v[144:147], v[124:127], v[40:43]\n\t" \
  "ds_read_b64_tr_b16 v[114:115], v231 offset:4096\n\t" \
  "v_mfma_f32_16x16x32_bf16 v[56:59], v[144:147], v[128:131], v[56:59]\n\t" \
  "buffer_load_dwordx4 v249, s[76:79], s81 offen offset:1024 lds\n\t" \
  "v_mfma_f32_16x16x32_bf16 v[72:75], v[144:147], v[132:135], v[72:75]\n\t" \
  "v_mfma_f32_16x16x32_bf16 v[12:15], v[148:151], v[116:119], v[12:15]\n\t" \
  "buffer_load_dwordx4 v250, s[76:79], s81 offen offset:2048 lds\n\t" \
  "v_mfma_f32_16x16x32_bf16 v[28:31], v[148:151], v[120:123], v[28:31]\n\t" \
  "v_mfma_f32_16x16x32_bf16 v[44:47], v[148:151], v[124:127], v[44:47]\n\t" \
  "buffer_load_dwordx4 v251, s[76:79], s81 offen offset:3072 lds\n\t" \
  "v_mfma_f32_16x16x32_bf16 v[60:63], v[148:151], v[128:131], v[60:63]\n\t" \
  "s_add_u32 s81, s81, 131072\n\t" \
  "s_sub_u32 s52, s52, 1\n\t" \
  "s_cmp_lg_u32 s52, 0\n\t" \
  "v_mfma_f32_16x16x32_bf16 v[76:79], v[148:151], v[132:135], v[76:79]\n\t" \
  "s_cbranch_scc1 10b\n\t" \
  "s_waitcnt vmcnt(12) lgkmcnt(0)\n\t" \
  "s_barrier\n\t" \
  "v_mfma_f32_16x16x32_bf16 v[0:3], v[100:103], v[80:83], v[0:3]\n\t" \
  "ds_read_b64_tr_b16 v[136:137], v228 offset:16384\n\t" \
  "s_add_u32 m0, s50, 0\n\t" \
  "v_mfma_f32_16x16x32_bf16 v[16:19], v[100:103], v[84:87], v[16:19]\n\t" \
  "ds_read_b64_tr_b16 v[138:139], v228 offset:20480\n\t" \
  "buffer_load_dwordx4 v236, s[40:43], s48 offen offset:0 sc1 lds\n\t" \
  "v_mfma_f32_16x16x32_bf16 v[32:35], v[100:103], v[88:91], v[32:35]\n\t" \
  "ds_read_b128 v[116:119], v227 offset:20480\n\t" \
  "v_mfma_f32_16x16x32_bf16 v[48:51], v[100:103], v[92:95], v[48:51]\n\t" \
  "ds_read_b128 v[120:123], v227 offset:22528\n\t" \
  "v_mfma_f32_16x16x32_bf16 v[64:67], v[100:103], v[96:99], v[64:67]\n\t" \
  "ds_read_b128 v[124:127], v227 offset:24576\n\t" \
  "v_mfma_f32_16x16x32_bf16 v[4:7], v[104:107], v[80:83], v[4:7]\n\t" \
  "ds_read_b128 v[128:131], v227 offset:26624\n\t" \
  "buffer_load_dwordx4 v237, s[40:43], s48 offen offset:1024 sc1 lds\n\t" \
  "v_mfma_f32_16x16x32_bf16 v[20:23], v[104:107], v[84:87], v[20:23]\n\t" \
  "ds_read_b128 v[132:135], v227 offset:36864\n\t" \
  "v_mfma_f32_16x16x32_bf16 v[36:39], v[104:107], v[88:91], v[36:39]\n\t" \
  "ds_read_b64_tr_b16 v[140:141], v229 offset:16384\n\t" \
  "v_mfma_f32_16x16x32_bf16 v[52:55], v[104:107], v[92:95], v[52:55]\n\t" \
  "ds_read_b64_tr_b16 v[142:143], v229 offset:20480\n\t" \
  "v_mfma_f32_16x16x32_bf16 v[68:71], v[104:107], v[96:99], v[68:71]\n\t" \
  "ds_read_b64_tr_b16 v[144:145], v230 offset:16384\n\t" \
  "buffer_load_dwordx4 v238, s[40:43], s48 offen offset:2048 sc1 lds\n\t" \
  "v_mfma_f32_16x16x32_bf16 v[8:11], v[108:111], v[80:83], v[8:11]\n\t" \
  "ds_read_b64_tr_b16 v[146:147], v230 offset:20480\n\t" \
  "v_mfma_f32_16x16x32_bf16 v[24:27], v[108:111], v[84:87], v[24:27]\n\t" \
  "ds_read_b64_tr_b16 v[148:149], v231 offset:16384\n\t" \
  "v_mfma_f32_16x16x32_bf16 v[40:43], v[108:111], v[88:91], v[40:43]\n\t" \
  "ds_read_b64_tr_b16 v[150:151], v231 offset:20480\n\t" \
  "v_mfma_f32_16x16x32_bf16 v[56:59], v[108:111], v[92:95], v[56:59]\n\t" \
  "buffer_load_dwordx4 v239, s[40:43], s48 offen offset:3072 sc1 lds\n\t" \
  "v_mfma_f32_16x16x32_bf16 v[72:75], v[108:111], v[96:99], v[72:75]\n\t" \
  "v_mfma_f32_16x16x32_bf16 v[12:15], v[112:115], v[80:83], v[12:15]\n\t" \
  "v_mfma_f32_16x16x32_bf16 v[28:31], v[112:115], v[84:87], v[28:31]\n\t" \
  "s_add_u32 m0, s51, 0\n\t" \
  "v_mfma_f32_16x16x32_bf16 v[44:47], v[112:115], v[88:91], v[44:47]\n\t" \
  "s_cbranch_vccz 11f\n\t" \
  "buffer_load_dwordx4 v226, s[40:43], s48 offen sc1 lds\n\t" \
  "11:\n\t" \
  "v_mfma_f32_16x16x32_bf16 v[60:63], v[112:115], v[92:95], v[60:63]\n\t" \
  "s_add_u32 s48, s48, 262144\n\t" \
  "v_mfma_f32_16x16x32_bf16 v[76:79], v[112:115], v[96:99], v[76:79]\n\t" \
  "s_waitcnt vmcnt(8) lgkmcnt(0)\n\t" \
  "s_barrier\n\t" \
  "v_mfma_f32_16x16x32_bf16 v[0:3], v[136:139], v[116:119], v[0:3]\n\t" \
  "ds_read_b64_tr_b16 v[100:101], v228 offset:32768\n\t" \
  "s_add_u32 m0, s50, 20480\n\t" \
  "s_mul_i32 s52, s72, 10240\n\t" \
  "s_add_u32 s52, s52, s73\n\t" \
  "v_mfma_f32_16x16x32_bf16 v[16:19], v[136:139], v[120:123], v[16:19]\n\t" \
  "ds_read_b64_tr_b16 v[102:103], v228 offset:36864\n\t" \
  "buffer_load_dwordx4 v236, s[40:43], s48 offen offset:0 sc1 lds\n\t" \
  "s_add_u32 s52, s52, 65536\n\t" \
  "v_mfma_f32_16x16x32_bf16 v[32:35], v[136:139], v[124:127], v[32:35]\n\t" \
  "ds_read_b128 v[80:83], v227 offset:40960\n\t" \
  "v_add_u32 v152, s52, v243\n\t" \
  "v_mfma_f32_16x16x32_bf16 v[48:51], v[136:139], v[128:131], v[48:51]\n\t" \
  "ds_read_b128 v[84:87], v227 offset:43008\n\t" \
  "s_xor_b32 s52, s72, 1\n\t" \
  "v_mfma_f32_16x16x32_bf16 v[64:67], v[136:139], v[132:135], v[64:67]\n\t" \
  "ds_read_b128 v[88:91], v227 offset:45056\n\t" \
  "s_mul_i32 s52, s52, 10240\n\t" \
  "v_mfma_f32_16x16x32_bf16 v[4:7], v[140:143], v[116:119], v[4:7]\n\t" \
  "ds_read_b128 v[92:95], v227 offset:47104\n\t" \
  "buffer_load_dwordx4 v237, s[40:43], s48 offen offset:1024 sc1 lds\n\t" \
  "s_add_u32 s52, s52, s73\n\t" \
  "v_mfma_f32_16x16x32_bf16 v[20:23], v[140:143], v[120:123], v[20:23]\n\t" \
  "ds_read_b128 v[96:99], v227 offset:57344\n\t" \
  "s_add_u32 s52, s52, 65536\n\t" \
  "v_mfma_f32_16x16x32_bf16 v[36:39], v[140:143], v[124:127], v[36:39]\n\t" \
  "ds_read_b64_tr_b16 v[104:105], v229 offset:32768\n\t" \
  "v_add_u32 v153, s52, v243\n\t" \
  "v_mfma_f32_16x16x32_bf16 v[52:55], v[140:143], v[128:131], v[52:55]\n\t" \
  "ds_read_b64_tr_b16 v[106:107], v229 offset:36864\n\t" \
  "v_mov_b32 v154, v243\n\t" \
  "v_mfma_f32_16x16x32_bf16 v[68:71], v[140:143], v[132:135], v[68:71]\n\t" \
  "ds_read_b64_tr_b16 v[108:109], v230 offset:32768\n\t" \
  "buffer_load_dwordx4 v238, s[40:43], s48 offen offset:2048 sc1 lds\n\t" \
  "s_xor_b32 s84, s54, 1\n\t" \
  "v_mfma_f32_16x16x32_bf16 v[8:11], v[144:147], v[116:119], v[8:11]\n\t" \
  "ds_read_b64_tr_b16 v[110:111], v230 offset:36864\n\t" \
  "s_lshl_b32 s52, s83, 8\n\t" \
  "v_mfma_f32_16x16x32_bf16 v[24:27], v[144:147], v[120:123], v[24:27]\n\t" \
  "ds_read_b64_tr_b16 v[112:113], v231 offset:32768\n\t" \
  "v_mov_b32 v155, 4608\n\t" \
  "v_mfma_f32_16x16x32_bf16 v[40:43], v[144:147], v[124:127], v[40:43]\n\t" \
  "ds_read_b64_tr_b16 v[114:115], v231 offset:36864\n\t" \
  "v_mov_b32 v156, 32\n\t" \
  "v_mfma_f32_16x16x32_bf16 v[56:59], v[144:147], v[128:131], v[56:59]\n\t" \
  "buffer_load_dwordx4 v239, s[40:43], s48 offen offset:3072 sc1 lds\n\t" \
  "v_writelane_b32 v155, s52, 1\n\t" \
  "v_mfma_f32_16x16x32_bf16 v[72:75], v[144:147], v[132:135], v[72:75]\n\t" \
  "s_lshr_b32 s52, s74, 4\n\t" \
  "v_mfma_f32_16x16x32_bf16 v[12:15], v[148:151], v[116:119], v[12:15]\n\t" \
  "s_lshl_b32 s52, s52, 1\n\t" \
  "v_mfma_f32_16x16x32_bf16 v[28:31], v[148:151], v[120:123], v[28:31]\n\t" \
  "s_add_u32 m0, s51, 20480\n\t" \
  "s_or_b32 s52, s52, s84\n\t" \
  "v_mfma_f32_16x16x32_bf16 v[44:47], v[148:151], v[124:127], v[44:47]\n\t" \
  "s_cbranch_vccz 11f\n\t" \
  "buffer_load_dwordx4 v226, s[40:43], s48 offen sc1 lds\n\t" \
  "11:\n\t" \
  "s_lshl_b32 s52, s52, 8\n\t" \
  "v_mfma_f32_16x16x32_bf16 v[60:63], v[148:151], v[128:131], v[60:63]\n\t" \
  "s_add_u32 s48, s48, 262144\n\t" \
  "v_writelane_b32 v155, s52, 2\n\t" \
  "v_mfma_f32_16x16x32_bf16 v[76:79], v[148:151], v[132:135], v[76:79]\n\t" \
  "s_waitcnt vmcnt(4) lgkmcnt(0)\n\t" \
  "s_barrier\n\t" \
  "v_mfma_f32_16x16x32_bf16 v[0:3], v[100:103], v[80:83], v[0:3]\n\t" \
  "ds_read_b64_tr_b16 v[136:137], v232 offset:0\n\t" \
  "s_add_u32 m0, s50, 40960\n\t" \
  "s_add_u32 s52, s74, 8\n\t" \
  "s_add_u32 s52, s52, s91\n\t" \
  "v_mfma_f32_16x16x32_bf16 v[16:19], v[100:103], v[84:87], v[16:19]\n\t" \
  "ds_read_b64_tr_b16 v[138:139], v232 offset:4096\n\t" \
  "buffer_load_dwordx4 v236, s[40:43], s48 offen offset:0 sc1 lds\n\t" \
  "s_lshr_b32 s52, s52, 4\n\t" \
  "v_mfma_f32_16x16x32_bf16 v[32:35], v[100:103], v[88:91], v[32:35]\n\t" \
  "ds_read_b128 v[116:119], v227 offset:0\n\t" \
  "s_lshl_b32 s52, s52, 1\n\t" \
  "v_mfma_f32_16x16x32_bf16 v[48:51], v[100:103], v[92:95], v[48:51]\n\t" \
  "ds_read_b128 v[120:123], v227 offset:2048\n\t" \
  "s_or_b32 s52, s52, s84\n\t" \
  "v_mfma_f32_16x16x32_bf16 v[64:67], v[100:103], v[96:99], v[64:67]\n\t" \
  "ds_read_b128 v[124:127], v227 offset:4096\n\t" \
  "s_lshl_b32 s52, s52, 8\n\t" \
  "v_mfma_f32_16x16x32_bf16 v[4:7], v[104:107], v[80:83], v[4:7]\n\t" \
  "ds_read_b128 v[128:131], v227 offset:6144\n\t" \
  "buffer_load_dwordx4 v237, s[40:43], s48 offen offset:1024 sc1 lds\n\t" \
  "v_writelane_b32 v155, s52, 3\n\t" \
  "v_mfma_f32_16x16x32_bf16 v[20:23], v[104:107], v[84:87], v[20:23]\n\t" \
  "ds_read_b128 v[132:135], v227 offset:16384\n\t" \
  "v_writelane_b32 v156, 14, 1\n\t" \
  "v_mfma_f32_16x16x32_bf16 v[36:39], v[104:107], v[88:91], v[36:39]\n\t" \
  "ds_read_b64_tr_b16 v[140:141], v233 offset:0\n\t" \
  "v_writelane_b32 v156, 14, 2\n\t" \
  "v_mfma_f32_16x16x32_bf16 v[52:55], v[104:107], v[92:95], v[52:55]\n\t" \
  "ds_read_b64_tr_b16 v[142:143], v233 offset:4096\n\t" \
  "v_writelane_b32 v156, 14, 3\n\t" \
  "v_mfma_f32_16x16x32_bf16 v[68:71], v[104:107], v[96:99], v[68:71]\n\t" \
  "ds_read_b64_tr_b16 v[144:145], v234 offset:0\n\t" \
  "buffer_load_dwordx4 v238, s[40:43], s48 offen offset:2048 sc1 lds\n\t" \
  "v_bfe_u32 v169, v243, 4, 4\n\t" \
  "v_mfma_f32_16x16x32_bf16 v[8:11], v[108:111], v[80:83], v[8:11]\n\t" \
  "ds_read_b64_tr_b16 v[146:147], v234 offset:4096\n\t" \
  "v_bfe_u32 v170, v243, 8, 1\n\t" \
  "v_mfma_f32_16x16x32_bf16 v[24:27], v[108:111], v[84:87], v[24:27]\n\t" \
  "ds_read_b64_tr_b16 v[148:149], v235 offset:0\n\t" \
  "v_lshrrev_b32 v171, 9, v243\n\t" \
  "v_mfma_f32_16x16x32_bf16 v[40:43], v[108:111], v[88:91], v[40:43]\n\t" \
  "ds_read_b64_tr_b16 v[150:151], v235 offset:4096\n\t" \
  "v_lshlrev_b32 v170, 5, v170\n\t" \
  "v_mfma_f32_16x16x32_bf16 v[56:59], v[108:111], v[92:95], v[56:59]\n\t" \
  "buffer_load_dwordx4 v239, s[40:43], s48 offen offset:3072 sc1 lds\n\t" \
  "v_lshl_or_b32 v170, v171, 4, v170\n\t" \
  "v_mfma_f32_16x16x32_bf16 v[72:75], v[108:111], v[96:99], v[72:75]\n\t" \
  "v_lshl_add_u32 v171, v169, 6, v170\n\t" \
  "v_mfma_f32_16x16x32_bf16 v[12:15], v[112:115], v[80:83], v[12:15]\n\t" \
  "v_lshl_add_u32 v172, v169, 11, v170\n\t" \
  "v_mfma_f32_16x16x32_bf16 v[28:31], v[112:115], v[84:87], v[28:31]\n\t" \
  "s_add_u32 m0, s51, 40960\n\t" \
  "s_lshl_b32 s85, s89, 2\n\t" \
  "v_mfma_f32_16x16x32_bf16 v[44:47], v[112:115], v[88:91], v[44:47]\n\t" \
  "s_cbranch_vccz 11f\n\t" \
  "buffer_load_dwordx4 v226, s[40:43], s48 offen sc1 lds\n\t" \
  "11:\n\t" \
  "s_add_u32 s85, s85, s74\n\t" \
  "v_mfma_f32_16x16x32_bf16 v[60:63], v[112:115], v[92:95], v[60:63]\n\t" \
  "s_add_u32 s48, s48, 262144\n\t" \
  "s_lshl_b32 s52, s54, 1\n\t" \
  "v_mfma_f32_16x16x32_bf16 v[76:79], v[112:115], v[96:99], v[76:79]\n\t" \
  "s_waitcnt vmcnt(4) lgkmcnt(0)\n\t" \
  "s_barrier\n\t" \
  "v_mfma_f32_16x16x32_bf16 v[0:3], v[136:139], v[116:119], v[0:3]\n\t" \
  "ds_read_b64_tr_b16 v[100:101], v232 offset:16384\n\t" \
  "s_add_u32 s85, s85, s52\n\t" \
  "s_add_u32 s86, s74, s89\n\t" \
  "v_mfma_f32_16x16x32_bf16 v[16:19], v[136:139], v[120:123], v[16:19]\n\t" \
  "ds_read_b64_tr_b16 v[102:103], v232 offset:20480\n\t" \
  "s_add_u32 s86, s86, 8\n\t" \
  "v_mfma_f32_16x16x32_bf16 v[32:35], v[136:139], v[124:127], v[32:35]\n\t" \
  "ds_read_b128 v[80:83], v227 offset:20480\n\t" \
  "s_lshl_b32 s87, s75, 2\n\t" \
  "v_mfma_f32_16x16x32_bf16 v[48:51], v[136:139], v[128:131], v[48:51]\n\t" \
  "ds_read_b128 v[84:87], v227 offset:22528\n\t" \
  "s_add_u32 s87, s87, s88\n\t" \
  "v_mfma_f32_16x16x32_bf16 v[64:67], v[136:139], v[132:135], v[64:67]\n\t" \
  "ds_read_b128 v[88:91], v227 offset:24576\n\t" \
  "s_cmp_eq_u32 s54, s89\n\t" \
  "s_cselect_b32 s92, s55, 0\n\t" \
  "v_mfma_f32_16x16x32_bf16 v[4:7], v[140:143], v[116:119], v[4:7]\n\t" \
  "ds_read_b128 v[92:95], v227 offset:26624\n\t" \
  "s_lshl_b32 s52, s87, 17\n\t" \
  "v_mfma_f32_16x16x32_bf16 v[20:23], v[140:143], v[120:123], v[20:23]\n\t" \
  "ds_read_b128 v[96:99], v227 offset:36864\n\t" \
  "s_add_u32 s52, s52, 9437184\n\t" \
  "v_mfma_f32_16x16x32_bf16 v[36:39], v[140:143], v[124:127], v[36:39]\n\t" \
  "ds_read_b64_tr_b16 v[104:105], v233 offset:16384\n\t" \
  "s_lshl_b32 s84, s85, 10\n\t" \
  "v_mfma_f32_16x16x32_bf16 v[52:55], v[140:143], v[128:131], v[52:55]\n\t" \
  "ds_read_b64_tr_b16 v[106:107], v233 offset:20480\n\t" \
  "s_add_u32 s84, s84, s52\n\t" \
  "v_mfma_f32_16x16x32_bf16 v[68:71], v[140:143], v[132:135], v[68:71]\n\t" \
  "ds_read_b64_tr_b16 v[108:109], v234 offset:16384\n\t" \
  "v_add_u32 v157, s84, v171\n\t" \
  "v_mfma_f32_16x16x32_bf16 v[8:11], v[144:147], v[116:119], v[8:11]\n\t" \
  "ds_read_b64_tr_b16 v[110:111], v234 offset:20480\n\t" \
  "v_add_u32 v158, 1024, v157\n\t" \
  "v_mfma_f32_16x16x32_bf16 v[24:27], v[144:147], v[120:123], v[24:27]\n\t" \
  "ds_read_b64_tr_b16 v[112:113], v235 offset:16384\n\t" \
  "s_lshl_b32 s84, s86, 10\n\t" \
  "v_mfma_f32_16x16x32_bf16 v[40:43], v[144:147], v[124:127], v[40:43]\n\t" \
  "ds_read_b64_tr_b16 v[114:115], v235 offset:20480\n\t" \
  "s_add_u32 s84, s84, s52\n\t" \
  "v_mfma_f32_16x16x32_bf16 v[56:59], v[144:147], v[128:131], v[56:59]\n\t" \
  "s_cmp_lg_u32 s92, 0\n\t" \
  "s_cselect_b32 s84, s84, 0x40000000\n\t" \
  "v_mfma_f32_16x16x32_bf16 v[72:75], v[144:147], v[132:135], v[72:75]\n\t" \
  "v_add_u32 v159, s84, v171\n\t" \
  "v_mfma_f32_16x16x32_bf16 v[12:15], v[148:151], v[116:119], v[12:15]\n\t" \
  "s_lshl_b32 s84, s85, 6\n\t" \
  "v_mfma_f32_16x16x32_bf16 v[28:31], v[148:151], v[120:123], v[28:31]\n\t" \
  "s_add_u32 s84, s84, 24576\n\t" \
  "v_mfma_f32_16x16x32_bf16 v[44:47], v[148:151], v[124:127], v[44:47]\n\t" \
  "v_lshl_add_u32 v160, v169, 2, s84\n\t" \
  "v_mfma_f32_16x16x32_bf16 v[60:63], v[148:151], v[128:131], v[60:63]\n\t" \
  "v_add_u32 v161, 64, v160\n\t" \
  "v_mfma_f32_16x16x32_bf16 v[76:79], v[148:151], v[132:135], v[76:79]\n\t" \
  "s_waitcnt vmcnt(0) lgkmcnt(0)\n\t" \
  "s_barrier\n\t" \
  "v_mfma_f32_16x16x32_bf16 v[0:3], v[100:103], v[80:83], v[0:3]\n\t" \
  "ds_read_b64_tr_b16 v[136:137], v232 offset:32768\n\t" \
  "s_lshl_b32 s84, s86, 6\n\t" \
  "s_add_u32 s84, s84, 24576\n\t" \
  "v_mfma_f32_16x16x32_bf16 v[16:19], v[100:103], v[84:87], v[16:19]\n\t" \
  "ds_read_b64_tr_b16 v[138:139], v232 offset:36864\n\t" \
  "s_cmp_lg_u32 s92, 0\n\t" \
  "s_cselect_b32 s84, s84, 0x40000000\n\t" \
  "v_mfma_f32_16x16x32_bf16 v[32:35], v[100:103], v[88:91], v[32:35]\n\t" \
  "ds_read_b128 v[116:119], v227 offset:40960\n\t" \
  "v_lshl_add_u32 v162, v169, 2, s84\n\t" \
  "v_mfma_f32_16x16x32_bf16 v[48:51], v[100:103], v[92:95], v[48:51]\n\t" \
  "ds_read_b128 v[120:123], v227 offset:43008\n\t" \
  "s_lshl_b32 s52, s87, 6\n\t" \
  "v_mfma_f32_16x16x32_bf16 v[64:67], v[100:103], v[96:99], v[64:67]\n\t" \
  "ds_read_b128 v[124:127], v227 offset:45056\n\t" \
  "s_lshl_b32 s84, s85, 15\n\t" \
  "v_mfma_f32_16x16x32_bf16 v[4:7], v[104:107], v[80:83], v[4:7]\n\t" \
  "ds_read_b128 v[128:131], v227 offset:47104\n\t" \
  "s_add_u32 s84, s84, s52\n\t" \
  "v_mfma_f32_16x16x32_bf16 v[20:23], v[104:107], v[84:87], v[20:23]\n\t" \
  "ds_read_b128 v[132:135], v227 offset:57344\n\t" \
  "v_add_u32 v163, s84, v172\n\t" \
  "v_mfma_f32_16x16x32_bf16 v[36:39], v[104:107], v[88:91], v[36:39]\n\t" \
  "ds_read_b64_tr_b16 v[140:141], v233 offset:32768\n\t" \
  "s_lshl_b32 s84, s86, 15\n\t" \
  "v_mfma_f32_16x16x32_bf16 v[52:55], v[104:107], v[92:95], v[52:55]\n\t" \
  "ds_read_b64_tr_b16 v[142:143], v233 offset:36864\n\t" \
  "s_add_u32 s84, s84, s52\n\t" \
  "v_mfma_f32_16x16x32_bf16 v[68:71], v[104:107], v[96:99], v[68:71]\n\t" \
  "ds_read_b64_tr_b16 v[144:145], v234 offset:32768\n\t" \
  "s_cmp_lg_u32 s92, 0\n\t" \
  "s_cselect_b32 s84, s84, 0x40000000\n\t" \
  "v_mfma_f32_16x16x32_bf16 v[8:11], v[108:111], v[80:83], v[8:11]\n\t" \
  "ds_read_b64_tr_b16 v[146:147], v234 offset:36864\n\t" \
  "v_add_u32 v168, s84, v172\n\t" \
  "v_mfma_f32_16x16x32_bf16 v[24:27], v[108:111], v[84:87], v[24:27]\n\t" \
  "ds_read_b64_tr_b16 v[148:149], v235 offset:32768\n\t" \
  "s_cmp_lg_u32 s92, 0\n\t" \
  "s_cselect_b32 s84, 2048, 0x40000000\n\t" \
  "v_mfma_f32_16x16x32_bf16 v[40:43], v[108:111], v[88:91], v[40:43]\n\t" \
  "ds_read_b64_tr_b16 v[150:151], v235 offset:36864\n\t" \
  "v_add_u32 v167, s84, v243\n\t" \
  "v_mfma_f32_16x16x32_bf16 v[56:59], v[108:111], v[92:95], v[56:59]\n\t" \
  "s_xor_b32 s84, s92, s55\n\t" \
  "v_mfma_f32_16x16x32_bf16 v[72:75], v[108:111], v[96:99], v[72:75]\n\t" \
  "s_cmp_lg_u32 s84, 0\n\t" \
  "s_cselect_b32 s84, 2048, 0x40000000\n\t" \
  "v_mfma_f32_16x16x32_bf16 v[12:15], v[112:115], v[80:83], v[12:15]\n\t" \
  "v_add_u32 v166, s84, v243\n\t" \
  "v_mfma_f32_16x16x32_bf16 v[28:31], v[112:115], v[84:87], v[28:31]\n\t" \
  "v_mov_b32 v164, 1\n\t" \
  "v_mfma_f32_16x16x32_bf16 v[44:47], v[112:115], v[88:91], v[44:47]\n\t" \
  "v_mov_b32 v165, 0x40000000\n\t" \
  "v_mfma_f32_16x16x32_bf16 v[60:63], v[112:115], v[92:95], v[60:63]\n\t" \
  "v_writelane_b32 v165, 0, 0\n\t" \
  "v_mfma_f32_16x16x32_bf16 v[76:79], v[112:115], v[96:99], v[76:79]\n\t" \
  "s_waitcnt lgkmcnt(0)\n\t" \
  "v_mfma_f32_16x16x32_bf16 v[8:11], v[144:147], v[116:119], v[8:11]\n\t" \
  "v_mfma_f32_16x16x32_bf16 v[24:27], v[144:147], v[120:123], v[24:27]\n\t" \
  "v_mfma_f32_16x16x32_bf16 v[40:43], v[144:147], v[124:127], v[40:43]\n\t" \
  "v_mfma_f32_16x16x32_bf16 v[56:59], v[144:147], v[128:131], v[56:59]\n\t" \
  "v_mfma_f32_16x16x32_bf16 v[72:75], v[144:147], v[132:135], v[72:75]\n\t" \
  "v_mfma_f32_16x16x32_bf16 v[12:15], v[148:151], v[116:119], v[12:15]\n\t" \
  "v_mfma_f32_16x16x32_bf16 v[28:31], v[148:151], v[120:123], v[28:31]\n\t" \
  "v_mfma_f32_16x16x32_bf16 v[44:47], v[148:151], v[124:127], v[44:47]\n\t" \
  "v_mfma_f32_16x16x32_bf16 v[60:63], v[148:151], v[128:131], v[60:63]\n\t" \
  "v_mfma_f32_16x16x32_bf16 v[76:79], v[148:151], v[132:135], v[76:79]\n\t" \
  "v_mfma_f32_16x16x32_bf16 v[0:3], v[136:139], v[116:119], v[0:3]\n\t" \
  "v_mfma_f32_16x16x32_bf16 v[16:19], v[136:139], v[120:123], v[16:19]\n\t" \
  "v_mfma_f32_16x16x32_bf16 v[32:35], v[136:139], v[124:127], v[32:35]\n\t" \
  "v_mfma_f32_16x16x32_bf16 v[48:51], v[136:139], v[128:131], v[48:51]\n\t" \
  "v_mfma_f32_16x16x32_bf16 v[64:67], v[136:139], v[132:135], v[64:67]\n\t" \
  "v_mfma_f32_16x16x32_bf16 v[4:7], v[140:143], v[116:119], v[4:7]\n\t" \
  "v_mfma_f32_16x16x32_bf16 v[20:23], v[140:143], v[120:123], v[20:23]\n\t" \
  "v_mfma_f32_16x16x32_bf16 v[36:39], v[140:143], v[124:127], v[36:39]\n\t" \
  "v_mfma_f32_16x16x32_bf16 v[52:55], v[140:143], v[128:131], v[52:55]\n\t" \
  "v_mfma_f32_16x16x32_bf16 v[68:71], v[140:143], v[132:135], v[68:71]\n\t" \
  "s_branch 49f\n\t" \
  "41:\n\t" \
  "v_mov_b32 v0, 0\n\t" \
  "v_mov_b32 v1, 0\n\t" \
  "v_mov_b32 v2, 0\n\t" \
  "v_mov_b32 v3, 0\n\t" \
  "v_mov_b32 v4, 0\n\t" \
  "v_mov_b32 v5, 0\n\t" \
  "v_mov_b32 v6, 0\n\t" \
  "v_mov_b32 v7, 0\n\t" \
  "v_mov_b32 v8, 0\n\t" \
  "v_mov_b32 v9, 0\n\t" \
  "v_mov_b32 v10, 0\n\t" \
  "v_mov_b32 v11, 0\n\t" \
  "v_mov_b32 v12, 0\n\t" \
  "v_mov_b32 v13, 0\n\t" \
  "v_mov_b32 v14, 0\n\t" \
  "v_mov_b32 v15, 0\n\t" \
  "v_mov_b32 v16, 0\n\t" \
  "v_mov_b32 v17, 0\n\t" \
  "v_mov_b32 v18, 0\n\t" \
  "v_mov_b32 v19, 0\n\t" \
  "v_mov_b32 v20, 0\n\t" \
  "v_mov_b32 v21, 0\n\t" \
  "v_mov_b32 v22, 0\n\t" \
  "v_mov_b32 v23, 0\n\t" \
  "v_mov_b32 v24, 0\n\t" \
  "v_mov_b32 v25, 0\n\t" \
  "v_mov_b32 v26, 0\n\t" \
  "v_mov_b32 v27, 0\n\t" \
  "v_mov_b32 v28, 0\n\t" \
  "v_mov_b32 v29, 0\n\t" \
  "v_mov_b32 v30, 0\n\t" \
  "v_mov_b32 v31, 0\n\t" \
  "v_mov_b32 v32, 0\n\t" \
  "v_mov_b32 v33, 0\n\t" \
  "v_mov_b32 v34, 0\n\t" \
  "v_mov_b32 v35, 0\n\t" \
  "v_mov_b32 v36, 0\n\t" \
  "v_mov_b32 v37, 0\n\t" \
  "v_mov_b32 v38, 0\n\t" \
  "v_mov_b32 v39, 0\n\t" \
  "v_mov_b32 v40, 0\n\t" \
  "v_mov_b32 v41, 0\n\t" \
  "v_mov_b32 v42, 0\n\t" \
  "v_mov_b32 v43, 0\n\t" \
  "v_mov_b32 v44, 0\n\t" \
  "v_mov_b32 v45, 0\n\t" \
  "v_mov_b32 v46, 0\n\t" \
  "v_mov_b32 v47, 0\n\t" \
  "v_mov_b32 v48, 0\n\t" \
  "v_mov_b32 v49, 0\n\t" \
  "v_mov_b32 v50, 0\n\t" \
  "v_mov_b32 v51, 0\n\t" \
  "v_mov_b32 v52, 0\n\t" \
  "v_mov_b32 v53, 0\n\t" \
  "v_mov_b32 v54, 0\n\t" \
  "v_mov_b32 v55, 0\n\t" \
  "v_mov_b32 v56, 0\n\t" \
  "v_mov_b32 v57, 0\n\t" \
  "v_mov_b32 v58, 0\n\t" \
  "v_mov_b32 v59, 0\n\t" \
  "v_mov_b32 v60, 0\n\t" \
  "v_mov_b32 v61, 0\n\t" \
  "v_mov_b32 v62, 0\n\t" \
  "v_mov_b32 v63, 0\n\t" \
  "s_waitcnt vmcnt(0)\n\t" \
  "s_barrier\n\t" \
  "ds_read_b64_tr_b16 v[100:101], v228 offset:32768\n\t" \
  "ds_read_b64_tr_b16 v[102:103], v228 offset:36864\n\t" \
  "ds_read_b128 v[80:83], v227 offset:40960\n\t" \
  "ds_read_b128 v[84:87], v227 offset:43008\n\t" \
  "ds_read_b128 v[88:91], v227 offset:45056\n\t" \
  "ds_read_b128 v[92:95], v227 offset:47104\n\t" \
  "ds_read_b64_tr_b16 v[104:105], v229 offset:32768\n\t" \
  "ds_read_b64_tr_b16 v[106:107], v229 offset:36864\n\t" \
  "ds_read_b64_tr_b16 v[108:109], v230 offset:32768\n\t" \
  "ds_read_b64_tr_b16 v[110:111], v230 offset:36864\n\t" \
  "ds_read_b64_tr_b16 v[112:113], v231 offset:32768\n\t" \
  "ds_read_b64_tr_b16 v[114:115], v231 offset:36864\n\t" \
  "s_mov_b32 s52, 4\n\t" \
  "s_branch 17f\n\t" \
  ".p2align 3\n\t" \
  "15:\n\t" \
  "s_waitcnt lgkmcnt(0)\n\t" \
  "s_barrier\n\t" \
  "v_mfma_f32_16x16x32_bf16 v[0:3], v[100:103], v[80:83], v[0:3]\n\t" \
  "ds_read_b64_tr_b16 v[136:137], v228 offset:16384\n\t" \
  "v_mfma_f32_16x16x32_bf16 v[16:19], v[100:103], v[84:87], v[16:19]\n\t" \
  "ds_read_b64_tr_b16 v[138:139], v228 offset:20480\n\t" \
  "v_mfma_f32_16x16x32_bf16 v[32:35], v[100:103], v[88:91], v[32:35]\n\t" \
  "ds_read_b128 v[116:119], v227 offset:20480\n\t" \
  "v_mfma_f32_16x16x32_bf16 v[48:51], v[100:103], v[92:95], v[48:51]\n\t" \
  "ds_read_b128 v[120:123], v227 offset:22528\n\t" \
  "v_mfma_f32_16x16x32_bf16 v[4:7], v[104:107], v[80:83], v[4:7]\n\t" \
  "ds_read_b128 v[124:127], v227 offset:24576\n\t" \
  "v_mfma_f32_16x16x32_bf16 v[20:23], v[104:107], v[84:87], v[20:23]\n\t" \
  "ds_read_b128 v[128:131], v227 offset:26624\n\t" \
  "v_mfma_f32_16x16x32_bf16 v[36:39], v[104:107], v[88:91], v[36:39]\n\t" \
  "ds_read_b64_tr_b16 v[140:141], v229 offset:16384\n\t" \
  "v_mfma_f32_16x16x32_bf16 v[52:55], v[104:107], v[92:95], v[52:55]\n\t" \
  "ds_read_b64_tr_b16 v[142:143], v229 offset:20480\n\t" \
  "v_mfma_f32_16x16x32_bf16 v[8:11], v[108:111], v[80:83], v[8:11]\n\t" \
  "ds_read_b64_tr_b16 v[144:145], v230 offset:16384\n\t" \
  "v_mfma_f32_16x16x32_bf16 v[24:27], v[108:111], v[84:87], v[24:27]\n\t" \
  "ds_read_b64_tr_b16 v[146:147], v230 offset:20480\n\t" \
  "v_mfma_f32_16x16x32_bf16 v[40:43], v[108:111], v[88:91], v[40:43]\n\t" \
  "ds_read_b64_tr_b16 v[148:149], v231 offset:16384\n\t" \
  "v_mfma_f32_16x16x32_bf16 v[56:59], v[108:111], v[92:95], v[56:59]\n\t" \
  "ds_read_b64_tr_b16 v[150:151], v231 offset:20480\n\t" \
  "v_mfma_f32_16x16x32_bf16 v[12:15], v[112:115], v[80:83], v[12:15]\n\t" \
  "v_mfma_f32_16x16x32_bf16 v[28:31], v[112:115], v[84:87], v[28:31]\n\t" \
  "v_mfma_f32_16x16x32_bf16 v[44:47], v[112:115], v[88:91], v[44:47]\n\t" \
  "v_mfma_f32_16x16x32_bf16 v[60:63], v[112:115], v[92:95], v[60:63]\n\t" \
  "s_waitcnt lgkmcnt(0)\n\t" \
  "s_barrier\n\t" \
  "v_mfma_f32_16x16x32_bf16 v[0:3], v[136:139], v[116:119], v[0:3]\n\t" \
  "ds_read_b64_tr_b16 v[100:101], v228 offset:32768\n\t" \
  "v_mfma_f32_16x16x32_bf16 v[16:19], v[136:139], v[120:123], v[16:19]\n\t" \
  "ds_read_b64_tr_b16 v[102:103], v228 offset:36864\n\t" \
  "v_mfma_f32_16x16x32_bf16 v[32:35], v[136:139], v[124:127], v[32:35]\n\t" \
  "ds_read_b128 v[80:83], v227 offset:40960\n\t" \
  "v_mfma_f32_16x16x32_bf16 v[48:51], v[136:139], v[128:131], v[48:51]\n\t" \
  "ds_read_b128 v[84:87], v227 offset:43008\n\t" \
  "v_mfma_f32_16x16x32_bf16 v[4:7], v[140:143], v[116:119], v[4:7]\n\t" \
  "ds_read_b128 v[88:91], v227 offset:45056\n\t" \
  "v_mfma_f32_16x16x32_bf16 v[20:23], v[140:143], v[120:123], v[20:23]\n\t" \
  "ds_read_b128 v[92:95], v227 offset:47104\n\t" \
  "v_mfma_f32_16x16x32_bf16 v[36:39], v[140:143], v[124:127], v[36:39]\n\t" \
  "ds_read_b64_tr_b16 v[104:105], v229 offset:32768\n\t" \
  "v_mfma_f32_16x16x32_bf16 v[52:55], v[140:143], v[128:131], v[52:55]\n\t" \
  "ds_read_b64_tr_b16 v[106:107], v229 offset:36864\n\t" \
  "v_mfma_f32_16x16x32_bf16 v[8:11], v[144:147], v[116:119], v[8:11]\n\t" \
  "ds_read_b64_tr_b16 v[108:109], v230 offset:32768\n\t" \
  "v_mfma_f32_16x16x32_bf16 v[24:27], v[144:147], v[120:123], v[24:27]\n\t" \
  "ds_read_b64_tr_b16 v[110:111], v230 offset:36864\n\t" \
  "v_mfma_f32_16x16x32_bf16 v[40:43], v[144:147], v[124:127], v[40:43]\n\t" \
  "ds_read_b64_tr_b16 v[112:113], v231 offset:32768\n\t" \
  "v_mfma_f32_16x16x32_bf16 v[56:59], v[144:147], v[128:131], v[56:59]\n\t" \
  "ds_read_b64_tr_b16 v[114:115], v231 offset:36864\n\t" \
  "v_mfma_f32_16x16x32_bf16 v[12:15], v[148:151], v[116:119], v[12:15]\n\t" \
  "v_mfma_f32_16x16x32_bf16 v[28:31], v[148:151], v[120:123], v[28:31]\n\t" \
  "v_mfma_f32_16x16x32_bf16 v[44:47], v[148:151], v[124:127], v[44:47]\n\t" \
  "v_mfma_f32_16x16x32_bf16 v[60:63], v[148:151], v[128:131], v[60:63]\n\t" \
  "17:\n\t" \
  "s_waitcnt lgkmcnt(0)\n\t" \
  "s_barrier\n\t" \
  "v_mfma_f32_16x16x32_bf16 v[0:3], v[100:103], v[80:83], v[0:3]\n\t" \
  "ds_read_b64_tr_b16 v[136:137], v232 offset:0\n\t" \
  "v_mfma_f32_16x16x32_bf16 v[16:19], v[100:103], v[84:87], v[16:19]\n\t" \
  "ds_read_b64_tr_b16 v[138:139], v232 offset:4096\n\t" \
  "v_mfma_f32_16x16x32_bf16 v[32:35], v[100:103], v[88:91], v[32:35]\n\t" \
  "ds_read_b128 v[116:119], v227 offset:0\n\t" \
  "v_mfma_f32_16x16x32_bf16 v[48:51], v[100:103], v[92:95], v[48:51]\n\t" \
  "ds_read_b128 v[120:123], v227 offset:2048\n\t" \
  "v_mfma_f32_16x16x32_bf16 v[4:7], v[104:107], v[80:83], v[4:7]\n\t" \
  "ds_read_b128 v[124:127], v227 offset:4096\n\t" \
  "v_mfma_f32_16x16x32_bf16 v[20:23], v[104:107], v[84:87], v[20:23]\n\t" \
  "ds_read_b128 v[128:131], v227 offset:6144\n\t" \
  "v_mfma_f32_16x16x32_bf16 v[36:39], v[104:107], v[88:91], v[36:39]\n\t" \
  "ds_read_b64_tr_b16 v[140:141], v233 offset:0\n\t" \
  "v_mfma_f32_16x16x32_bf16 v[52:55], v[104:107], v[92:95], v[52:55]\n\t" \
  "ds_read_b64_tr_b16 v[142:143], v233 offset:4096\n\t" \
  "v_mfma_f32_16x16x32_bf16 v[8:11], v[108:111], v[80:83], v[8:11]\n\t" \
  "ds_read_b64_tr_b16 v[144:145], v234 offset:0\n\t" \
  "v_mfma_f32_16x16x32_bf16 v[24:27], v[108:111], v[84:87], v[24:27]\n\t" \
  "ds_read_b64_tr_b16 v[146:147], v234 offset:4096\n\t" \
  "v_mfma_f32_16x16x32_bf16 v[40:43], v[108:111], v[88:91], v[40:43]\n\t" \
  "ds_read_b64_tr_b16 v[148:149], v235 offset:0\n\t" \
  "v_mfma_f32_16x16x32_bf16 v[56:59], v[108:111], v[92:95], v[56:59]\n\t" \
  "ds_read_b64_tr_b16 v[150:151], v235 offset:4096\n\t" \
  "v_mfma_f32_16x16x32_bf16 v[12:15], v[112:115], v[80:83], v[12:15]\n\t" \
  "v_mfma_f32_16x16x32_bf16 v[28:31], v[112:115], v[84:87], v[28:31]\n\t" \
  "v_mfma_f32_16x16x32_bf16 v[44:47], v[112:115], v[88:91], v[44:47]\n\t" \
  "v_mfma_f32_16x16x32_bf16 v[60:63], v[112:115], v[92:95], v[60:63]\n\t" \
  "s_waitcnt lgkmcnt(0)\n\t" \
  "s_barrier\n\t" \
  "v_mfma_f32_16x16x32_bf16 v[0:3], v[136:139], v[116:119], v[0:3]\n\t" \
  "ds_read_b64_tr_b16 v[100:101], v232 offset:16384\n\t" \
  "v_mfma_f32_16x16x32_bf16 v[16:19], v[136:139], v[120:123], v[16:19]\n\t" \
  "ds_read_b64_tr_b16 v[102:103], v232 offset:20480\n\t" \
  "v_mfma_f32_16x16x32_bf16 v[32:35], v[136:139], v[124:127], v[32:35]\n\t" \
  "ds_read_b128 v[80:83], v227 offset:20480\n\t" \
  "v_mfma_f32_16x16x32_bf16 v[48:51], v[136:139], v[128:131], v[48:51]\n\t" \
  "ds_read_b128 v[84:87], v227 offset:22528\n\t" \
  "v_mfma_f32_16x16x32_bf16 v[4:7], v[140:143], v[116:119], v[4:7]\n\t" \
  "ds_read_b128 v[88:91], v227 offset:24576\n\t" \
  "v_mfma_f32_16x16x32_bf16 v[20:23], v[140:143], v[120:123], v[20:23]\n\t" \
  "ds_read_b128 v[92:95], v227 offset:26624\n\t" \
  "v_mfma_f32_16x16x32_bf16 v[36:39], v[140:143], v[124:127], v[36:39]\n\t" \
  "ds_read_b64_tr_b16 v[104:105], v233 offset:16384\n\t" \
  "v_mfma_f32_16x16x32_bf16 v[52:55], v[140:143], v[128:131], v[52:55]\n\t" \
  "ds_read_b64_tr_b16 v[106:107], v233 offset:20480\n\t" \
  "v_mfma_f32_16x16x32_bf16 v[8:11], v[144:147], v[116:119], v[8:11]\n\t" \
  "ds_read_b64_tr_b16 v[108:109], v234 offset:16384\n\t" \
  "v_mfma_f32_16x16x32_bf16 v[24:27], v[144:147], v[120:123], v[24:27]\n\t" \
  "ds_read_b64_tr_b16 v[110:111], v234 offset:20480\n\t" \
  "v_mfma_f32_16x16x32_bf16 v[40:43], v[144:147], v[124:127], v[40:43]\n\t" \
  "ds_read_b64_tr_b16 v[112:113], v235 offset:16384\n\t" \
  "v_mfma_f32_16x16x32_bf16 v[56:59], v[144:147], v[128:131], v[56:59]\n\t" \
  "ds_read_b64_tr_b16 v[114:115], v235 offset:20480\n\t" \
  "v_mfma_f32_16x16x32_bf16 v[12:15], v[148:151], v[116:119], v[12:15]\n\t" \
  "v_mfma_f32_16x16x32_bf16 v[28:31], v[148:151], v[120:123], v[28:31]\n\t" \
  "v_mfma_f32_16x16x32_bf16 v[44:47], v[148:151], v[124:127], v[44:47]\n\t" \
  "v_mfma_f32_16x16x32_bf16 v[60:63], v[148:151], v[128:131], v[60:63]\n\t" \
  "s_waitcnt lgkmcnt(0)\n\t" \
  "s_barrier\n\t" \
  "v_mfma_f32_16x16x32_bf16 v[0:3], v[100:103], v[80:83], v[0:3]\n\t" \
  "ds_read_b64_tr_b16 v[136:137], v232 offset:32768\n\t" \
  "v_mfma_f32_16x16x32_bf16 v[16:19], v[100:103], v[84:87], v[16:19]\n\t" \
  "ds_read_b64_tr_b16 v[138:139], v232 offset:36864\n\t" \
  "v_mfma_f32_16x16x32_bf16 v[32:35], v[100:103], v[88:91], v[32:35]\n\t" \
  "ds_read_b128 v[116:119], v227 offset:40960\n\t" \
  "v_mfma_f32_16x16x32_bf16 v[48:51], v[100:103], v[92:95], v[48:51]\n\t" \
  "ds_read_b128 v[120:123], v227 offset:43008\n\t" \
  "v_mfma_f32_16x16x32_bf16 v[4:7], v[104:107], v[80:83], v[4:7]\n\t" \
  "ds_read_b128 v[124:127], v227 offset:45056\n\t" \
  "v_mfma_f32_16x16x32_bf16 v[20:23], v[104:107], v[84:87], v[20:23]\n\t" \
  "ds_read_b128 v[128:131], v227 offset:47104\n\t" \
  "v_mfma_f32_16x16x32_bf16 v[36:39], v[104:107], v[88:91], v[36:39]\n\t" \
  "ds_read_b64_tr_b16 v[140:141], v233 offset:32768\n\t" \
  "v_mfma_f32_16x16x32_bf16 v[52:55], v[104:107], v[92:95], v[52:55]\n\t" \
  "ds_read_b64_tr_b16 v[142:143], v233 offset:36864\n\t" \
  "v_mfma_f32_16x16x32_bf16 v[8:11], v[108:111], v[80:83], v[8:11]\n\t" \
  "ds_read_b64_tr_b16 v[144:145], v234 offset:32768\n\t" \
  "v_mfma_f32_16x16x32_bf16 v[24:27], v[108:111], v[84:87], v[24:27]\n\t" \
  "ds_read_b64_tr_b16 v[146:147], v234 offset:36864\n\t" \
  "v_mfma_f32_16x16x32_bf16 v[40:43], v[108:111], v[88:91], v[40:43]\n\t" \
  "ds_read_b64_tr_b16 v[148:149], v235 offset:32768\n\t" \
  "v_mfma_f32_16x16x32_bf16 v[56:59], v[108:111], v[92:95], v[56:59]\n\t" \
  "ds_read_b64_tr_b16 v[150:151], v235 offset:36864\n\t" \
  "v_mfma_f32_16x16x32_bf16 v[12:15], v[112:115], v[80:83], v[12:15]\n\t" \
  "v_mfma_f32_16x16x32_bf16 v[28:31], v[112:115], v[84:87], v[28:31]\n\t" \
  "v_mfma_f32_16x16x32_bf16 v[44:47], v[112:115], v[88:91], v[44:47]\n\t" \
  "v_mfma_f32_16x16x32_bf16 v[60:63], v[112:115], v[92:95], v[60:63]\n\t" \
  "s_waitcnt lgkmcnt(0)\n\t" \
  "s_barrier\n\t" \
  "v_mfma_f32_16x16x32_bf16 v[0:3], v[136:139], v[116:119], v[0:3]\n\t" \
  "ds_read_b64_tr_b16 v[100:101], v228 offset:0\n\t" \
  "v_mfma_f32_16x16x32_bf16 v[16:19], v[136:139], v[120:123], v[16:19]\n\t" \
  "ds_read_b64_tr_b16 v[102:103], v228 offset:4096\n\t" \
  "v_mfma_f32_16x16x32_bf16 v[32:35], v[136:139], v[124:127], v[32:35]\n\t" \
  "ds_read_b128 v[80:83], v227 offset:0\n\t" \
  "v_mfma_f32_16x16x32_bf16 v[48:51], v[136:139], v[128:131], v[48:51]\n\t" \
  "ds_read_b128 v[84:87], v227 offset:2048\n\t" \
  "v_mfma_f32_16x16x32_bf16 v[4:7], v[140:143], v[116:119], v[4:7]\n\t" \
  "ds_read_b128 v[88:91], v227 offset:4096\n\t" \
  "v_mfma_f32_16x16x32_bf16 v[20:23], v[140:143], v[120:123], v[20:23]\n\t" \
  "ds_read_b128 v[92:95], v227 offset:6144\n\t" \
  "v_mfma_f32_16x16x32_bf16 v[36:39], v[140:143], v[124:127], v[36:39]\n\t" \
  "ds_read_b64_tr_b16 v[104:105], v229 offset:0\n\t" \
  "v_mfma_f32_16x16x32_bf16 v[52:55], v[140:143], v[128:131], v[52:55]\n\t" \
  "ds_read_b64_tr_b16 v[106:107], v229 offset:4096\n\t" \
  "v_mfma_f32_16x16x32_bf16 v[8:11], v[144:147], v[116:119], v[8:11]\n\t" \
  "ds_read_b64_tr_b16 v[108:109], v230 offset:0\n\t" \
  "v_mfma_f32_16x16x32_bf16 v[24:27], v[144:147], v[120:123], v[24:27]\n\t" \
  "ds_read_b64_tr_b16 v[110:111], v230 offset:4096\n\t" \
  "v_mfma_f32_16x16x32_bf16 v[40:43], v[144:147], v[124:127], v[40:43]\n\t" \
  "ds_read_b64_tr_b16 v[112:113], v231 offset:0\n\t" \
  "v_mfma_f32_16x16x32_bf16 v[56:59], v[144:147], v[128:131], v[56:59]\n\t" \
  "ds_read_b64_tr_b16 v[114:115], v231 offset:4096\n\t" \
  "v_mfma_f32_16x16x32_bf16 v[12:15], v[148:151], v[116:119], v[12:15]\n\t" \
  "v_mfma_f32_16x16x32_bf16 v[28:31], v[148:151], v[120:123], v[28:31]\n\t" \
  "v_mfma_f32_16x16x32_bf16 v[44:47], v[148:151], v[124:127], v[44:47]\n\t" \
  "s_sub_u32 s52, s52, 1\n\t" \
  "s_cmp_lg_u32 s52, 0\n\t" \
  "v_mfma_f32_16x16x32_bf16 v[60:63], v[148:151], v[128:131], v[60:63]\n\t" \
  "s_cbranch_scc1 15b\n\t" \
  "s_waitcnt lgkmcnt(0)\n\t" \
  "s_barrier\n\t" \
  "v_mfma_f32_16x16x32_bf16 v[0:3], v[100:103], v[80:83], v[0:3]\n\t" \
  "ds_read_b64_tr_b16 v[136:137], v228 offset:16384\n\t" \
  "v_mfma_f32_16x16x32_bf16 v[16:19], v[100:103], v[84:87], v[16:19]\n\t" \
  "ds_read_b64_tr_b16 v[138:139], v228 offset:20480\n\t" \
  "v_mfma_f32_16x16x32_bf16 v[32:35], v[100:103], v[88:91], v[32:35]\n\t" \
  "ds_read_b128 v[116:119], v227 offset:20480\n\t" \
  "v_mfma_f32_16x16x32_bf16 v[48:51], v[100:103], v[92:95], v[48:51]\n\t" \
  "ds_read_b128 v[120:123], v227 offset:22528\n\t" \
  "v_mfma_f32_16x16x32_bf16 v[4:7], v[104:107], v[80:83], v[4:7]\n\t" \
  "ds_read_b128 v[124:127], v227 offset:24576\n\t" \
  "v_mfma_f32_16x16x32_bf16 v[20:23], v[104:107], v[84:87], v[20:23]\n\t" \
  "ds_read_b128 v[128:131], v227 offset:26624\n\t" \
  "v_mfma_f32_16x16x32_bf16 v[36:39], v[104:107], v[88:91], v[36:39]\n\t" \
  "ds_read_b64_tr_b16 v[140:141], v229 offset:16384\n\t" \
  "v_mfma_f32_16x16x32_bf16 v[52:55], v[104:107], v[92:95], v[52:55]\n\t" \
  "ds_read_b64_tr_b16 v[142:143], v229 offset:20480\n\t" \
  "v_mfma_f32_16x16x32_bf16 v[8:11], v[108:111], v[80:83], v[8:11]\n\t" \
  "ds_read_b64_tr_b16 v[144:145], v230 offset:16384\n\t" \
  "v_mfma_f32_16x16x32_bf16 v[24:27], v[108:111], v[84:87], v[24:27]\n\t" \
  "ds_read_b64_tr_b16 v[146:147], v230 offset:20480\n\t" \
  "v_mfma_f32_16x16x32_bf16 v[40:43], v[108:111], v[88:91], v[40:43]\n\t" \
  "ds_read_b64_tr_b16 v[148:149], v231 offset:16384\n\t" \
  "v_mfma_f32_16x16x32_bf16 v[56:59], v[108:111], v[92:95], v[56:59]\n\t" \
  "ds_read_b64_tr_b16 v[150:151], v231 offset:20480\n\t" \
  "v_mfma_f32_16x16x32_bf16 v[12:15], v[112:115], v[80:83], v[12:15]\n\t" \
  "v_mfma_f32_16x16x32_bf16 v[28:31], v[112:115], v[84:87], v[28:31]\n\t" \
  "v_mfma_f32_16x16x32_bf16 v[44:47], v[112:115], v[88:91], v[44:47]\n\t" \
  "v_mfma_f32_16x16x32_bf16 v[60:63], v[112:115], v[92:95], v[60:63]\n\t" \
  "s_waitcnt lgkmcnt(0)\n\t" \
  "s_barrier\n\t" \
  "v_mfma_f32_16x16x32_bf16 v[0:3], v[136:139], v[116:119], v[0:3]\n\t" \
  "ds_read_b64_tr_b16 v[100:101], v228 offset:32768\n\t" \
  "s_mul_i32 s52, s72, 10240\n\t" \
  "s_add_u32 s52, s52, s73\n\t" \
  "v_mfma_f32_16x16x32_bf16 v[16:19], v[136:139], v[120:123], v[16:19]\n\t" \
  "ds_read_b64_tr_b16 v[102:103], v228 offset:36864\n\t" \
  "s_add_u32 s52, s52, 65536\n\t" \
  "v_mfma_f32_16x16x32_bf16 v[32:35], v[136:139], v[124:127], v[32:35]\n\t" \
  "ds_read_b128 v[80:83], v227 offset:40960\n\t" \
  "v_add_u32 v152, s52, v243\n\t" \
  "v_mfma_f32_16x16x32_bf16 v[48:51], v[136:139], v[128:131], v[48:51]\n\t" \
  "ds_read_b128 v[84:87], v227 offset:43008\n\t" \
  "s_xor_b32 s52, s72, 1\n\t" \
  "s_mul_i32 s52, s52, 10240\n\t" \
  "v_mfma_f32_16x16x32_bf16 v[4:7], v[140:143], v[116:119], v[4:7]\n\t" \
  "ds_read_b128 v[88:91], v227 offset:45056\n\t" \
  "s_add_u32 s52, s52, s73\n\t" \
  "v_mfma_f32_16x16x32_bf16 v[20:23], v[140:143], v[120:123], v[20:23]\n\t" \
  "ds_read_b128 v[92:95], v227 offset:47104\n\t" \
  "s_add_u32 s52, s52, 65536\n\t" \
  "v_mfma_f32_16x16x32_bf16 v[36:39], v[140:143], v[124:127], v[36:39]\n\t" \
  "ds_read_b64_tr_b16 v[104:105], v229 offset:32768\n\t" \
  "v_add_u32 v153, s52, v243\n\t" \
  "v_mov_b32 v154, v243\n\t" \
  "v_mfma_f32_16x16x32_bf16 v[52:55], v[140:143], v[128:131], v[52:55]\n\t" \
  "ds_read_b64_tr_b16 v[106:107], v229 offset:36864\n\t" \
  "s_xor_b32 s84, s54, 1\n\t" \
  "v_mfma_f32_16x16x32_bf16 v[8:11], v[144:147], v[116:119], v[8:11]\n\t" \
  "ds_read_b64_tr_b16 v[108:109], v230 offset:32768\n\t" \
  "s_lshl_b32 s52, s83, 8\n\t" \
  "v_mfma_f32_16x16x32_bf16 v[24:27], v[144:147], v[120:123], v[24:27]\n\t" \
  "ds_read_b64_tr_b16 v[110:111], v230 offset:36864\n\t" \
  "v_mov_b32 v155, 4608\n\t" \
  "v_mov_b32 v156, 32\n\t" \
  "v_mfma_f32_16x16x32_bf16 v[40:43], v[144:147], v[124:127], v[40:43]\n\t" \
  "ds_read_b64_tr_b16 v[112:113], v231 offset:32768\n\t" \
  "v_writelane_b32 v155, s52, 1\n\t" \
  "v_mfma_f32_16x16x32_bf16 v[56:59], v[144:147], v[128:131], v[56:59]\n\t" \
  "ds_read_b64_tr_b16 v[114:115], v231 offset:36864\n\t" \
  "s_lshr_b32 s52, s74, 4\n\t" \
  "v_mfma_f32_16x16x32_bf16 v[12:15], v[148:151], v[116:119], v[12:15]\n\t" \
  "s_lshl_b32 s52, s52, 1\n\t" \
  "s_or_b32 s52, s52, s84\n\t" \
  "v_mfma_f32_16x16x32_bf16 v[28:31], v[148:151], v[120:123], v[28:31]\n\t" \
  "s_lshl_b32 s52, s52, 8\n\t" \
  "v_mfma_f32_16x16x32_bf16 v[44:47], v[148:151], v[124:127], v[44:47]\n\t" \
  "v_writelane_b32 v155, s52, 2\n\t" \
  "v_mfma_f32_16x16x32_bf16 v[60:63], v[148:151], v[128:131], v[60:63]\n\t" \
  "s_waitcnt lgkmcnt(0)\n\t" \
  "s_barrier\n\t" \
  "v_mfma_f32_16x16x32_bf16 v[0:3], v[100:103], v[80:83], v[0:3]\n\t" \
  "ds_read_b64_tr_b16 v[136:137], v232 offset:0\n\t" \
  "s_add_u32 s52, s74, 8\n\t" \
  "s_add_u32 s52, s52, s91\n\t" \
  "v_mfma_f32_16x16x32_bf16 v[16:19], v[100:103], v[84:87], v[16:19]\n\t" \
  "ds_read_b64_tr_b16 v[138:139], v232 offset:4096\n\t" \
  "s_lshr_b32 s52, s52, 4\n\t" \
  "v_mfma_f32_16x16x32_bf16 v[32:35], v[100:103], v[88:91], v[32:35]\n\t" \
  "ds_read_b128 v[116:119], v227 offset:0\n\t" \
  "s_lshl_b32 s52, s52, 1\n\t" \
  "v_mfma_f32_16x16x32_bf16 v[48:51], v[100:103], v[92:95], v[48:51]\n\t" \
  "ds_read_b128 v[120:123], v227 offset:2048\n\t" \
  "s_or_b32 s52, s52, s84\n\t" \
  "s_lshl_b32 s52, s52, 8\n\t" \
  "v_mfma_f32_16x16x32_bf16 v[4:7], v[104:107], v[80:83], v[4:7]\n\t" \
  "ds_read_b128 v[124:127], v227 offset:4096\n\t" \
  "v_writelane_b32 v155, s52, 3\n\t" \
  "v_mfma_f32_16x16x32_bf16 v[20:23], v[104:107], v[84:87], v[20:23]\n\t" \
  "ds_read_b128 v[128:131], v227 offset:6144\n\t" \
  "v_writelane_b32 v156, 14, 1\n\t" \
  "v_mfma_f32_16x16x32_bf16 v[36:39], v[104:107], v[88:91], v[36:39]\n\t" \
  "ds_read_b64_tr_b16 v[140:141], v233 offset:0\n\t" \
  "v_writelane_b32 v156, 14, 2\n\t" \
  "v_writelane_b32 v156, 14, 3\n\t" \
  "v_mfma_f32_16x16x32_bf16 v[52:55], v[104:107], v[92:95], v[52:55]\n\t" \
  "ds_read_b64_tr_b16 v[142:143], v233 offset:4096\n\t" \
  "v_bfe_u32 v169, v243, 4, 4\n\t" \
  "v_mfma_f32_16x16x32_bf16 v[8:11], v[108:111], v[80:83], v[8:11]\n\t" \
  "ds_read_b64_tr_b16 v[144:145], v234 offset:0\n\t" \
  "v_bfe_u32 v170, v243, 8, 1\n\t" \
  "v_mfma_f32_16x16x32_bf16 v[24:27], v[108:111], v[84:87], v[24:27]\n\t" \
  "ds_read_b64_tr_b16 v[146:147], v234 offset:4096\n\t" \
  "v_lshrrev_b32 v171, 9, v243\n\t" \
  "v_lshlrev_b32 v170, 5, v170\n\t" \
  "v_mfma_f32_16x16x32_bf16 v[40:43], v[108:111], v[88:91], v[40:43]\n\t" \
  "ds_read_b64_tr_b16 v[148:149], v235 offset:0\n\t" \
  "v_lshl_or_b32 v170, v171, 4, v170\n\t" \
  "v_mfma_f32_16x16x32_bf16 v[56:59], v[108:111], v[92:95], v[56:59]\n\t" \
  "ds_read_b64_tr_b16 v[150:151], v235 offset:4096\n\t" \
  "v_lshl_add_u32 v171, v169, 6, v170\n\t" \
  "v_mfma_f32_16x16x32_bf16 v[12:15], v[112:115], v[80:83], v[12:15]\n\t" \
  "v_lshl_add_u32 v172, v169, 11, v170\n\t" \
  "s_lshl_b32 s85, s89, 2\n\t" \
  "v_mfma_f32_16x16x32_bf16 v[28:31], v[112:115], v[84:87], v[28:31]\n\t" \
  "s_add_u32 s85, s85, s74\n\t" \
  "v_mfma_f32_16x16x32_bf16 v[44:47], v[112:115], v[88:91], v[44:47]\n\t" \
  "s_lshl_b32 s52, s54, 1\n\t" \
  "v_mfma_f32_16x16x32_bf16 v[60:63], v[112:115], v[92:95], v[60:63]\n\t" \
  "s_waitcnt lgkmcnt(0)\n\t" \
  "s_barrier\n\t" \
  "v_mfma_f32_16x16x32_bf16 v[0:3], v[136:139], v[116:119], v[0:3]\n\t" \
  "ds_read_b64_tr_b16 v[100:101], v232 offset:16384\n\t" \
  "s_add_u32 s85, s85, s52\n\t" \
  "s_add_u32 s86, s74, s89\n\t" \
  "v_mfma_f32_16x16x32_bf16 v[16:19], v[136:139], v[120:123], v[16:19]\n\t" \
  "ds_read_b64_tr_b16 v[102:103], v232 offset:20480\n\t" \
  "s_add_u32 s86, s86, 8\n\t" \
  "v_mfma_f32_16x16x32_bf16 v[32:35], v[136:139], v[124:127], v[32:35]\n\t" \
  "ds_read_b128 v[80:83], v227 offset:20480\n\t" \
  "s_lshl_b32 s87, s75, 2\n\t" \
  "v_mfma_f32_16x16x32_bf16 v[48:51], v[136:139], v[128:131], v[48:51]\n\t" \
  "ds_read_b128 v[84:87], v227 offset:22528\n\t" \
  "s_add_u32 s87, s87, s88\n\t" \
  "s_cmp_eq_u32 s54, s89\n\t" \
  "s_cselect_b32 s92, s55, 0\n\t" \
  "v_mfma_f32_16x16x32_bf16 v[4:7], v[140:143], v[116:119], v[4:7]\n\t" \
  "ds_read_b128 v[88:91], v227 offset:24576\n\t" \
  "s_lshl_b32 s52, s87, 17\n\t" \
  "v_mfma_f32_16x16x32_bf16 v[20:23], v[140:143], v[120:123], v[20:23]\n\t" \
  "ds_read_b128 v[92:95], v227 offset:26624\n\t" \
  "s_add_u32 s52, s52, 9437184\n\t" \
  "v_mfma_f32_16x16x32_bf16 v[36:39], v[140:143], v[124:127], v[36:39]\n\t" \
  "ds_read_b64_tr_b16 v[104:105], v233 offset:16384\n\t" \
  "s_lshl_b32 s84, s85, 10\n\t" \
  "s_add_u32 s84, s84, s52\n\t" \
  "v_mfma_f32_16x16x32_bf16 v[52:55], v[140:143], v[128:131], v[52:55]\n\t" \
  "ds_read_b64_tr_b16 v[106:107], v233 offset:20480\n\t" \
  "v_add_u32 v157, s84, v171\n\t" \
  "v_mfma_f32_16x16x32_bf16 v[8:11], v[144:147], v[116:119], v[8:11]\n\t" \
  "ds_read_b64_tr_b16 v[108:109], v234 offset:16384\n\t" \
  "v_add_u32 v158, 1024, v157\n\t" \
  "v_mfma_f32_16x16x32_bf16 v[24:27], v[144:147], v[120:123], v[24:27]\n\t" \
  "ds_read_b64_tr_b16 v[110:111], v234 offset:20480\n\t" \
  "s_lshl_b32 s84, s86, 10\n\t" \
  "s_add_u32 s84, s84, s52\n\t" \
  "v_mfma_f32_16x16x32_bf16 v[40:43], v[144:147], v[124:127], v[40:43]\n\t" \
  "ds_read_b64_tr_b16 v[112:113], v235 offset:16384\n\t" \
  "s_cmp_lg_u32 s92, 0\n\t" \
  "s_cselect_b32 s84, s84, 0x40000000\n\t" \
  "v_mfma_f32_16x16x32_bf16 v[56:59], v[144:147], v[128:131], v[56:59]\n\t" \
  "ds_read_b64_tr_b16 v[114:115], v235 offset:20480\n\t" \
  "v_add_u32 v159, s84, v171\n\t" \
  "v_mfma_f32_16x16x32_bf16 v[12:15], v[148:151], v[116:119], v[12:15]\n\t" \
  "s_lshl_b32 s84, s85, 6\n\t" \
  "s_add_u32 s84, s84, 24576\n\t" \
  "v_mfma_f32_16x16x32_bf16 v[28:31], v[148:151], v[120:123], v[28:31]\n\t" \
  "v_lshl_add_u32 v160, v169, 2, s84\n\t" \
  "v_mfma_f32_16x16x32_bf16 v[44:47], v[148:151], v[124:127], v[44:47]\n\t" \
  "v_add_u32 v161, 64, v160\n\t" \
  "v_mfma_f32_16x16x32_bf16 v[60:63], v[148:151], v[128:131], v[60:63]\n\t" \
  "s_waitcnt lgkmcnt(0)\n\t" \
  "s_barrier\n\t" \
  "v_mfma_f32_16x16x32_bf16 v[0:3], v[100:103], v[80:83], v[0:3]\n\t" \
  "ds_read_b64_tr_b16 v[136:137], v232 offset:32768\n\t" \
  "s_lshl_b32 s84, s86, 6\n\t" \
  "s_add_u32 s84, s84, 24576\n\t" \
  "v_mfma_f32_16x16x32_bf16 v[16:19], v[100:103], v[84:87], v[16:19]\n\t" \
  "ds_read_b64_tr_b16 v[138:139], v232 offset:36864\n\t" \
  "s_cmp_lg_u32 s92, 0\n\t" \
  "s_cselect_b32 s84, s84, 0x40000000\n\t" \
  "v_mfma_f32_16x16x32_bf16 v[32:35], v[100:103], v[88:91], v[32:35]\n\t" \
  "ds_read_b128 v[116:119], v227 offset:40960\n\t" \
  "v_lshl_add_u32 v162, v169, 2, s84\n\t" \
  "v_mfma_f32_16x16x32_bf16 v[48:51], v[100:103], v[92:95], v[48:51]\n\t" \
  "ds_read_b128 v[120:123], v227 offset:43008\n\t" \
  "s_lshl_b32 s52, s87, 6\n\t" \
  "s_lshl_b32 s84, s85, 15\n\t" \
  "v_mfma_f32_16x16x32_bf16 v[4:7], v[104:107], v[80:83], v[4:7]\n\t" \
  "ds_read_b128 v[124:127], v227 offset:45056\n\t" \
  "s_add_u32 s84, s84, s52\n\t" \
  "v_mfma_f32_16x16x32_bf16 v[20:23], v[104:107], v[84:87], v[20:23]\n\t" \
  "ds_read_b128 v[128:131], v227 offset:47104\n\t" \
  "v_add_u32 v163, s84, v172\n\t" \
  "v_mfma_f32_16x16x32_bf16 v[36:39], v[104:107], v[88:91], v[36:39]\n\t" \
  "ds_read_b64_tr_b16 v[140:141], v233 offset:32768\n\t" \
  "s_lshl_b32 s84, s86, 15\n\t" \
  "s_add_u32 s84, s84, s52\n\t" \
  "v_mfma_f32_16x16x32_bf16 v[52:55], v[104:107], v[92:95], v[52:55]\n\t" \
  "ds_read_b64_tr_b16 v[142:143], v233 offset:36864\n\t" \
  "s_cmp_lg_u32 s92, 0\n\t" \
  "s_cselect_b32 s84, s84, 0x40000000\n\t" \
  "v_mfma_f32_16x16x32_bf16 v[8:11], v[108:111], v[80:83], v[8:11]\n\t" \
  "ds_read_b64_tr_b16 v[144:145], v234 offset:32768\n\t" \
  "v_add_u32 v168, s84, v172\n\t" \
  "v_mfma_f32_16x16x32_bf16 v[24:27], v[108:111], v[84:87], v[24:27]\n\t" \
  "ds_read_b64_tr_b16 v[146:147], v234 offset:36864\n\t" \
  "s_cmp_lg_u32 s92, 0\n\t" \
  "s_cselect_b32 s84, 2048, 0x40000000\n\t" \
  "v_add_u32 v167, s84, v243\n\t" \
  "v_mfma_f32_16x16x32_bf16 v[40:43], v[108:111], v[88:91], v[40:43]\n\t" \
  "ds_read_b64_tr_b16 v[148:149], v235 offset:32768\n\t" \
  "s_xor_b32 s84, s92, s55\n\t" \
  "v_mfma_f32_16x16x32_bf16 v[56:59], v[108:111], v[92:95], v[56:59]\n\t" \
  "ds_read_b64_tr_b16 v[150:151], v235 offset:36864\n\t" \
  "s_cmp_lg_u32 s84, 0\n\t" \
  "s_cselect_b32 s84, 2048, 0x40000000\n\t" \
  "v_mfma_f32_16x16x32_bf16 v[12:15], v[112:115], v[80:83], v[12:15]\n\t" \
  "v_add_u32 v166, s84, v243\n\t" \
  "v_mov_b32 v164, 1\n\t" \
  "v_mfma_f32_16x16x32_bf16 v[28:31], v[112:115], v[84:87], v[28:31]\n\t" \
  "v_mov_b32 v165, 0x40000000\n\t" \
  "v_mfma_f32_16x16x32_bf16 v[44:47], v[112:115], v[88:91], v[44:47]\n\t" \
  "v_writelane_b32 v165, 0, 0\n\t" \
  "v_mfma_f32_16x16x32_bf16 v[60:63], v[112:115], v[92:95], v[60:63]\n\t" \
  "s_waitcnt lgkmcnt(0)\n\t" \
  "v_mfma_f32_16x16x32_bf16 v[8:11], v[144:147], v[116:119], v[8:11]\n\t" \
  "v_mfma_f32_16x16x32_bf16 v[24:27], v[144:147], v[120:123], v[24:27]\n\t" \
  "v_mfma_f32_16x16x32_bf16 v[40:43], v[144:147], v[124:127], v[40:43]\n\t" \
  "v_mfma_f32_16x16x32_bf16 v[56:59], v[144:147], v[128:131], v[56:59]\n\t" \
  "v_mfma_f32_16x16x32_bf16 v[12:15], v[148:151], v[116:119], v[12:15]\n\t" \
  "v_mfma_f32_16x16x32_bf16 v[28:31], v[148:151], v[120:123], v[28:31]\n\t" \
  "v_mfma_f32_16x16x32_bf16 v[44:47], v[148:151], v[124:127], v[44:47]\n\t" \
  "v_mfma_f32_16x16x32_bf16 v[60:63], v[148:151], v[128:131], v[60:63]\n\t" \
  "v_mfma_f32_16x16x32_bf16 v[0:3], v[136:139], v[116:119], v[0:3]\n\t" \
  "v_mfma_f32_16x16x32_bf16 v[16:19], v[136:139], v[120:123], v[16:19]\n\t" \
  "v_mfma_f32_16x16x32_bf16 v[32:35], v[136:139], v[124:127], v[32:35]\n\t" \
  "v_mfma_f32_16x16x32_bf16 v[48:51], v[136:139], v[128:131], v[48:51]\n\t" \
  "v_mfma_f32_16x16x32_bf16 v[4:7], v[140:143], v[116:119], v[4:7]\n\t" \
  "v_mfma_f32_16x16x32_bf16 v[20:23], v[140:143], v[120:123], v[20:23]\n\t" \
  "v_mfma_f32_16x16x32_bf16 v[36:39], v[140:143], v[124:127], v[36:39]\n\t" \
  "v_mfma_f32_16x16x32_bf16 v[52:55], v[140:143], v[128:131], v[52:55]\n\t" \
  "s_branch 49f\n\t" \
  "42:\n\t" \
  "v_mov_b32 v0, 0\n\t" \
  "v_mov_b32 v1, 0\n\t" \
  "v_mov_b32 v2, 0\n\t" \
  "v_mov_b32 v3, 0\n\t" \
  "v_mov_b32 v4, 0\n\t" \
  "v_mov_b32 v5, 0\n\t" \
  "v_mov_b32 v6, 0\n\t" \
  "v_mov_b32 v7, 0\n\t" \
  "v_mov_b32 v8, 0\n\t" \
  "v_mov_b32 v9, 0\n\t" \
  "v_mov_b32 v10, 0\n\t" \
  "v_mov_b32 v11, 0\n\t" \
  "v_mov_b32 v12, 0\n\t" \
  "v_mov_b32 v13, 0\n\t" \
  "v_mov_b32 v14, 0\n\t" \
  "v_mov_b32 v15, 0\n\t" \
  "v_mov_b32 v16, 0\n\t" \
  "v_mov_b32 v17, 0\n\t" \
  "v_mov_b32 v18, 0\n\t" \
  "v_mov_b32 v19, 0\n\t" \
  "v_mov_b32 v20, 0\n\t" \
  "v_mov_b32 v21, 0\n\t" \
  "v_mov_b32 v22, 0\n\t" \
  "v_mov_b32 v23, 0\n\t" \
  "v_mov_b32 v24, 0\n\t" \
  "v_mov_b32 v25, 0\n\t" \
  "v_mov_b32 v26, 0\n\t" \
  "v_mov_b32 v27, 0\n\t" \
  "v_mov_b32 v28, 0\n\t" \
  "v_mov_b32 v29, 0\n\t" \
  "v_mov_b32 v30, 0\n\t" \
  "v_mov_b32 v31, 0\n\t" \
  "v_mov_b32 v32, 0\n\t" \
  "v_mov_b32 v33, 0\n\t" \
  "v_mov_b32 v34, 0\n\t" \
  "v_mov_b32 v35, 0\n\t" \
  "v_mov_b32 v36, 0\n\t" \
  "v_mov_b32 v37, 0\n\t" \
  "v_mov_b32 v38, 0\n\t" \
  "v_mov_b32 v39, 0\n\t" \
  "v_mov_b32 v40, 0\n\t" \
  "v_mov_b32 v41, 0\n\t" \
  "v_mov_b32 v42, 0\n\t" \
  "v_mov_b32 v43, 0\n\t" \
  "v_mov_b32 v44, 0\n\t" \
  "v_mov_b32 v45, 0\n\t" \
  "v_mov_b32 v46, 0\n\t" \
  "v_mov_b32 v47, 0\n\t" \
  "v_mov_b32 v48, 0\n\t" \
  "v_mov_b32 v49, 0\n\t" \
  "v_mov_b32 v50, 0\n\t" \
  "v_mov_b32 v51, 0\n\t" \
  "v_mov_b32 v52, 0\n\t" \
  "v_mov_b32 v53, 0\n\t" \
  "v_mov_b32 v54, 0\n\t" \
  "v_mov_b32 v55, 0\n\t" \
  "v_mov_b32 v56, 0\n\t" \
  "v_mov_b32 v57, 0\n\t" \
  "v_mov_b32 v58, 0\n\t" \
  "v_mov_b32 v59, 0\n\t" \
  "v_mov_b32 v60, 0\n\t" \
  "v_mov_b32 v61, 0\n\t" \
  "v_mov_b32 v62, 0\n\t" \
  "v_mov_b32 v63, 0\n\t" \
  "v_mov_b32 v64, 0\n\t" \
  "v_mov_b32 v65, 0\n\t" \
  "v_mov_b32 v66, 0\n\t" \
  "v_mov_b32 v67, 0\n\t" \
  "v_mov_b32 v68, 0\n\t" \
  "v_mov_b32 v69, 0\n\t" \
  "v_mov_b32 v70, 0\n\t" \
  "v_mov_b32 v71, 0\n\t" \
  "v_mov_b32 v72, 0\n\t" \
  "v_mov_b32 v73, 0\n\t" \
  "v_mov_b32 v74, 0\n\t" \
  "v_mov_b32 v75, 0\n\t" \
  "v_mov_b32 v76, 0\n\t" \
  "v_mov_b32 v77, 0\n\t" \
  "v_mov_b32 v78, 0\n\t" \
  "v_mov_b32 v79, 0\n\t" \
  "s_waitcnt vmcnt(0)\n\t" \
  "s_barrier\n\t" \
  "ds_read_b64_tr_b16 v[100:101], v228 offset:32768\n\t" \
  "ds_read_b64_tr_b16 v[102:103], v228 offset:36864\n\t" \
  "ds_read_b128 v[80:83], v227 offset:40960\n\t" \
  "ds_read_b128 v[84:87], v227 offset:43008\n\t" \
  "ds_read_b128 v[88:91], v227 offset:45056\n\t" \
  "ds_read_b128 v[92:95], v227 offset:47104\n\t" \
  "ds_read_b128 v[96:99], v227 offset:51200\n\t" \
  "ds_read_b64_tr_b16 v[104:105], v229 offset:32768\n\t" \
  "ds_read_b64_tr_b16 v[106:107], v229 offset:36864\n\t" \
  "ds_read_b64_tr_b16 v[108:109], v230 offset:32768\n\t" \
  "ds_read_b64_tr_b16 v[110:111], v230 offset:36864\n\t" \
  "ds_read_b64_tr_b16 v[112:113], v231 offset:32768\n\t" \
  "ds_read_b64_tr_b16 v[114:115], v231 offset:36864\n\t" \
  "s_mov_b32 s52, 4\n\t" \
  "s_branch 22f\n\t" \
  ".p2align 3\n\t" \
  "20:\n\t" \
  "s_waitcnt lgkmcnt(0)\n\t" \
  "s_barrier\n\t" \
  "v_mfma_f32_16x16x32_bf16 v[0:3], v[100:103], v[80:83], v[0:3]\n\t" \
  "ds_read_b64_tr_b16 v[136:137], v228 offset:16384\n\t" \
  "v_mfma_f32_16x16x32_bf16 v[16:19], v[100:103], v[84:87], v[16:19]\n\t" \
  "ds_read_b64_tr_b16 v[138:139], v228 offset:20480\n\t" \
  "v_mfma_f32_16x16x32_bf16 v[32:35], v[100:103], v[88:91], v[32:35]\n\t" \
  "ds_read_b128 v[116:119], v227 offset:20480\n\t" \
  "v_mfma_f32_16x16x32_bf16 v[48:51], v[100:103], v[92:95], v[48:51]\n\t" \
  "ds_read_b128 v[120:123], v227 offset:22528\n\t" \
  "v_mfma_f32_16x16x32_bf16 v[64:67], v[100:103], v[96:99], v[64:67]\n\t" \
  "ds_read_b128 v[124:127], v227 offset:24576\n\t" \
  "v_mfma_f32_16x16x32_bf16 v[4:7], v[104:107], v[80:83], v[4:7]\n\t" \
  "ds_read_b128 v[128:131], v227 offset:26624\n\t" \
  "v_mfma_f32_16x16x32_bf16 v[20:23], v[104:107], v[84:87], v[20:23]\n\t" \
  "ds_read_b128 v[132:135], v227 offset:30720\n\t" \
  "v_mfma_f32_16x16x32_bf16 v[36:39], v[104:107], v[88:91], v[36:39]\n\t" \
  "ds_read_b64_tr_b16 v[140:141], v229 offset:16384\n\t" \
  "v_mfma_f32_16x16x32_bf16 v[52:55], v[104:107], v[92:95], v[52:55]\n\t" \
  "ds_read_b64_tr_b16 v[142:143], v229 offset:20480\n\t" \
  "v_mfma_f32_16x16x32_bf16 v[68:71], v[104:107], v[96:99], v[68:71]\n\t" \
  "ds_read_b64_tr_b16 v[144:145], v230 offset:16384\n\t" \
  "v_mfma_f32_16x16x32_bf16 v[8:11], v[108:111], v[80:83], v[8:11]\n\t" \
  "ds_read_b64_tr_b16 v[146:147], v230 offset:20480\n\t" \
  "v_mfma_f32_16x16x32_bf16 v[24:27], v[108:111], v[84:87], v[24:27]\n\t" \
  "ds_read_b64_tr_b16 v[148:149], v231 offset:16384\n\t" \
  "v_mfma_f32_16x16x32_bf16 v[40:43], v[108:111], v[88:91], v[40:43]\n\t" \
  "ds_read_b64_tr_b16 v[150:151], v231 offset:20480\n\t" \
  "v_mfma_f32_16x16x32_bf16 v[56:59], v[108:111], v[92:95], v[56:59]\n\t" \
  "v_mfma_f32_16x16x32_bf16 v[72:75], v[108:111], v[96:99], v[72:75]\n\t" \
  "v_mfma_f32_16x16x32_bf16 v[12:15], v[112:115], v[80:83], v[12:15]\n\t" \
  "v_mfma_f32_16x16x32_bf16 v[28:31], v[112:115], v[84:87], v[28:31]\n\t" \
  "v_mfma_f32_16x16x32_bf16 v[44:47], v[112:115], v[88:91], v[44:47]\n\t" \
  "v_mfma_f32_16x16x32_bf16 v[60:63], v[112:115], v[92:95], v[60:63]\n\t" \
  "v_mfma_f32_16x16x32_bf16 v[76:79], v[112:115], v[96:99], v[76:79]\n\t" \
  "s_waitcnt lgkmcnt(0)\n\t" \
  "s_barrier\n\t" \
  "v_mfma_f32_16x16x32_bf16 v[0:3], v[136:139], v[116:119], v[0:3]\n\t" \
  "ds_read_b64_tr_b16 v[100:101], v228 offset:32768\n\t" \
  "v_mfma_f32_16x16x32_bf16 v[16:19], v[136:139], v[120:123], v[16:19]\n\t" \
  "ds_read_b64_tr_b16 v[102:103], v228 offset:36864\n\t" \
  "v_mfma_f32_16x16x32_bf16 v[32:35], v[136:139], v[124:127], v[32:35]\n\t" \
  "ds_read_b128 v[80:83], v227 offset:40960\n\t" \
  "v_mfma_f32_16x16x32_bf16 v[48:51], v[136:139], v[128:131], v[48:51]\n\t" \
  "ds_read_b128 v[84:87], v227 offset:43008\n\t" \
  "v_mfma_f32_16x16x32_bf16 v[64:67], v[136:139], v[132:135], v[64:67]\n\t" \
  "ds_read_b128 v[88:91], v227 offset:45056\n\t" \
  "v_mfma_f32_16x16x32_bf16 v[4:7], v[140:143], v[116:119], v[4:7]\n\t" \
  "ds_read_b128 v[92:95], v227 offset:47104\n\t" \
  "v_mfma_f32_16x16x32_bf16 v[20:23], v[140:143], v[120:123], v[20:23]\n\t" \
  "ds_read_b128 v[96:99], v227 offset:51200\n\t" \
  "v_mfma_f32_16x16x32_bf16 v[36:39], v[140:143], v[124:127], v[36:39]\n\t" \
  "ds_read_b64_tr_b16 v[104:105], v229 offset:32768\n\t" \
  "v_mfma_f32_16x16x32_bf16 v[52:55], v[140:143], v[128:131], v[52:55]\n\t" \
  "ds_read_b64_tr_b16 v[106:107], v229 offset:36864\n\t" \
  "v_mfma_f32_16x16x32_bf16 v[68:71], v[140:143], v[132:135], v[68:71]\n\t" \
  "ds_read_b64_tr_b16 v[108:109], v230 offset:32768\n\t" \
  "v_mfma_f32_16x16x32_bf16 v[8:11], v[144:147], v[116:119], v[8:11]\n\t" \
  "ds_read_b64_tr_b16 v[110:111], v230 offset:36864\n\t" \
  "v_mfma_f32_16x16x32_bf16 v[24:27], v[144:147], v[120:123], v[24:27]\n\t" \
  "ds_read_b64_tr_b16 v[112:113], v231 offset:32768\n\t" \
  "v_mfma_f32_16x16x32_bf16 v[40:43], v[144:147], v[124:127], v[40:43]\n\t" \
  "ds_read_b64_tr_b16 v[114:115], v231 offset:36864\n\t" \
  "v_mfma_f32_16x16x32_bf16 v[56:59], v[144:147], v[128:131], v[56:59]\n\t" \
  "v_mfma_f32_16x16x32_bf16 v[72:75], v[144:147], v[132:135], v[72:75]\n\t" \
  "v_mfma_f32_16x16x32_bf16 v[12:15], v[148:151], v[116:119], v[12:15]\n\t" \
  "v_mfma_f32_16x16x32_bf16 v[28:31], v[148:151], v[120:123], v[28:31]\n\t" \
  "v_mfma_f32_16x16x32_bf16 v[44:47], v[148:151], v[124:127], v[44:47]\n\t" \
  "v_mfma_f32_16x16x32_bf16 v[60:63], v[148:151], v[128:131], v[60:63]\n\t" \
  "v_mfma_f32_16x16x32_bf16 v[76:79], v[148:151], v[132:135], v[76:79]\n\t" \
  "22:\n\t" \
  "s_waitcnt lgkmcnt(0)\n\t" \
  "s_barrier\n\t" \
  "v_mfma_f32_16x16x32_bf16 v[0:3], v[100:103], v[80:83], v[0:3]\n\t" \
  "ds_read_b64_tr_b16 v[136:137], v232 offset:0\n\t" \
  "v_mfma_f32_16x16x32_bf16 v[16:19], v[100:103], v[84:87], v[16:19]\n\t" \
  "ds_read_b64_tr_b16 v[138:139], v232 offset:4096\n\t" \
  "v_mfma_f32_16x16x32_bf16 v[32:35], v[100:103], v[88:91], v[32:35]\n\t" \
  "ds_read_b128 v[116:119], v227 offset:0\n\t" \
  "v_mfma_f32_16x16x32_bf16 v[48:51], v[100:103], v[92:95], v[48:51]\n\t" \
  "ds_read_b128 v[120:123], v227 offset:2048\n\t" \
  "v_mfma_f32_16x16x32_bf16 v[64:67], v[100:103], v[96:99], v[64:67]\n\t" \
  "ds_read_b128 v[124:127], v227 offset:4096\n\t" \
  "v_mfma_f32_16x16x32_bf16 v[4:7], v[104:107], v[80:83], v[4:7]\n\t" \
  "ds_read_b128 v[128:131], v227 offset:6144\n\t" \
  "v_mfma_f32_16x16x32_bf16 v[20:23], v[104:107], v[84:87], v[20:23]\n\t" \
  "ds_read_b128 v[132:135], v227 offset:10240\n\t" \
  "v_mfma_f32_16x16x32_bf16 v[36:39], v[104:107], v[88:91], v[36:39]\n\t" \
  "ds_read_b64_tr_b16 v[140:141], v233 offset:0\n\t" \
  "v_mfma_f32_16x16x32_bf16 v[52:55], v[104:107], v[92:95], v[52:55]\n\t" \
  "ds_read_b64_tr_b16 v[142:143], v233 offset:4096\n\t" \
  "v_mfma_f32_16x16x32_bf16 v[68:71], v[104:107], v[96:99], v[68:71]\n\t" \
  "ds_read_b64_tr_b16 v[144:145], v234 offset:0\n\t" \
  "v_mfma_f32_16x16x32_bf16 v[8:11], v[108:111], v[80:83], v[8:11]\n\t" \
  "ds_read_b64_tr_b16 v[146:147], v234 offset:4096\n\t" \
  "v_mfma_f32_16x16x32_bf16 v[24:27], v[108:111], v[84:87], v[24:27]\n\t" \
  "ds_read_b64_tr_b16 v[148:149], v235 offset:0\n\t" \
  "v_mfma_f32_16x16x32_bf16 v[40:43], v[108:111], v[88:91], v[40:43]\n\t" \
  "ds_read_b64_tr_b16 v[150:151], v235 offset:4096\n\t" \
  "v_mfma_f32_16x16x32_bf16 v[56:59], v[108:111], v[92:95], v[56:59]\n\t" \
  "v_mfma_f32_16x16x32_bf16 v[72:75], v[108:111], v[96:99], v[72:75]\n\t" \
  "v_mfma_f32_16x16x32_bf16 v[12:15], v[112:115], v[80:83], v[12:15]\n\t" \
  "v_mfma_f32_16x16x32_bf16 v[28:31], v[112:115], v[84:87], v[28:31]\n\t" \
  "v_mfma_f32_16x16x32_bf16 v[44:47], v[112:115], v[88:91], v[44:47]\n\t" \
  "v_mfma_f32_16x16x32_bf16 v[60:63], v[112:115], v[92:95], v[60:63]\n\t" \
  "v_mfma_f32_16x16x32_bf16 v[76:79], v[112:115], v[96:99], v[76:79]\n\t" \
  "s_waitcnt lgkmcnt(0)\n\t" \
  "s_barrier\n\t" \
  "v_mfma_f32_16x16x32_bf16 v[0:3], v[136:139], v[116:119], v[0:3]\n\t" \
  "ds_read_b64_tr_b16 v[100:101], v232 offset:16384\n\t" \
  "v_mfma_f32_16x16x32_bf16 v[16:19], v[136:139], v[120:123], v[16:19]\n\t" \
  "ds_read_b64_tr_b16 v[102:103], v232 offset:20480\n\t" \
  "v_mfma_f32_16x16x32_bf16 v[32:35], v[136:139], v[124:127], v[32:35]\n\t" \
  "ds_read_b128 v[80:83], v227 offset:20480\n\t" \
  "v_mfma_f32_16x16x32_bf16 v[48:51], v[136:139], v[128:131], v[48:51]\n\t" \
  "ds_read_b128 v[84:87], v227 offset:22528\n\t" \
  "v_mfma_f32_16x16x32_bf16 v[64:67], v[136:139], v[132:135], v[64:67]\n\t" \
  "ds_read_b128 v[88:91], v227 offset:24576\n\t" \
  "v_mfma_f32_16x16x32_bf16 v[4:7], v[140:143], v[116:119], v[4:7]\n\t" \
  "ds_read_b128 v[92:95], v227 offset:26624\n\t" \
  "v_mfma_f32_16x16x32_bf16 v[20:23], v[140:143], v[120:123], v[20:23]\n\t" \
  "ds_read_b128 v[96:99], v227 offset:30720\n\t" \
  "v_mfma_f32_16x16x32_bf16 v[36:39], v[140:143], v[124:127], v[36:39]\n\t" \
  "ds_read_b64_tr_b16 v[104:105], v233 offset:16384\n\t" \
  "v_mfma_f32_16x16x32_bf16 v[52:55], v[140:143], v[128:131], v[52:55]\n\t" \
  "ds_read_b64_tr_b16 v[106:107], v233 offset:20480\n\t" \
  "v_mfma_f32_16x16x32_bf16 v[68:71], v[140:143], v[132:135], v[68:71]\n\t" \
  "ds_read_b64_tr_b16 v[108:109], v234 offset:16384\n\t" \
  "v_mfma_f32_16x16x32_bf16 v[8:11], v[144:147], v[116:119], v[8:11]\n\t" \
  "ds_read_b64_tr_b16 v[110:111], v234 offset:20480\n\t" \
  "v_mfma_f32_16x16x32_bf16 v[24:27], v[144:147], v[120:123], v[24:27]\n\t" \
  "ds_read_b64_tr_b16 v[112:113], v235 offset:16384\n\t" \
  "v_mfma_f32_16x16x32_bf16 v[40:43], v[144:147], v[124:127], v[40:43]\n\t" \
  "ds_read_b64_tr_b16 v[114:115], v235 offset:20480\n\t" \
  "v_mfma_f32_16x16x32_bf16 v[56:59], v[144:147], v[128:131], v[56:59]\n\t" \
  "v_mfma_f32_16x16x32_bf16 v[72:75], v[144:147], v[132:135], v[72:75]\n\t" \
  "v_mfma_f32_16x16x32_bf16 v[12:15], v[148:151], v[116:119], v[12:15]\n\t" \
  "v_mfma_f32_16x16x32_bf16 v[28:31], v[148:151], v[120:123], v[28:31]\n\t" \
  "v_mfma_f32_16x16x32_bf16 v[44:47], v[148:151], v[124:127], v[44:47]\n\t" \
  "v_mfma_f32_16x16x32_bf16 v[60:63], v[148:151], v[128:131], v[60:63]\n\t" \
  "v_mfma_f32_16x16x32_bf16 v[76:79], v[148:151], v[132:135], v[76:79]\n\t" \
  "s_waitcnt lgkmcnt(0)\n\t" \
  "s_barrier\n\t" \
  "v_mfma_f32_16x16x32_bf16 v[0:3], v[100:103], v[80:83], v[0:3]\n\t" \
  "ds_read_b64_tr_b16 v[136:137], v232 offset:32768\n\t" \
  "v_mfma_f32_16x16x32_bf16 v[16:19], v[100:103], v[84:87], v[16:19]\n\t" \
  "ds_read_b64_tr_b16 v[138:139], v232 offset:36864\n\t" \
  "v_mfma_f32_16x16x32_bf16 v[32:35], v[100:103], v[88:91], v[32:35]\n\t" \
  "ds_read_b128 v[116:119], v227 offset:40960\n\t" \
  "v_mfma_f32_16x16x32_bf16 v[48:51], v[100:103], v[92:95], v[48:51]\n\t" \
  "ds_read_b128 v[120:123], v227 offset:43008\n\t" \
  "v_mfma_f32_16x16x32_bf16 v[64:67], v[100:103], v[96:99], v[64:67]\n\t" \
  "ds_read_b128 v[124:127], v227 offset:45056\n\t" \
  "v_mfma_f32_16x16x32_bf16 v[4:7], v[104:107], v[80:83], v[4:7]\n\t" \
  "ds_read_b128 v[128:131], v227 offset:47104\n\t" \
  "v_mfma_f32_16x16x32_bf16 v[20:23], v[104:107], v[84:87], v[20:23]\n\t" \
  "ds_read_b128 v[132:135], v227 offset:51200\n\t" \
  "v_mfma_f32_16x16x32_bf16 v[36:39], v[104:107], v[88:91], v[36:39]\n\t" \
  "ds_read_b64_tr_b16 v[140:141], v233 offset:32768\n\t" \
  "v_mfma_f32_16x16x32_bf16 v[52:55], v[104:107], v[92:95], v[52:55]\n\t" \
  "ds_read_b64_tr_b16 v[142:143], v233 offset:36864\n\t" \
  "v_mfma_f32_16x16x32_bf16 v[68:71], v[104:107], v[96:99], v[68:71]\n\t" \
  "ds_read_b64_tr_b16 v[144:145], v234 offset:32768\n\t" \
  "v_mfma_f32_16x16x32_bf16 v[8:11], v[108:111], v[80:83], v[8:11]\n\t" \
  "ds_read_b64_tr_b16 v[146:147], v234 offset:36864\n\t" \
  "v_mfma_f32_16x16x32_bf16 v[24:27], v[108:111], v[84:87], v[24:27]\n\t" \
  "ds_read_b64_tr_b16 v[148:149], v235 offset:32768\n\t" \
  "v_mfma_f32_16x16x32_bf16 v[40:43], v[108:111], v[88:91], v[40:43]\n\t" \
  "ds_read_b64_tr_b16 v[150:151], v235 offset:36864\n\t" \
  "v_mfma_f32_16x16x32_bf16 v[56:59], v[108:111], v[92:95], v[56:59]\n\t" \
  "v_mfma_f32_16x16x32_bf16 v[72:75], v[108:111], v[96:99], v[72:75]\n\t" \
  "v_mfma_f32_16x16x32_bf16 v[12:15], v[112:115], v[80:83], v[12:15]\n\t" \
  "v_mfma_f32_16x16x32_bf16 v[28:31], v[112:115], v[84:87], v[28:31]\n\t" \
  "v_mfma_f32_16x16x32_bf16 v[44:47], v[112:115], v[88:91], v[44:47]\n\t" \
  "v_mfma_f32_16x16x32_bf16 v[60:63], v[112:115], v[92:95], v[60:63]\n\t" \
  "v_mfma_f32_16x16x32_bf16 v[76:79], v[112:115], v[96:99], v[76:79]\n\t" \
  "s_waitcnt lgkmcnt(0)\n\t" \
  "s_barrier\n\t" \
  "v_mfma_f32_16x16x32_bf16 v[0:3], v[136:139], v[116:119], v[0:3]\n\t" \
  "ds_read_b64_tr_b16 v[100:101], v228 offset:0\n\t" \
  "v_mfma_f32_16x16x32_bf16 v[16:19], v[136:139], v[120:123], v[16:19]\n\t" \
  "ds_read_b64_tr_b16 v[102:103], v228 offset:4096\n\t" \
  "v_mfma_f32_16x16x32_bf16 v[32:35], v[136:139], v[124:127], v[32:35]\n\t" \
  "ds_read_b128 v[80:83], v227 offset:0\n\t" \
  "v_mfma_f32_16x16x32_bf16 v[48:51], v[136:139], v[128:131], v[48:51]\n\t" \
  "ds_read_b128 v[84:87], v227 offset:2048\n\t" \
  "v_mfma_f32_16x16x32_bf16 v[64:67], v[136:139], v[132:135], v[64:67]\n\t" \
  "ds_read_b128 v[88:91], v227 offset:4096\n\t" \
  "v_mfma_f32_16x16x32_bf16 v[4:7], v[140:143], v[116:119], v[4:7]\n\t" \
  "ds_read_b128 v[92:95], v227 offset:6144\n\t" \
  "v_mfma_f32_16x16x32_bf16 v[20:23], v[140:143], v[120:123], v[20:23]\n\t" \
  "ds_read_b128 v[96:99], v227 offset:10240\n\t" \
  "v_mfma_f32_16x16x32_bf16 v[36:39], v[140:143], v[124:127], v[36:39]\n\t" \
  "ds_read_b64_tr_b16 v[104:105], v229 offset:0\n\t" \
  "v_mfma_f32_16x16x32_bf16 v[52:55], v[140:143], v[128:131], v[52:55]\n\t" \
  "ds_read_b64_tr_b16 v[106:107], v229 offset:4096\n\t" \
  "v_mfma_f32_16x16x32_bf16 v[68:71], v[140:143], v[132:135], v[68:71]\n\t" \
  "ds_read_b64_tr_b16 v[108:109], v230 offset:0\n\t" \
  "v_mfma_f32_16x16x32_bf16 v[8:11], v[144:147], v[116:119], v[8:11]\n\t" \
  "ds_read_b64_tr_b16 v[110:111], v230 offset:4096\n\t" \
  "v_mfma_f32_16x16x32_bf16 v[24:27], v[144:147], v[120:123], v[24:27]\n\t" \
  "ds_read_b64_tr_b16 v[112:113], v231 offset:0\n\t" \
  "v_mfma_f32_16x16x32_bf16 v[40:43], v[144:147], v[124:127], v[40:43]\n\t" \
  "ds_read_b64_tr_b16 v[114:115], v231 offset:4096\n\t" \
  "v_mfma_f32_16x16x32_bf16 v[56:59], v[144:147], v[128:131], v[56:59]\n\t" \
  "v_mfma_f32_16x16x32_bf16 v[72:75], v[144:147], v[132:135], v[72:75]\n\t" \
  "v_mfma_f32_16x16x32_bf16 v[12:15], v[148:151], v[116:119], v[12:15]\n\t" \
  "v_mfma_f32_16x16x32_bf16 v[28:31], v[148:151], v[120:123], v[28:31]\n\t" \
  "v_mfma_f32_16x16x32_bf16 v[44:47], v[148:151], v[124:127], v[44:47]\n\t" \
  "v_mfma_f32_16x16x32_bf16 v[60:63], v[148:151], v[128:131], v[60:63]\n\t" \
  "s_sub_u32 s52, s52, 1\n\t" \
  "s_cmp_lg_u32 s52, 0\n\t" \
  "v_mfma_f32_16x16x32_bf16 v[76:79], v[148:151], v[132:135], v[76:79]\n\t" \
  "s_cbranch_scc1 20b\n\t" \
  "s_waitcnt lgkmcnt(0)\n\t" \
  "s_barrier\n\t" \
  "v_mfma_f32_16x16x32_bf16 v[0:3], v[100:103], v[80:83], v[0:3]\n\t" \
  "ds_read_b64_tr_b16 v[136:137], v228 offset:16384\n\t" \
  "v_mfma_f32_16x16x32_bf16 v[16:19], v[100:103], v[84:87], v[16:19]\n\t" \
  "ds_read_b64_tr_b16 v[138:139], v228 offset:20480\n\t" \
  "v_mfma_f32_16x16x32_bf16 v[32:35], v[100:103], v[88:91], v[32:35]\n\t" \
  "ds_read_b128 v[116:119], v227 offset:20480\n\t" \
  "v_mfma_f32_16x16x32_bf16 v[48:51], v[100:103], v[92:95], v[48:51]\n\t" \
  "ds_read_b128 v[120:123], v227 offset:22528\n\t" \
  "v_mfma_f32_16x16x32_bf16 v[64:67], v[100:103], v[96:99], v[64:67]\n\t" \
  "ds_read_b128 v[124:127], v227 offset:24576\n\t" \
  "v_mfma_f32_16x16x32_bf16 v[4:7], v[104:107], v[80:83], v[4:7]\n\t" \
  "ds_read_b128 v[128:131], v227 offset:26624\n\t" \
  "v_mfma_f32_16x16x32_bf16 v[20:23], v[104:107], v[84:87], v[20:23]\n\t" \
  "ds_read_b128 v[132:135], v227 offset:30720\n\t" \
  "v_mfma_f32_16x16x32_bf16 v[36:39], v[104:107], v[88:91], v[36:39]\n\t" \
  "ds_read_b64_tr_b16 v[140:141], v229 offset:16384\n\t" \
  "v_mfma_f32_16x16x32_bf16 v[52:55], v[104:107], v[92:95], v[52:55]\n\t" \
  "ds_read_b64_tr_b16 v[142:143], v229 offset:20480\n\t" \
  "v_mfma_f32_16x16x32_bf16 v[68:71], v[104:107], v[96:99], v[68:71]\n\t" \
  "ds_read_b64_tr_b16 v[144:145], v230 offset:16384\n\t" \
  "v_mfma_f32_16x16x32_bf16 v[8:11], v[108:111], v[80:83], v[8:11]\n\t" \
  "ds_read_b64_tr_b16 v[146:147], v230 offset:20480\n\t" \
  "v_mfma_f32_16x16x32_bf16 v[24:27], v[108:111], v[84:87], v[24:27]\n\t" \
  "ds_read_b64_tr_b16 v[148:149], v231 offset:16384\n\t" \
  "v_mfma_f32_16x16x32_bf16 v[40:43], v[108:111], v[88:91], v[40:43]\n\t" \
  "ds_read_b64_tr_b16 v[150:151], v231 offset:20480\n\t" \
  "v_mfma_f32_16x16x32_bf16 v[56:59], v[108:111], v[92:95], v[56:59]\n\t" \
  "v_mfma_f32_16x16x32_bf16 v[72:75], v[108:111], v[96:99], v[72:75]\n\t" \
  "v_mfma_f32_16x16x32_bf16 v[12:15], v[112:115], v[80:83], v[12:15]\n\t" \
  "v_mfma_f32_16x16x32_bf16 v[28:31], v[112:115], v[84:87], v[28:31]\n\t" \
  "v_mfma_f32_16x16x32_bf16 v[44:47], v[112:115], v[88:91], v[44:47]\n\t" \
  "v_mfma_f32_16x16x32_bf16 v[60:63], v[112:115], v[92:95], v[60:63]\n\t" \
  "v_mfma_f32_16x16x32_bf16 v[76:79], v[112:115], v[96:99], v[76:79]\n\t" \
  "s_waitcnt lgkmcnt(0)\n\t" \
  "s_barrier\n\t" \
  "v_mfma_f32_16x16x32_bf16 v[0:3], v[136:139], v[116:119], v[0:3]\n\t" \
  "ds_read_b64_tr_b16 v[100:101], v228 offset:32768\n\t" \
  "s_mul_i32 s52, s72, 10240\n\t" \
  "s_add_u32 s52, s52, s73\n\t" \
  "v_mfma_f32_16x16x32_bf16 v[16:19], v[136:139], v[120:123], v[16:19]\n\t" \
  "ds_read_b64_tr_b16 v[102:103], v228 offset:36864\n\t" \
  "s_add_u32 s52, s52, 65536\n\t" \
  "v_mfma_f32_16x16x32_bf16 v[32:35], v[136:139], v[124:127], v[32:35]\n\t" \
  "ds_read_b128 v[80:83], v227 offset:40960\n\t" \
  "v_add_u32 v152, s52, v243\n\t" \
  "v_mfma_f32_16x16x32_bf16 v[48:51], v[136:139], v[128:131], v[48:51]\n\t" \
  "ds_read_b128 v[84:87], v227 offset:43008\n\t" \
  "s_xor_b32 s52, s72, 1\n\t" \
  "v_mfma_f32_16x16x32_bf16 v[64:67], v[136:139], v[132:135], v[64:67]\n\t" \
  "ds_read_b128 v[88:91], v227 offset:45056\n\t" \
  "s_mul_i32 s52, s52, 10240\n\t" \
  "v_mfma_f32_16x16x32_bf16 v[4:7], v[140:143], v[116:119], v[4:7]\n\t" \
  "ds_read_b128 v[92:95], v227 offset:47104\n\t" \
  "s_add_u32 s52, s52, s73\n\t" \
  "v_mfma_f32_16x16x32_bf16 v[20:23], v[140:143], v[120:123], v[20:23]\n\t" \
  "ds_read_b128 v[96:99], v227 offset:51200\n\t" \
  "s_add_u32 s52, s52, 65536\n\t" \
  "v_mfma_f32_16x16x32_bf16 v[36:39], v[140:143], v[124:127], v[36:39]\n\t" \
  "ds_read_b64_tr_b16 v[104:105], v229 offset:32768\n\t" \
  "v_add_u32 v153, s52, v243\n\t" \
  "v_mfma_f32_16x16x32_bf16 v[52:55], v[140:143], v[128:131], v[52:55]\n\t" \
  "ds_read_b64_tr_b16 v[106:107], v229 offset:36864\n\t" \
  "v_mov_b32 v154, v243\n\t" \
  "v_mfma_f32_16x16x32_bf16 v[68:71], v[140:143], v[132:135], v[68:71]\n\t" \
  "ds_read_b64_tr_b16 v[108:109], v230 offset:32768\n\t" \
  "s_xor_b32 s84, s54, 1\n\t" \
  "v_mfma_f32_16x16x32_bf16 v[8:11], v[144:147], v[116:119], v[8:11]\n\t" \
  "ds_read_b64_tr_b16 v[110:111], v230 offset:36864\n\t" \
  "s_lshl_b32 s52, s83, 8\n\t" \
  "v_mfma_f32_16x16x32_bf16 v[24:27], v[144:147], v[120:123], v[24:27]\n\t" \
  "ds_read_b64_tr_b16 v[112:113], v231 offset:32768\n\t" \
  "v_mov_b32 v155, 4608\n\t" \
  "v_mfma_f32_16x16x32_bf16 v[40:43], v[144:147], v[124:127], v[40:43]\n\t" \
  "ds_read_b64_tr_b16 v[114:115], v231 offset:36864\n\t" \
  "v_mov_b32 v156, 32\n\t" \
  "v_mfma_f32_16x16x32_bf16 v[56:59], v[144:147], v[128:131], v[56:59]\n\t" \
  "v_writelane_b32 v155, s52, 1\n\t" \
  "v_mfma_f32_16x16x32_bf16 v[72:75], v[144:147], v[132:135], v[72:75]\n\t" \
  "s_lshr_b32 s52, s74, 4\n\t" \
  "v_mfma_f32_16x16x32_bf16 v[12:15], v[148:151], v[116:119], v[12:15]\n\t" \
  "s_lshl_b32 s52, s52, 1\n\t" \
  "v_mfma_f32_16x16x32_bf16 v[28:31], v[148:151], v[120:123], v[28:31]\n\t" \
  "s_or_b32 s52, s52, s84\n\t" \
  "v_mfma_f32_16x16x32_bf16 v[44:47], v[148:151], v[124:127], v[44:47]\n\t" \
  "s_lshl_b32 s52, s52, 8\n\t" \
  "v_mfma_f32_16x16x32_bf16 v[60:63], v[148:151], v[128:131], v[60:63]\n\t" \
  "v_writelane_b32 v155, s52, 2\n\t" \
  "v_mfma_f32_16x16x32_bf16 v[76:79], v[148:151], v[132:135], v[76:79]\n\t" \
  "s_waitcnt lgkmcnt(0)\n\t" \
  "s_barrier\n\t" \
  "v_mfma_f32_16x16x32_bf16 v[0:3], v[100:103], v[80:83], v[0:3]\n\t" \
  "ds_read_b64_tr_b16 v[136:137], v232 offset:0\n\t" \
  "s_add_u32 s52, s74, 8\n\t" \
  "s_add_u32 s52, s52, s91\n\t" \
  "v_mfma_f32_16x16x32_bf16 v[16:19], v[100:103], v[84:87], v[16:19]\n\t" \
  "ds_read_b64_tr_b16 v[138:139], v232 offset:4096\n\t" \
  "s_lshr_b32 s52, s52, 4\n\t" \
  "v_mfma_f32_16x16x32_bf16 v[32:35], v[100:103], v[88:91], v[32:35]\n\t" \
  "ds_read_b128 v[116:119], v227 offset:0\n\t" \
  "s_lshl_b32 s52, s52, 1\n\t" \
  "v_mfma_f32_16x16x32_bf16 v[48:51], v[100:103], v[92:95], v[48:51]\n\t" \
  "ds_read_b128 v[120:123], v227 offset:2048\n\t" \
  "s_or_b32 s52, s52, s84\n\t" \
  "v_mfma_f32_16x16x32_bf16 v[64:67], v[100:103], v[96:99], v[64:67]\n\t" \
  "ds_read_b128 v[124:127], v227 offset:4096\n\t" \
  "s_lshl_b32 s52, s52, 8\n\t" \
  "v_mfma_f32_16x16x32_bf16 v[4:7], v[104:107], v[80:83], v[4:7]\n\t" \
  "ds_read_b128 v[128:131], v227 offset:6144\n\t" \
  "v_writelane_b32 v155, s52, 3\n\t" \
  "v_mfma_f32_16x16x32_bf16 v[20:23], v[104:107], v[84:87], v[20:23]\n\t" \
  "ds_read_b128 v[132:135], v227 offset:10240\n\t" \
  "v_writelane_b32 v156, 14, 1\n\t" \
  "v_mfma_f32_16x16x32_bf16 v[36:39], v[104:107], v[88:91], v[36:39]\n\t" \
  "ds_read_b64_tr_b16 v[140:141], v233 offset:0\n\t" \
  "v_writelane_b32 v156, 14, 2\n\t" \
  "v_mfma_f32_16x16x32_bf16 v[52:55], v[104:107], v[92:95], v[52:55]\n\t" \
  "ds_read_b64_tr_b16 v[142:143], v233 offset:4096\n\t" \
  "v_writelane_b32 v156, 14, 3\n\t" \
  "v_mfma_f32_16x16x32_bf16 v[68:71], v[104:107], v[96:99], v[68:71]\n\t" \
  "ds_read_b64_tr_b16 v[144:145], v234 offset:0\n\t" \
  "v_bfe_u32 v169, v243, 4, 4\n\t" \
  "v_mfma_f32_16x16x32_bf16 v[8:11], v[108:111], v[80:83], v[8:11]\n\t" \
  "ds_read_b64_tr_b16 v[146:147], v234 offset:4096\n\t" \
  "v_bfe_u32 v170, v243, 8, 1\n\t" \
  "v_mfma_f32_16x16x32_bf16 v[24:27], v[108:111], v[84:87], v[24:27]\n\t" \
  "ds_read_b64_tr_b16 v[148:149], v235 offset:0\n\t" \
  "v_lshrrev_b32 v171, 9, v243\n\t" \
  "v_mfma_f32_16x16x32_bf16 v[40:43], v[108:111], v[88:91], v[40:43]\n\t" \
  "ds_read_b64_tr_b16 v[150:151], v235 offset:4096\n\t" \
  "v_lshlrev_b32 v170, 5, v170\n\t" \
  "v_mfma_f32_16x16x32_bf16 v[56:59], v[108:111], v[92:95], v[56:59]\n\t" \
  "v_lshl_or_b32 v170, v171, 4, v170\n\t" \
  "v_mfma_f32_16x16x32_bf16 v[72:75], v[108:111], v[96:99], v[72:75]\n\t" \
  "v_lshl_add_u32 v171, v169, 6, v170\n\t" \
  "v_mfma_f32_16x16x32_bf16 v[12:15], v[112:115], v[80:83], v[12:15]\n\t" \
  "v_lshl_add_u32 v172, v169, 11, v170\n\t" \
  "v_mfma_f32_16x16x32_bf16 v[28:31], v[112:115], v[84:87], v[28:31]\n\t" \
  "s_lshl_b32 s85, s89, 2\n\t" \
  "v_mfma_f32_16x16x32_bf16 v[44:47], v[112:115], v[88:91], v[44:47]\n\t" \
  "s_add_u32 s85, s85, s74\n\t" \
  "v_mfma_f32_16x16x32_bf16 v[60:63], v[112:115], v[92:95], v[60:63]\n\t" \
  "s_lshl_b32 s52, s54, 1\n\t" \
  "v_mfma_f32_16x16x32_bf16 v[76:79], v[112:115], v[96:99], v[76:79]\n\t" \
  "s_waitcnt lgkmcnt(0)\n\t" \
  "s_barrier\n\t" \
  "v_mfma_f32_16x16x32_bf16 v[0:3], v[136:139], v[116:119], v[0:3]\n\t" \
  "ds_read_b64_tr_b16 v[100:101], v232 offset:16384\n\t" \
  "s_add_u32 s85, s85, s52\n\t" \
  "s_add_u32 s86, s74, s89\n\t" \
  "v_mfma_f32_16x16x32_bf16 v[16:19], v[136:139], v[120:123], v[16:19]\n\t" \
  "ds_read_b64_tr_b16 v[102:103], v232 offset:20480\n\t" \
  "s_add_u32 s86, s86, 8\n\t" \
  "v_mfma_f32_16x16x32_bf16 v[32:35], v[136:139], v[124:127], v[32:35]\n\t" \
  "ds_read_b128 v[80:83], v227 offset:20480\n\t" \
  "s_lshl_b32 s87, s75, 2\n\t" \
  "v_mfma_f32_16x16x32_bf16 v[48:51], v[136:139], v[128:131], v[48:51]\n\t" \
  "ds_read_b128 v[84:87], v227 offset:22528\n\t" \
  "s_add_u32 s87, s87, s88\n\t" \
  "v_mfma_f32_16x16x32_bf16 v[64:67], v[136:139], v[132:135], v[64:67]\n\t" \
  "ds_read_b128 v[88:91], v227 offset:24576\n\t" \
  "s_cmp_eq_u32 s54, s89\n\t" \
  "s_cselect_b32 s92, s55, 0\n\t" \
  "v_mfma_f32_16x16x32_bf16 v[4:7], v[140:143], v[116:119], v[4:7]\n\t" \
  "ds_read_b128 v[92:95], v227 offset:26624\n\t" \
  "s_lshl_b32 s52, s87, 17\n\t" \
  "v_mfma_f32_16x16x32_bf16 v[20:23], v[140:143], v[120:123], v[20:23]\n\t" \
  "ds_read_b128 v[96:99], v227 offset:30720\n\t" \
  "s_add_u32 s52, s52, 9437184\n\t" \
  "v_mfma_f32_16x16x32_bf16 v[36:39], v[140:143], v[124:127], v[36:39]\n\t" \
  "ds_read_b64_tr_b16 v[104:105], v233 offset:16384\n\t" \
  "s_lshl_b32 s84, s85, 10\n\t" \
  "v_mfma_f32_16x16x32_bf16 v[52:55], v[140:143], v[128:131], v[52:55]\n\t" \
  "ds_read_b64_tr_b16 v[106:107], v233 offset:20480\n\t" \
  "s_add_u32 s84, s84, s52\n\t" \
  "v_mfma_f32_16x16x32_bf16 v[68:71], v[140:143], v[132:135], v[68:71]\n\t" \
  "ds_read_b64_tr_b16 v[108:109], v234 offset:16384\n\t" \
  "v_add_u32 v157, s84, v171\n\t" \
  "v_mfma_f32_16x16x32_bf16 v[8:11], v[144:147], v[116:119], v[8:11]\n\t" \
  "ds_read_b64_tr_b16 v[110:111], v234 offset:20480\n\t" \
  "v_add_u32 v158, 1024, v157\n\t" \
  "v_mfma_f32_16x16x32_bf16 v[24:27], v[144:147], v[120:123], v[24:27]\n\t" \
  "ds_read_b64_tr_b16 v[112:113], v235 offset:16384\n\t" \
  "s_lshl_b32 s84, s86, 10\n\t" \
  "v_mfma_f32_16x16x32_bf16 v[40:43], v[144:147], v[124:127], v[40:43]\n\t" \
  "ds_read_b64_tr_b16 v[114:115], v235 offset:20480\n\t" \
  "s_add_u32 s84, s84, s52\n\t" \
  "v_mfma_f32_16x16x32_bf16 v[56:59], v[144:147], v[128:131], v[56:59]\n\t" \
  "s_cmp_lg_u32 s92, 0\n\t" \
  "s_cselect_b32 s84, s84, 0x40000000\n\t" \
  "v_mfma_f32_16x16x32_bf16 v[72:75], v[144:147], v[132:135], v[72:75]\n\t" \
  "v_add_u32 v159, s84, v171\n\t" \
  "v_mfma_f32_16x16x32_bf16 v[12:15], v[148:151], v[116:119], v[12:15]\n\t" \
  "s_lshl_b32 s84, s85, 6\n\t" \
  "v_mfma_f32_16x16x32_bf16 v[28:31], v[148:151], v[120:123], v[28:31]\n\t" \
  "s_add_u32 s84, s84, 24576\n\t" \
  "v_mfma_f32_16x16x32_bf16 v[44:47], v[148:151], v[124:127], v[44:47]\n\t" \
  "v_lshl_add_u32 v160, v169, 2, s84\n\t" \
  "v_mfma_f32_16x16x32_bf16 v[60:63], v[148:151], v[128:131], v[60:63]\n\t" \
  "v_add_u32 v161, 64, v160\n\t" \
  "v_mfma_f32_16x16x32_bf16 v[76:79], v[148:151], v[132:135], v[76:79]\n\t" \
  "s_waitcnt lgkmcnt(0)\n\t" \
  "s_barrier\n\t" \
  "v_mfma_f32_16x16x32_bf16 v[0:3], v[100:103], v[80:83], v[0:3]\n\t" \
  "ds_read_b64_tr_b16 v[136:137], v232 offset:32768\n\t" \
  "s_lshl_b32 s84, s86, 6\n\t" \
  "s_add_u32 s84, s84, 24576\n\t" \
  "v_mfma_f32_16x16x32_bf16 v[16:19], v[100:103], v[84:87], v[16:19]\n\t" \
  "ds_read_b64_tr_b16 v[138:139], v232 offset:36864\n\t" \
  "s_cmp_lg_u32 s92, 0\n\t" \
  "s_cselect_b32 s84, s84, 0x40000000\n\t" \
  "v_mfma_f32_16x16x32_bf16 v[32:35], v[100:103], v[88:91], v[32:35]\n\t" \
  "ds_read_b128 v[116:119], v227 offset:40960\n\t" \
  "v_lshl_add_u32 v162, v169, 2, s84\n\t" \
  "v_mfma_f32_16x16x32_bf16 v[48:51], v[100:103], v[92:95], v[48:51]\n\t" \
  "ds_read_b128 v[120:123], v227 offset:43008\n\t" \
  "s_lshl_b32 s52, s87, 6\n\t" \
  "v_mfma_f32_16x16x32_bf16 v[64:67], v[100:103], v[96:99], v[64:67]\n\t" \
  "ds_read_b128 v[124:127], v227 offset:45056\n\t" \
  "s_lshl_b32 s84, s85, 15\n\t" \
  "v_mfma_f32_16x16x32_bf16 v[4:7], v[104:107], v[80:83], v[4:7]\n\t" \
  "ds_read_b128 v[128:131], v227 offset:47104\n\t" \
  "s_add_u32 s84, s84, s52\n\t" \
  "v_mfma_f32_16x16x32_bf16 v[20:23], v[104:107], v[84:87], v[20:23]\n\t" \
  "ds_read_b128 v[132:135], v227 offset:51200\n\t" \
  "v_add_u32 v163, s84, v172\n\t" \
  "v_mfma_f32_16x16x32_bf16 v[36:39], v[104:107], v[88:91], v[36:39]\n\t" \
  "ds_read_b64_tr_b16 v[140:141], v233 offset:32768\n\t" \
  "s_lshl_b32 s84, s86, 15\n\t" \
  "v_mfma_f32_16x16x32_bf16 v[52:55], v[104:107], v[92:95], v[52:55]\n\t" \
  "ds_read_b64_tr_b16 v[142:143], v233 offset:36864\n\t" \
  "s_add_u32 s84, s84, s52\n\t" \
  "v_mfma_f32_16x16x32_bf16 v[68:71], v[104:107], v[96:99], v[68:71]\n\t" \
  "ds_read_b64_tr_b16 v[144:145], v234 offset:32768\n\t" \
  "s_cmp_lg_u32 s92, 0\n\t" \
  "s_cselect_b32 s84, s84, 0x40000000\n\t" \
  "v_mfma_f32_16x16x32_bf16 v[8:11], v[108:111], v[80:83], v[8:11]\n\t" \
  "ds_read_b64_tr_b16 v[146:147], v234 offset:36864\n\t" \
  "v_add_u32 v168, s84, v172\n\t" \
  "v_mfma_f32_16x16x32_bf16 v[24:27], v[108:111], v[84:87], v[24:27]\n\t" \
  "ds_read_b64_tr_b16 v[148:149], v235 offset:32768\n\t" \
  "s_cmp_lg_u32 s92, 0\n\t" \
  "s_cselect_b32 s84, 2048, 0x40000000\n\t" \
  "v_mfma_f32_16x16x32_bf16 v[40:43], v[108:111], v[88:91], v[40:43]\n\t" \
  "ds_read_b64_tr_b16 v[150:151], v235 offset:36864\n\t" \
  "v_add_u32 v167, s84, v243\n\t" \
  "v_mfma_f32_16x16x32_bf16 v[56:59], v[108:111], v[92:95], v[56:59]\n\t" \
  "s_xor_b32 s84, s92, s55\n\t" \
  "v_mfma_f32_16x16x32_bf16 v[72:75], v[108:111], v[96:99], v[72:75]\n\t" \
  "s_cmp_lg_u32 s84, 0\n\t" \
  "s_cselect_b32 s84, 2048, 0x40000000\n\t" \
  "v_mfma_f32_16x16x32_bf16 v[12:15], v[112:115], v[80:83], v[12:15]\n\t" \
  "v_add_u32 v166, s84, v243\n\t" \
  "v_mfma_f32_16x16x32_bf16 v[28:31], v[112:115], v[84:87], v[28:31]\n\t" \
  "v_mov_b32 v164, 1\n\t" \
  "v_mfma_f32_16x16x32_bf16 v[44:47], v[112:115], v[88:91], v[44:47]\n\t" \
  "v_mov_b32 v165, 0x40000000\n\t" \
  "v_mfma_f32_16x16x32_bf16 v[60:63], v[112:115], v[92:95], v[60:63]\n\t" \
  "v_writelane_b32 v165, 0, 0\n\t" \
  "v_mfma_f32_16x16x32_bf16 v[76:79], v[112:115], v[96:99], v[76:79]\n\t" \
  "s_waitcnt lgkmcnt(0)\n\t" \
  "v_mfma_f32_16x16x32_bf16 v[8:11], v[144:147], v[116:119], v[8:11]\n\t" \
  "v_mfma_f32_16x16x32_bf16 v[24:27], v[144:147], v[120:123], v[24:27]\n\t" \
  "v_mfma_f32_16x16x32_bf16 v[40:43], v[144:147], v[124:127], v[40:43]\n\t" \
  "v_mfma_f32_16x16x32_bf16 v[56:59], v[144:147], v[128:131], v[56:59]\n\t" \
  "v_mfma_f32_16x16x32_bf16 v[72:75], v[144:147], v[132:135], v[72:75]\n\t" \
  "v_mfma_f32_16x16x32_bf16 v[12:15], v[148:151], v[116:119], v[12:15]\n\t" \
  "v_mfma_f32_16x16x32_bf16 v[28:31], v[148:151], v[120:123], v[28:31]\n\t" \
  "v_mfma_f32_16x16x32_bf16 v[44:47], v[148:151], v[124:127], v[44:47]\n\t" \
  "v_mfma_f32_16x16x32_bf16 v[60:63], v[148:151], v[128:131], v[60:63]\n\t" \
  "v_mfma_f32_16x16x32_bf16 v[76:79], v[148:151], v[132:135], v[76:79]\n\t" \
  "v_mfma_f32_16x16x32_bf16 v[0:3], v[136:139], v[116:119], v[0:3]\n\t" \
  "v_mfma_f32_16x16x32_bf16 v[16:19], v[136:139], v[120:123], v[16:19]\n\t" \
  "v_mfma_f32_16x16x32_bf16 v[32:35], v[136:139], v[124:127], v[32:35]\n\t" \
  "v_mfma_f32_16x16x32_bf16 v[48:51], v[136:139], v[128:131], v[48:51]\n\t" \
  "v_mfma_f32_16x16x32_bf16 v[64:67], v[136:139], v[132:135], v[64:67]\n\t" \
  "v_mfma_f32_16x16x32_bf16 v[4:7], v[140:143], v[116:119], v[4:7]\n\t" \
  "v_mfma_f32_16x16x32_bf16 v[20:23], v[140:143], v[120:123], v[20:23]\n\t" \
  "v_mfma_f32_16x16x32_bf16 v[36:39], v[140:143], v[124:127], v[36:39]\n\t" \
  "v_mfma_f32_16x16x32_bf16 v[52:55], v[140:143], v[128:131], v[52:55]\n\t" \
  "v_mfma_f32_16x16x32_bf16 v[68:71], v[140:143], v[132:135], v[68:71]\n\t" \
  "49:\n\t" \
  ".p2align 3\n\t" \
  "buffer_load_dword v220, v155, s[44:47], 0 offen sc1\n\t" \
  "s_nop 4\n\t" \
  "s_cmp_eq_u32 s54, 0\n\t" \
  "s_cbranch_scc1 60f\n\t" \
  "s_cmp_lg_u32 s55, 0\n\t" \
  "ds_write_b128 v152, v[8:11] offset:0\n\t" \
  "ds_write_b128 v152, v[12:15] offset:1024\n\t" \
  "ds_write_b128 v152, v[24:27] offset:2048\n\t" \
  "ds_write_b128 v152, v[28:31] offset:3072\n\t" \
  "s_cbranch_scc0 51f\n\t" \
  "ds_write_b128 v152, v[72:75] offset:8192\n\t" \
  "ds_write_b128 v152, v[76:79] offset:9216\n\t" \
  "51:\n\t" \
  "s_waitcnt lgkmcnt(0)\n\t" \
  "s_barrier\n\t" \
  "s_nop 0\n\t" \
  "ds_read_b128 v[80:83], v153 offset:0\n\t" \
  "ds_read_b128 v[84:87], v153 offset:1024\n\t" \
  "ds_read_b128 v[88:91], v153 offset:2048\n\t" \
  "ds_read_b128 v[92:95], v153 offset:3072\n\t" \
  "ds_read_b128 v[96:99], v153 offset:8192\n\t" \
  "ds_read_b128 v[100:103], v153 offset:9216\n\t" \
  "ds_write_b128 v152, v[40:43] offset:4096\n\t" \
  "ds_write_b128 v152, v[44:47] offset:5120\n\t" \
  "ds_write_b128 v152, v[56:59] offset:6144\n\t" \
  "ds_write_b128 v152, v[60:63] offset:7168\n\t" \
  "s_waitcnt lgkmcnt(9)\n\t" \
  "v_pk_add_f32 v[80:81], v[80:81], v[0:1]\n\t" \
  "v_pk_add_f32 v[82:83], v[82:83], v[2:3]\n\t" \
  "s_waitcnt lgkmcnt(8)\n\t" \
  "v_pk_add_f32 v[84:85], v[84:85], v[4:5]\n\t" \
  "v_pk_add_f32 v[86:87], v[86:87], v[6:7]\n\t" \
  "v_cvt_pk_bf16_f32 v80, v80, v81\n\t" \
  "v_cvt_pk_bf16_f32 v81, v82, v83\n\t" \
  "v_cvt_pk_bf16_f32 v82, v84, v85\n\t" \
  "v_cvt_pk_bf16_f32 v83, v86, v87\n\t" \
  "buffer_store_dwordx4 v[80:83], v154, s[56:59], 0 offen offset:0 sc1\n\t" \
  "s_waitcnt lgkmcnt(7)\n\t" \
  "v_pk_add_f32 v[88:89], v[88:89], v[16:17]\n\t" \
  "v_pk_add_f32 v[90:91], v[90:91], v[18:19]\n\t" \
  "s_waitcnt lgkmcnt(6)\n\t" \
  "v_pk_add_f32 v[92:93], v[92:93], v[20:21]\n\t" \
  "v_pk_add_f32 v[94:95], v[94:95], v[22:23]\n\t" \
  "v_cvt_pk_bf16_f32 v88, v88, v89\n\t" \
  "v_cvt_pk_bf16_f32 v89, v90, v91\n\t" \
  "v_cvt_pk_bf16_f32 v90, v92, v93\n\t" \
  "v_cvt_pk_bf16_f32 v91, v94, v95\n\t" \
  "buffer_store_dwordx4 v[88:91], v154, s[56:59], 0 offen offset:1024 sc1\n\t" \
  "s_waitcnt lgkmcnt(5)\n\t" \
  "v_pk_add_f32 v[120:121], v[96:97], v[64:65]\n\t" \
  "v_pk_add_f32 v[122:123], v[98:99], v[66:67]\n\t" \
  "s_waitcnt lgkmcnt(4)\n\t" \
  "v_pk_add_f32 v[124:125], v[100:101], v[68:69]\n\t" \
  "v_pk_add_f32 v[126:127], v[102:103], v[70:71]\n\t" \
  "v_cvt_pk_bf16_f32 v96, v120, v121\n\t" \
  "v_cvt_pk_bf16_f32 v97, v122, v123\n\t" \
  "v_cvt_pk_bf16_f32 v98, v124, v125\n\t" \
  "v_cvt_pk_bf16_f32 v99, v126, v127\n\t" \
  "buffer_store_dwordx4 v[96:99], v166, s[56:59], 0 offen sc1\n\t" \
  "s_waitcnt lgkmcnt(0)\n\t" \
  "s_barrier\n\t" \
  "ds_read_b128 v[104:107], v153 offset:4096\n\t" \
  "ds_read_b128 v[108:111], v153 offset:5120\n\t" \
  "ds_read_b128 v[112:115], v153 offset:6144\n\t" \
  "ds_read_b128 v[116:119], v153 offset:7168\n\t" \
  "s_waitcnt lgkmcnt(3)\n\t" \
  "v_pk_add_f32 v[104:105], v[104:105], v[32:33]\n\t" \
  "v_pk_add_f32 v[106:107], v[106:107], v[34:35]\n\t" \
  "s_waitcnt lgkmcnt(2)\n\t" \
  "v_pk_add_f32 v[108:109], v[108:109], v[36:37]\n\t" \
  "v_pk_add_f32 v[110:111], v[110:111], v[38:39]\n\t" \
  "s_waitcnt lgkmcnt(1)\n\t" \
  "v_pk_add_f32 v[112:113], v[112:113], v[48:49]\n\t" \
  "v_pk_add_f32 v[114:115], v[114:115], v[50:51]\n\t" \
  "s_waitcnt lgkmcnt(0)\n\t" \
  "v_pk_add_f32 v[116:117], v[116:117], v[52:53]\n\t" \
  "v_pk_add_f32 v[118:119], v[118:119], v[54:55]\n\t" \
  "s_nop 0\n\t" \
  "s_branch 61f\n\t" \
  "60:\n\t" \
  "s_cmp_lg_u32 s55, 0\n\t" \
  "ds_write_b128 v152, v[40:43] offset:0\n\t" \
  "ds_write_b128 v152, v[44:47] offset:1024\n\t" \
  "ds_write_b128 v152, v[56:59] offset:2048\n\t" \
  "ds_write_b128 v152, v[60:63] offset:3072\n\t" \
  "s_cbranch_scc0 55f\n\t" \
  "ds_write_b128 v152, v[72:75] offset:8192\n\t" \
  "ds_write_b128 v152, v[76:79] offset:9216\n\t" \
  "55:\n\t" \
  "s_waitcnt lgkmcnt(0)\n\t" \
  "s_barrier\n\t" \
  "ds_read_b128 v[80:83], v153 offset:0\n\t" \
  "ds_read_b128 v[84:87], v153 offset:1024\n\t" \
  "ds_read_b128 v[88:91], v153 offset:2048\n\t" \
  "ds_read_b128 v[92:95], v153 offset:3072\n\t" \
  "ds_read_b128 v[96:99], v153 offset:8192\n\t" \
  "ds_read_b128 v[100:103], v153 offset:9216\n\t" \
  "ds_write_b128 v152, v[8:11] offset:4096\n\t" \
  "ds_write_b128 v152, v[12:15] offset:5120\n\t" \
  "ds_write_b128 v152, v[24:27] offset:6144\n\t" \
  "ds_write_b128 v152, v[28:31] offset:7168\n\t" \
  "s_waitcnt lgkmcnt(9)\n\t" \
  "v_pk_add_f32 v[80:81], v[80:81], v[32:33]\n\t" \
  "v_pk_add_f32 v[82:83], v[82:83], v[34:35]\n\t" \
  "s_waitcnt lgkmcnt(8)\n\t" \
  "v_pk_add_f32 v[84:85], v[84:85], v[36:37]\n\t" \
  "v_pk_add_f32 v[86:87], v[86:87], v[38:39]\n\t" \
  "v_cvt_pk_bf16_f32 v80, v80, v81\n\t" \
  "v_cvt_pk_bf16_f32 v81, v82, v83\n\t" \
  "v_cvt_pk_bf16_f32 v82, v84, v85\n\t" \
  "v_cvt_pk_bf16_f32 v83, v86, v87\n\t" \
  "buffer_store_dwordx4 v[80:83], v154, s[56:59], 0 offen offset:0 sc1\n\t" \
  "s_waitcnt lgkmcnt(7)\n\t" \
  "v_pk_add_f32 v[88:89], v[88:89], v[48:49]\n\t" \
  "v_pk_add_f32 v[90:91], v[90:91], v[50:51]\n\t" \
  "s_waitcnt lgkmcnt(6)\n\t" \
  "v_pk_add_f32 v[92:93], v[92:93], v[52:53]\n\t" \
  "v_pk_add_f32 v[94:95], v[94:95], v[54:55]\n\t" \
  "v_cvt_pk_bf16_f32 v88, v88, v89\n\t" \
  "v_cvt_pk_bf16_f32 v89, v90, v91\n\t" \
  "v_cvt_pk_bf16_f32 v90, v92, v93\n\t" \
  "v_cvt_pk_bf16_f32 v91, v94, v95\n\t" \
  "buffer_store_dwordx4 v[88:91], v154, s[56:59], 0 offen offset:1024 sc1\n\t" \
  "s_waitcnt lgkmcnt(5)\n\t" \
  "v_pk_add_f32 v[120:121], v[96:97], v[64:65]\n\t" \
  "v_pk_add_f32 v[122:123], v[98:99], v[66:67]\n\t" \
  "s_waitcnt lgkmcnt(4)\n\t" \
  "v_pk_add_f32 v[124:125], v[100:101], v[68:69]\n\t" \
  "v_pk_add_f32 v[126:127], v[102:103], v[70:71]\n\t" \
  "v_cvt_pk_bf16_f32 v96, v120, v121\n\t" \
  "v_cvt_pk_bf16_f32 v97, v122, v123\n\t" \
  "v_cvt_pk_bf16_f32 v98, v124, v125\n\t" \
  "v_cvt_pk_bf16_f32 v99, v126, v127\n\t" \
  "buffer_store_dwordx4 v[96:99], v166, s[56:59], 0 offen sc1\n\t" \
  "s_waitcnt lgkmcnt(0)\n\t" \
  "s_barrier\n\t" \
  "ds_read_b128 v[104:107], v153 offset:4096\n\t" \
  "ds_read_b128 v[108:111], v153 offset:5120\n\t" \
  "ds_read_b128 v[112:115], v153 offset:6144\n\t" \
  "ds_read_b128 v[116:119], v153 offset:7168\n\t" \
  "s_waitcnt lgkmcnt(3)\n\t" \
  "v_pk_add_f32 v[104:105], v[104:105], v[0:1]\n\t" \
  "v_pk_add_f32 v[106:107], v[106:107], v[2:3]\n\t" \
  "s_waitcnt lgkmcnt(2)\n\t" \
  "v_pk_add_f32 v[108:109], v[108:109], v[4:5]\n\t" \
  "v_pk_add_f32 v[110:111], v[110:111], v[6:7]\n\t" \
  "s_waitcnt lgkmcnt(1)\n\t" \
  "v_pk_add_f32 v[112:113], v[112:113], v[16:17]\n\t" \
  "v_pk_add_f32 v[114:115], v[114:115], v[18:19]\n\t" \
  "s_waitcnt lgkmcnt(0)\n\t" \
  "v_pk_add_f32 v[116:117], v[116:117], v[20:21]\n\t" \
  "v_pk_add_f32 v[118:119], v[118:119], v[22:23]\n\t" \
  "s_nop 1\n\t" \
  "61:\n\t" \
  "s_waitcnt vmcnt(0)\n\t" \
  "buffer_store_dword v164, v165, s[56:59], 0 offen offset:3072 sc1\n\t" \
  "v_cmp_lt_u32 vcc, v220, v156\n\t" \
  "s_mov_b32 s52, 0\n\t" \
  "s_nop 3\n\t" \
  "s_cbranch_vccz 53f\n\t" \
  "52:\n\t" \
  "s_sleep 32\n\t" \
  "buffer_load_dword v220, v155, s[44:47], 0 offen sc1\n\t" \
  "s_add_u32 s52, s52, 1\n\t" \
  "s_waitcnt vmcnt(0)\n\t" \
  "v_cmp_lt_u32 vcc, v220, v156\n\t" \
  "s_cmp_ge_u32 s52, 0x8000\n\t" \
  "s_nop 2\n\t" \
  "s_cbranch_scc1 53f\n\t" \
  "s_nop 1\n\t" \
  "s_cbranch_vccnz 52b\n\t" \
  "53:\n\t" \
  "buffer_load_dword v221, off, s[60:63], 0 offset:3072 sc1\n\t" \
  "buffer_load_dword v180, v160, s[64:67], 0 offen sc1\n\t" \
  "buffer_load_dwordx4 v[184:187], v157, s[64:67], 0 offen sc1\n\t" \
  "buffer_load_dword v181, v161, s[64:67], 0 offen sc1\n\t" \
  "buffer_load_dwordx4 v[188:191], v158, s[64:67], 0 offen sc1\n\t" \
  "buffer_load_dword v182, v162, s[64:67], 0 offen sc1\n\t" \
  "buffer_load_dwordx4 v[192:195], v159, s[64:67], 0 offen sc1\n\t" \
  "s_waitcnt vmcnt(6)\n\t" \
  "v_cmp_eq_u32 vcc, 0, v221\n\t" \
  "s_mov_b32 s52, 0\n\t" \
  "s_nop 3\n\t" \
  "s_cbranch_vccz 63f\n\t" \
  "62:\n\t" \
  "s_sleep 1\n\t" \
  "buffer_load_dword v221, off, s[60:63], 0 offset:3072 sc1\n\t" \
  "s_add_u32 s52, s52, 1\n\t" \
  "s_waitcnt vmcnt(0)\n\t" \
  "v_cmp_eq_u32 vcc, 0, v221\n\t" \
  "s_cmp_ge_u32 s52, 0x8000\n\t" \
  "s_nop 2\n\t" \
  "s_cbranch_scc1 63f\n\t" \
  "s_nop 1\n\t" \
  "s_cbranch_vccnz 62b\n\t" \
  "63:\n\t" \
  "buffer_load_dwordx4 v[128:131], v154, s[60:63], 0 offen offset:0 sc1\n\t" \
  "buffer_load_dwordx4 v[132:135], v154, s[60:63], 0 offen offset:1024 sc1\n\t" \
  "buffer_load_dwordx4 v[136:139], v167, s[60:63], 0 offen offset:0 sc1\n\t" \
  "s_waitcnt vmcnt(3)\n\t" \
  "v_lshlrev_b32_e64 v196, 16, v184\n\t" \
  "v_and_b32 v197, 0xffff0000, v184\n\t" \
  "v_lshlrev_b32_e64 v198, 16, v185\n\t" \
  "v_and_b32 v199, 0xffff0000, v185\n\t" \
  "v_lshlrev_b32_e64 v200, 16, v186\n\t" \
  "v_and_b32 v201, 0xffff0000, v186\n\t" \
  "v_lshlrev_b32_e64 v202, 16, v187\n\t" \
  "v_and_b32 v203, 0xffff0000, v187\n\t" \
  "v_mul_f32 v196, v180, v196\n\t" \
  "v_mul_f32 v197, v180, v197\n\t" \
  "v_mul_f32 v198, v180, v198\n\t" \
  "v_mul_f32 v199, v180, v199\n\t" \
  "v_mul_f32 v200, v180, v200\n\t" \
  "v_mul_f32 v201, v180, v201\n\t" \
  "v_mul_f32 v202, v180, v202\n\t" \
  "v_mul_f32 v203, v180, v203\n\t" \
  "v_sub_f32_e64 v180, 1.0, v180\n\t" \
  "v_cvt_pk_bf16_f32 v180, v180, v180\n\t" \
  "v_and_b32 v180, 0xffff0000, v180\n\t" \
  "v_lshlrev_b32_e64 v204, 16, v188\n\t" \
  "v_and_b32 v205, 0xffff0000, v188\n\t" \
  "v_lshlrev_b32_e64 v206, 16, v189\n\t" \
  "v_and_b32 v207, 0xffff0000, v189\n\t" \
  "v_lshlrev_b32_e64 v208, 16, v190\n\t" \
  "v_and_b32 v209, 0xffff0000, v190\n\t" \
  "v_lshlrev_b32_e64 v210, 16, v191\n\t" \
  "v_and_b32 v211, 0xffff0000, v191\n\t" \
  "v_mul_f32 v204, v181, v204\n\t" \
  "v_mul_f32 v205, v181, v205\n\t" \
  "v_mul_f32 v206, v181, v206\n\t" \
  "v_mul_f32 v207, v181, v207\n\t" \
  "v_mul_f32 v208, v181, v208\n\t" \
  "v_mul_f32 v209, v181, v209\n\t" \
  "v_mul_f32 v210, v181, v210\n\t" \
  "v_mul_f32 v211, v181, v211\n\t" \
  "v_sub_f32_e64 v181, 1.0, v181\n\t" \
  "v_cvt_pk_bf16_f32 v181, v181, v181\n\t" \
  "v_and_b32 v181, 0xffff0000, v181\n\t" \
  "v_lshlrev_b32_e64 v212, 16, v192\n\t" \
  "v_and_b32 v213, 0xffff0000, v192\n\t" \
  "v_lshlrev_b32_e64 v214, 16, v193\n\t" \
  "v_and_b32 v215, 0xffff0000, v193\n\t" \
  "v_lshlrev_b32_e64 v216, 16, v194\n\t" \
  "v_and_b32 v217, 0xffff0000, v194\n\t" \
  "v_lshlrev_b32_e64 v218, 16, v195\n\t" \
  "v_and_b32 v219, 0xffff0000, v195\n\t" \
  "v_mul_f32 v212, v182, v212\n\t" \
  "v_mul_f32 v213, v182, v213\n\t" \
  "v_mul_f32 v214, v182, v214\n\t" \
  "v_mul_f32 v215, v182, v215\n\t" \
  "v_mul_f32 v216, v182, v216\n\t" \
  "v_mul_f32 v217, v182, v217\n\t" \
  "v_mul_f32 v218, v182, v218\n\t" \
  "v_mul_f32 v219, v182, v219\n\t" \
  "v_sub_f32_e64 v182, 1.0, v182\n\t" \
  "v_cvt_pk_bf16_f32 v182, v182, v182\n\t" \
  "v_and_b32 v182, 0xffff0000, v182\n\t" \
  "s_mov_b32 s52, 32768\n\t" \
  "s_waitcnt vmcnt(2)\n\t" \
  "v_lshlrev_b32 v140, 16, v128\n\t" \
  "v_and_b32 v141, 0xffff0000, v128\n\t" \
  "v_lshlrev_b32_e64 v142, 16, v129\n\t" \
  "v_and_b32 v143, 0xffff0000, v129\n\t" \
  "v_lshlrev_b32_e64 v144, 16, v130\n\t" \
  "v_and_b32 v145, 0xffff0000, v130\n\t" \
  "v_lshlrev_b32_e64 v146, 16, v131\n\t" \
  "v_and_b32 v147, 0xffff0000, v131\n\t" \
  "v_pk_add_f32 v[140:141], v[140:141], v[104:105]\n\t" \
  "v_pk_add_f32 v[142:143], v[142:143], v[106:107]\n\t" \
  "v_pk_add_f32 v[144:145], v[144:145], v[108:109]\n\t" \
  "v_pk_add_f32 v[146:147], v[146:147], v[110:111]\n\t" \
  "s_nop 0\n\t" \
  "v_permlane16_swap_b32 v140, v144\n\t" \
  "v_permlane16_swap_b32 v141, v145\n\t" \
  "v_permlane16_swap_b32 v142, v146\n\t" \
  "v_permlane16_swap_b32 v143, v147\n\t" \
  "v_fmac_f32 v196, v180, v140\n\t" \
  "v_fmac_f32 v197, v180, v141\n\t" \
  "v_fmac_f32 v198, v180, v142\n\t" \
  "v_fmac_f32 v199, v180, v143\n\t" \
  "v_fmac_f32 v200, v180, v144\n\t" \
  "v_fmac_f32 v201, v180, v145\n\t" \
  "v_fmac_f32 v202, v180, v146\n\t" \
  "v_fmac_f32_e64 v203, v180, v147\n\t" \
  "v_cvt_pk_bf16_f32 v0, v196, v197\n\t" \
  "v_cvt_pk_bf16_f32 v1, v198, v199\n\t" \
  "v_cvt_pk_bf16_f32 v2, v200, v201\n\t" \
  "v_cvt_pk_bf16_f32 v3, v202, v203\n\t" \
  "buffer_store_dwordx4 v[0:3], v163, s[68:71], 0 offen\n\t" \
  "s_waitcnt vmcnt(2)\n\t" \
  "v_lshlrev_b32 v140, 16, v132\n\t" \
  "v_and_b32 v141, 0xffff0000, v132\n\t" \
  "v_lshlrev_b32_e64 v142, 16, v133\n\t" \
  "v_and_b32 v143, 0xffff0000, v133\n\t" \
  "v_lshlrev_b32_e64 v144, 16, v134\n\t" \
  "v_and_b32 v145, 0xffff0000, v134\n\t" \
  "v_lshlrev_b32_e64 v146, 16, v135\n\t" \
  "v_and_b32 v147, 0xffff0000, v135\n\t" \
  "v_pk_add_f32 v[140:141], v[140:141], v[112:113]\n\t" \
  "v_pk_add_f32 v[142:143], v[142:143], v[114:115]\n\t" \
  "v_pk_add_f32 v[144:145], v[144:145], v[116:117]\n\t" \
  "v_pk_add_f32 v[146:147], v[146:147], v[118:119]\n\t" \
  "s_nop 0\n\t" \
  "v_permlane16_swap_b32 v140, v144\n\t" \
  "v_permlane16_swap_b32 v141, v145\n\t" \
  "v_permlane16_swap_b32 v142, v146\n\t" \
  "v_permlane16_swap_b32 v143, v147\n\t" \
  "v_fmac_f32 v204, v181, v140\n\t" \
  "v_fmac_f32 v205, v181, v141\n\t" \
  "v_fmac_f32 v206, v181, v142\n\t" \
  "v_fmac_f32 v207, v181, v143\n\t" \
  "v_fmac_f32 v208, v181, v144\n\t" \
  "v_fmac_f32 v209, v181, v145\n\t" \
  "v_fmac_f32 v210, v181, v146\n\t" \
  "v_fmac_f32_e64 v211, v181, v147\n\t" \
  "v_cvt_pk_bf16_f32 v4, v204, v205\n\t" \
  "v_cvt_pk_bf16_f32 v5, v206, v207\n\t" \
  "v_cvt_pk_bf16_f32 v6, v208, v209\n\t" \
  "v_cvt_pk_bf16_f32 v7, v210, v211\n\t" \
  "buffer_store_dwordx4 v[4:7], v163, s[68:71], s52 offen\n\t" \
  "s_waitcnt vmcnt(2)\n\t" \
  "v_lshlrev_b32 v140, 16, v136\n\t" \
  "v_and_b32 v141, 0xffff0000, v136\n\t" \
  "v_lshlrev_b32_e64 v142, 16, v137\n\t" \
  "v_and_b32 v143, 0xffff0000, v137\n\t" \
  "v_lshlrev_b32_e64 v144, 16, v138\n\t" \
  "v_and_b32 v145, 0xffff0000, v138\n\t" \
  "v_lshlrev_b32_e64 v146, 16, v139\n\t" \
  "v_and_b32 v147, 0xffff0000, v139\n\t" \
  "v_pk_add_f32 v[140:141], v[140:141], v[120:121]\n\t" \
  "v_pk_add_f32 v[142:143], v[142:143], v[122:123]\n\t" \
  "v_pk_add_f32 v[144:145], v[144:145], v[124:125]\n\t" \
  "v_pk_add_f32 v[146:147], v[146:147], v[126:127]\n\t" \
  "s_nop 0\n\t" \
  "v_permlane16_swap_b32 v140, v144\n\t" \
  "v_permlane16_swap_b32 v141, v145\n\t" \
  "v_permlane16_swap_b32 v142, v146\n\t" \
  "v_permlane16_swap_b32 v143, v147\n\t" \
  "v_fmac_f32 v212, v182, v140\n\t" \
  "v_fmac_f32 v213, v182, v141\n\t" \
  "v_fmac_f32 v214, v182, v142\n\t" \
  "v_fmac_f32 v215, v182, v143\n\t" \
  "v_fmac_f32 v216, v182, v144\n\t" \
  "v_fmac_f32 v217, v182, v145\n\t" \
  "v_fmac_f32 v218, v182, v146\n\t" \
  "v_fmac_f32_e64 v219, v182, v147\n\t" \
  "v_cvt_pk_bf16_f32 v8, v212, v213\n\t" \
  "v_cvt_pk_bf16_f32 v9, v214, v215\n\t" \
  "v_cvt_pk_bf16_f32 v10, v216, v217\n\t" \
  "v_cvt_pk_bf16_f32 v11, v218, v219\n\t" \
  "buffer_store_dwordx4 v[8:11], v168, s[68:71], 0 offen\n\t"

#define FFT_P1 \
  ".p2align 3\n\t" \
  "s_nop 4\n\t" \
  "s_mov_b32 s52, 0\n\t" \
  "s_add_u32 m0, s51, 0\n\t" \
  "s_nop 0\n\t" \
  "buffer_load_dwordx4 v136, s[44:47], s52 offen offset:0 lds\n\t" \
  "buffer_load_dwordx4 v137, s[44:47], s52 offen offset:1024 lds\n\t" \
  "buffer_load_dwordx4 v138, s[44:47], s52 offen offset:2048 lds\n\t" \
  "buffer_load_dwordx4 v139, s[44:47], s52 offen offset:3072 lds\n\t" \
  "s_mov_b32 s52, 2097152\n\t" \
  "s_add_u32 m0, s51, 4096\n\t" \
  "s_nop 0\n\t" \
  "s_nop 0\n\t" \
  "buffer_load_dwordx4 v136, s[44:47], s52 offen offset:0 lds\n\t" \
  "buffer_load_dwordx4 v137, s[44:47], s52 offen offset:1024 lds\n\t" \
  "buffer_load_dwordx4 v138, s[44:47], s52 offen offset:2048 lds\n\t" \
  "buffer_load_dwordx4 v139, s[44:47], s52 offen offset:3072 lds\n\t" \
  "buffer_load_dwordx2 v[144:145], v148, s[68:71], 0 offen sc1\n\t" \
  "s_mov_b32 s52, 0\n\t" \
  "s_add_u32 m0, s51, 8192\n\t" \
  "s_nop 0\n\t" \
  "buffer_load_dwordx4 v140, s[44:47], s52 offen offset:0 lds\n\t" \
  "buffer_load_dwordx4 v141, s[44:47], s52 offen offset:1024 lds\n\t" \
  "buffer_load_dwordx4 v142, s[44:47], s52 offen offset:2048 lds\n\t" \
  "buffer_load_dwordx4 v143, s[44:47], s52 offen offset:3072 lds\n\t" \
  "s_mov_b32 s52, 2097152\n\t" \
  "s_add_u32 m0, s51, 12288\n\t" \
  "s_nop 0\n\t" \
  "s_nop 0\n\t" \
  "buffer_load_dwordx4 v140, s[44:47], s52 offen offset:0 lds\n\t" \
  "buffer_load_dwordx4 v141, s[44:47], s52 offen offset:1024 lds\n\t" \
  "buffer_load_dwordx4 v142, s[44:47], s52 offen offset:2048 lds\n\t" \
  "buffer_load_dwordx4 v143, s[44:47], s52 offen offset:3072 lds\n\t" \
  "s_mov_b32 s53, 0x3fcfee39\n\t" \
  "v_mov_b32_e64 v200, v226\n\t" \
  "v_fmamk_f32 v201, v224, 0x42000000, v226\n\t" \
  "v_cos_f32 v202, v200\n\t" \
  "v_cos_f32 v203, v201\n\t" \
  "v_sin_f32 v204, v200\n\t" \
  "v_sin_f32 v205, v201\n\t" \
  "v_mul_f32 v202, s53, v202\n\t" \
  "v_mul_f32 v203, s53, v203\n\t" \
  "v_mul_f32 v204, s53, v204\n\t" \
  "v_mul_f32 v205, s53, v205\n\t" \
  "v_cvt_pk_bf16_f32 v0, v202, v203\n\t" \
  "v_cvt_pk_bf16_f32 v8, v204, v205\n\t" \
  "v_fmamk_f32 v206, v224, 0x42800000, v226\n\t" \
  "v_fmamk_f32 v207, v224, 0x42c00000, v226\n\t" \
  "v_cos_f32 v208, v206\n\t" \
  "v_cos_f32 v209, v207\n\t" \
  "v_sin_f32 v210, v206\n\t" \
  "v_sin_f32 v211, v207\n\t" \
  "v_mul_f32 v208, s53, v208\n\t" \
  "v_mul_f32 v209, s53, v209\n\t" \
  "v_mul_f32 v210, s53, v210\n\t" \
  "v_mul_f32 v211, s53, v211\n\t" \
  "v_cvt_pk_bf16_f32 v1, v208, v209\n\t" \
  "v_cvt_pk_bf16_f32 v9, v210, v211\n\t" \
  "v_fmamk_f32 v200, v224, 0x43800000, v226\n\t" \
  "v_fmamk_f32 v201, v224, 0x43900000, v226\n\t" \
  "v_cos_f32 v202, v200\n\t" \
  "v_cos_f32 v203, v201\n\t" \
  "v_sin_f32 v204, v200\n\t" \
  "v_sin_f32 v205, v201\n\t" \
  "v_mul_f32 v202, s53, v202\n\t" \
  "v_mul_f32 v203, s53, v203\n\t" \
  "v_mul_f32 v204, s53, v204\n\t" \
  "v_mul_f32 v205, s53, v205\n\t" \
  "v_cvt_pk_bf16_f32 v2, v202, v203\n\t" \
  "v_cvt_pk_bf16_f32 v10, v204, v205\n\t" \
  "v_fmamk_f32 v206, v224, 0x43a00000, v226\n\t" \
  "v_fmamk_f32 v207, v224, 0x43b00000, v226\n\t" \
  "v_cos_f32 v208, v206\n\t" \
  "v_cos_f32 v209, v207\n\t" \
  "v_sin_f32 v210, v206\n\t" \
  "v_sin_f32 v211, v207\n\t" \
  "v_mul_f32 v208, s53, v208\n\t" \
  "v_mul_f32 v209, s53, v209\n\t" \
  "v_mul_f32 v210, s53, v210\n\t" \
  "v_mul_f32 v211, s53, v211\n\t" \
  "v_cvt_pk_bf16_f32 v3, v208, v209\n\t" \
  "v_cvt_pk_bf16_f32 v11, v210, v211\n\t" \
  "v_fmamk_f32 v200, v224, 0x44000000, v226\n\t" \
  "v_fmamk_f32 v201, v224, 0x44080000, v226\n\t" \
  "v_cos_f32 v202, v200\n\t" \
  "v_cos_f32 v203, v201\n\t" \
  "v_sin_f32 v204, v200\n\t" \
  "v_sin_f32 v205, v201\n\t" \
  "v_mul_f32 v202, s53, v202\n\t" \
  "v_mul_f32 v203, s53, v203\n\t" \
  "v_mul_f32 v204, s53, v204\n\t" \
  "v_mul_f32 v205, s53, v205\n\t" \
  "v_cvt_pk_bf16_f32 v4, v202, v203\n\t" \
  "v_cvt_pk_bf16_f32 v12, v204, v205\n\t" \
  "v_fmamk_f32 v206, v224, 0x44100000, v226\n\t" \
  "v_fmamk_f32 v207, v224, 0x44180000, v226\n\t" \
  "v_cos_f32 v208, v206\n\t" \
  "v_cos_f32 v209, v207\n\t" \
  "v_sin_f32 v210, v206\n\t" \
  "v_sin_f32 v211, v207\n\t" \
  "v_mul_f32 v208, s53, v208\n\t" \
  "v_mul_f32 v209, s53, v209\n\t" \
  "v_mul_f32 v210, s53, v210\n\t" \
  "v_mul_f32 v211, s53, v211\n\t" \
  "v_cvt_pk_bf16_f32 v5, v208, v209\n\t" \
  "v_cvt_pk_bf16_f32 v13, v210, v211\n\t" \
  "v_fmamk_f32 v200, v224, 0x44400000, v226\n\t" \
  "v_fmamk_f32 v201, v224, 0x44480000, v226\n\t" \
  "v_cos_f32 v202, v200\n\t" \
  "v_cos_f32 v203, v201\n\t" \
  "v_sin_f32 v204, v200\n\t" \
  "v_sin_f32 v205, v201\n\t" \
  "v_mul_f32 v202, s53, v202\n\t" \
  "v_mul_f32 v203, s53, v203\n\t" \
  "v_mul_f32 v204, s53, v204\n\t" \
  "v_mul_f32 v205, s53, v205\n\t" \
  "v_cvt_pk_bf16_f32 v6, v202, v203\n\t" \
  "v_cvt_pk_bf16_f32 v14, v204, v205\n\t" \
  "v_fmamk_f32 v206, v224, 0x44500000, v226\n\t" \
  "v_fmamk_f32 v207, v224, 0x44580000, v226\n\t" \
  "v_cos_f32 v208, v206\n\t" \
  "v_cos_f32 v209, v207\n\t" \
  "v_sin_f32 v210, v206\n\t" \
  "v_sin_f32 v211, v207\n\t" \
  "v_mul_f32 v208, s53, v208\n\t" \
  "v_mul_f32 v209, s53, v209\n\t" \
  "v_mul_f32 v210, s53, v210\n\t" \
  "v_mul_f32 v211, s53, v211\n\t" \
  "v_cvt_pk_bf16_f32 v7, v208, v209\n\t" \
  "v_cvt_pk_bf16_f32 v15, v210, v211\n\t" \
  "v_xor_b32 v16, 0x80008000, v8\n\t" \
  "v_xor_b32 v17, 0x80008000, v9\n\t" \
  "v_xor_b32 v18, 0x80008000, v10\n\t" \
  "v_xor_b32 v19, 0x80008000, v11\n\t" \
  "v_xor_b32 v20, 0x80008000, v12\n\t" \
  "v_xor_b32 v21, 0x80008000, v13\n\t" \
  "v_xor_b32 v22, 0x80008000, v14\n\t" \
  "v_xor_b32 v23, 0x80008000, v15\n\t" \
  "v_fmamk_f32 v200, v224, 0x00000000, v225\n\t" \
  "v_fmamk_f32 v201, v224, 0x00000000, v150\n\t" \
  "v_fmamk_f32 v202, v224, 0x40000000, v225\n\t" \
  "v_fmamk_f32 v203, v224, 0x40000000, v150\n\t" \
  "v_cos_f32 v88, v200\n\t" \
  "v_sin_f32 v92, v200\n\t" \
  "v_cos_f32 v89, v201\n\t" \
  "v_sin_f32 v93, v201\n\t" \
  "v_cos_f32 v90, v202\n\t" \
  "v_sin_f32 v94, v202\n\t" \
  "v_cos_f32 v91, v203\n\t" \
  "v_sin_f32 v95, v203\n\t" \
  "s_waitcnt vmcnt(8)\n\t" \
  "v_add_u32 v248, 1, v144\n\t" \
  "v_cmp_eq_u32 vcc, v248, v145\n\t" \
  "v_mov_b32 v249, v148\n\t" \
  "v_mov_b32_e64 v220, v149\n\t" \
  "v_addc_co_u32 v248, vcc, 0, v248, vcc\n\t" \
  "buffer_store_dword v248, v220, s[68:71], 0 offen sc1\n\t" \
  "v_xor_b32 v216, 0, v227\n\t" \
  "v_xor_b32 v217, 0, v228\n\t" \
  "v_xor_b32 v218, 128, v227\n\t" \
  "v_xor_b32 v219, 128, v228\n\t" \
  "ds_read_b64_tr_b16 v[24:25], v216 offset:0\n\t" \
  "ds_read_b64_tr_b16 v[26:27], v217 offset:2048\n\t" \
  "ds_read_b64_tr_b16 v[32:33], v218 offset:0\n\t" \
  "ds_read_b64_tr_b16 v[34:35], v219 offset:2048\n\t" \
  "ds_read_b64_tr_b16 v[28:29], v216 offset:4096\n\t" \
  "ds_read_b64_tr_b16 v[30:31], v217 offset:6144\n\t" \
  "ds_read_b64_tr_b16 v[36:37], v218 offset:4096\n\t" \
  "ds_read_b64_tr_b16 v[38:39], v219 offset:6144\n\t" \
  "v_xor_b32 v216, 64, v227\n\t" \
  "v_xor_b32 v217, 64, v228\n\t" \
  "v_xor_b32 v218, 192, v227\n\t" \
  "v_xor_b32 v219, 192, v228\n\t" \
  "ds_read_b64_tr_b16 v[40:41], v216 offset:0\n\t" \
  "ds_read_b64_tr_b16 v[42:43], v217 offset:2048\n\t" \
  "ds_read_b64_tr_b16 v[48:49], v218 offset:0\n\t" \
  "ds_read_b64_tr_b16 v[50:51], v219 offset:2048\n\t" \
  "ds_read_b64_tr_b16 v[44:45], v216 offset:4096\n\t" \
  "ds_read_b64_tr_b16 v[46:47], v217 offset:6144\n\t" \
  "ds_read_b64_tr_b16 v[52:53], v218 offset:4096\n\t" \
  "ds_read_b64_tr_b16 v[54:55], v219 offset:6144\n\t" \
  "s_waitcnt lgkmcnt(14)\n\t" \
  "v_mfma_f32_32x32x16_bf16 v[136:151], v[24:27], v[0:3], 0\n\t" \
  "s_waitcnt lgkmcnt(12)\n\t" \
  "v_mfma_f32_32x32x16_bf16 v[152:167], v[32:35], v[0:3], 0\n\t" \
  "v_mfma_f32_32x32x16_bf16 v[136:151], v[32:35], v[8:11], v[136:151]\n\t" \
  "v_mfma_f32_32x32x16_bf16 v[152:167], v[24:27], v[16:19], v[152:167]\n\t" \
  "s_waitcnt lgkmcnt(10)\n\t" \
  "v_mfma_f32_32x32x16_bf16 v[136:151], v[28:31], v[4:7], v[136:151]\n\t" \
  "s_waitcnt lgkmcnt(8)\n\t" \
  "v_mfma_f32_32x32x16_bf16 v[152:167], v[36:39], v[4:7], v[152:167]\n\t" \
  "v_mfma_f32_32x32x16_bf16 v[136:151], v[36:39], v[12:15], v[136:151]\n\t" \
  "v_mfma_f32_32x32x16_bf16 v[152:167], v[28:31], v[20:23], v[152:167]\n\t" \
  "s_waitcnt lgkmcnt(6)\n\t" \
  "v_mfma_f32_32x32x16_bf16 v[168:183], v[40:43], v[0:3], 0\n\t" \
  "s_waitcnt lgkmcnt(4)\n\t" \
  "v_mfma_f32_32x32x16_bf16 v[184:199], v[48:51], v[0:3], 0\n\t" \
  "s_nop 6\n\t" \
  "v_mul_f32 v104, v136, v88\n\t" \
  "v_mul_f32 v108, v152, v88\n\t" \
  "v_mul_f32 v105, v137, v88\n\t" \
  "v_mul_f32 v109, v153, v88\n\t" \
  "v_fmac_f32 v104, v152, v92\n\t" \
  "v_fma_f32 v108, -v136, v92, v108\n\t" \
  "v_fmac_f32_e64 v105, v153, v92\n\t" \
  "v_fma_f32 v109, -v137, v92, v109\n\t" \
  "v_mul_f32 v106, v138, v88\n\t" \
  "v_mul_f32 v110, v154, v88\n\t" \
  "v_mul_f32 v107, v139, v88\n\t" \
  "v_mul_f32 v111, v155, v88\n\t" \
  "v_fmac_f32_e64 v106, v154, v92\n\t" \
  "v_mfma_f32_32x32x16_bf16 v[168:183], v[48:51], v[8:11], v[168:183]\n\t" \
  "v_fma_f32 v110, -v138, v92, v110\n\t" \
  "v_fmac_f32_e64 v107, v155, v92\n\t" \
  "v_fma_f32 v111, -v139, v92, v111\n\t" \
  "v_cvt_pk_bf16_f32 v200, v104, v105\n\t" \
  "v_cvt_pk_bf16_f32 v201, v106, v107\n\t" \
  "v_xor_b32_e64 v202, 0, v229\n\t" \
  "ds_write_b64 v202, v[200:201] offset:0\n\t" \
  "v_cvt_pk_bf16_f32 v204, v108, v109\n\t" \
  "v_cvt_pk_bf16_f32 v205, v110, v111\n\t" \
  "v_xor_b32 v206, 128, v229\n\t" \
  "ds_write_b64 v206, v[204:205] offset:0\n\t" \
  "v_mul_f32 v112, v140, v88\n\t" \
  "v_mul_f32 v116, v156, v88\n\t" \
  "v_mul_f32_e64 v113, v141, v88\n\t" \
  "v_mfma_f32_32x32x16_bf16 v[184:199], v[40:43], v[16:19], v[184:199]\n\t" \
  "v_mul_f32 v117, v157, v88\n\t" \
  "v_fmac_f32 v112, v156, v92\n\t" \
  "v_fma_f32 v116, -v140, v92, v116\n\t" \
  "v_fmac_f32_e64 v113, v157, v92\n\t" \
  "v_fma_f32 v117, -v141, v92, v117\n\t" \
  "v_mul_f32 v114, v142, v88\n\t" \
  "v_mul_f32 v118, v158, v88\n\t" \
  "v_mul_f32 v115, v143, v88\n\t" \
  "v_mul_f32 v119, v159, v88\n\t" \
  "v_fmac_f32_e64 v114, v158, v92\n\t" \
  "v_fma_f32 v118, -v142, v92, v118\n\t" \
  "v_fmac_f32_e64 v115, v159, v92\n\t" \
  "v_fma_f32 v119, -v143, v92, v119\n\t" \
  "v_cvt_pk_bf16_f32 v208, v112, v113\n\t" \
  "s_waitcnt lgkmcnt(4)\n\t" \
  "v_mfma_f32_32x32x16_bf16 v[168:183], v[44:47], v[4:7], v[168:183]\n\t" \
  "v_cvt_pk_bf16_f32 v209, v114, v115\n\t" \
  "v_xor_b32 v210, 16, v229\n\t" \
  "ds_write_b64 v210, v[208:209] offset:0\n\t" \
  "v_cvt_pk_bf16_f32 v212, v116, v117\n\t" \
  "v_cvt_pk_bf16_f32 v213, v118, v119\n\t" \
  "v_xor_b32 v214, 144, v229\n\t" \
  "ds_write_b64 v214, v[212:213] offset:0\n\t" \
  "v_mul_f32 v104, v144, v88\n\t" \
  "v_mul_f32 v108, v160, v88\n\t" \
  "v_mul_f32 v105, v145, v88\n\t" \
  "v_mul_f32 v109, v161, v88\n\t" \
  "v_fmac_f32_e64 v104, v160, v92\n\t" \
  "v_fma_f32 v108, -v144, v92, v108\n\t" \
  "s_waitcnt lgkmcnt(4)\n\t" \
  "v_mfma_f32_32x32x16_bf16 v[184:199], v[52:55], v[4:7], v[184:199]\n\t" \
  "v_fmac_f32 v105, v161, v92\n\t" \
  "v_fma_f32 v109, -v145, v92, v109\n\t" \
  "v_mul_f32 v106, v146, v88\n\t" \
  "v_mul_f32 v110, v162, v88\n\t" \
  "v_mul_f32 v107, v147, v88\n\t" \
  "v_mul_f32 v111, v163, v88\n\t" \
  "v_fmac_f32_e64 v106, v162, v92\n\t" \
  "v_fma_f32 v110, -v146, v92, v110\n\t" \
  "v_fmac_f32_e64 v107, v163, v92\n\t" \
  "v_fma_f32 v111, -v147, v92, v111\n\t" \
  "v_cvt_pk_bf16_f32 v200, v104, v105\n\t" \
  "v_cvt_pk_bf16_f32 v201, v106, v107\n\t" \
  "v_xor_b32_e64 v202, 32, v229\n\t" \
  "ds_write_b64 v202, v[200:201] offset:0\n\t" \
  "v_mfma_f32_32x32x16_bf16 v[168:183], v[52:55], v[12:15], v[168:183]\n\t" \
  "v_cvt_pk_bf16_f32 v204, v108, v109\n\t" \
  "v_cvt_pk_bf16_f32 v205, v110, v111\n\t" \
  "v_xor_b32 v206, 160, v229\n\t" \
  "ds_write_b64 v206, v[204:205] offset:0\n\t" \
  "v_mul_f32 v112, v148, v88\n\t" \
  "v_mul_f32 v116, v164, v88\n\t" \
  "v_mul_f32 v113, v149, v88\n\t" \
  "v_mul_f32 v117, v165, v88\n\t" \
  "v_fmac_f32_e64 v112, v164, v92\n\t" \
  "v_fma_f32 v116, -v148, v92, v116\n\t" \
  "v_fmac_f32_e64 v113, v165, v92\n\t" \
  "v_fma_f32 v117, -v149, v92, v117\n\t" \
  "v_mul_f32 v114, v150, v88\n\t" \
  "v_mul_f32 v118, v166, v88\n\t" \
  "v_mfma_f32_32x32x16_bf16 v[184:199], v[44:47], v[20:23], v[184:199]\n\t" \
  "v_mul_f32 v115, v151, v88\n\t" \
  "v_mul_f32 v119, v167, v88\n\t" \
  "v_fmac_f32_e64 v114, v166, v92\n\t" \
  "v_fma_f32 v118, -v150, v92, v118\n\t" \
  "v_fmac_f32_e64 v115, v167, v92\n\t" \
  "v_fma_f32 v119, -v151, v92, v119\n\t" \
  "v_cvt_pk_bf16_f32 v208, v112, v113\n\t" \
  "v_cvt_pk_bf16_f32 v209, v114, v115\n\t" \
  "v_xor_b32_e64 v210, 48, v229\n\t" \
  "ds_write_b64 v210, v[208:209] offset:0\n\t" \
  "v_cvt_pk_bf16_f32 v212, v116, v117\n\t" \
  "v_cvt_pk_bf16_f32 v213, v118, v119\n\t" \
  "v_xor_b32 v214, 176, v229\n\t" \
  "ds_write_b64 v214, v[212:213] offset:0\n\t" \
  "v_mul_f32 v104, v168, v90\n\t" \
  "v_mul_f32 v108, v184, v90\n\t" \
  "v_mul_f32 v105, v169, v90\n\t" \
  "v_mul_f32 v109, v185, v90\n\t" \
  "v_fmac_f32_e64 v104, v184, v94\n\t" \
  "v_fma_f32 v108, -v168, v94, v108\n\t" \
  "v_fmac_f32_e64 v105, v185, v94\n\t" \
  "v_fma_f32 v109, -v169, v94, v109\n\t" \
  "v_mul_f32 v106, v170, v90\n\t" \
  "v_mul_f32 v110, v186, v90\n\t" \
  "v_mul_f32 v107, v171, v90\n\t" \
  "v_mul_f32 v111, v187, v90\n\t" \
  "v_fmac_f32_e64 v106, v186, v94\n\t" \
  "v_fma_f32 v110, -v170, v94, v110\n\t" \
  "v_fmac_f32_e64 v107, v187, v94\n\t" \
  "v_fma_f32 v111, -v171, v94, v111\n\t" \
  "v_cvt_pk_bf16_f32 v200, v104, v105\n\t" \
  "v_cvt_pk_bf16_f32 v201, v106, v107\n\t" \
  "v_xor_b32_e64 v202, 64, v229\n\t" \
  "ds_write_b64 v202, v[200:201] offset:0\n\t" \
  "v_cvt_pk_bf16_f32 v204, v108, v109\n\t" \
  "v_cvt_pk_bf16_f32 v205, v110, v111\n\t" \
  "v_xor_b32 v206, 192, v229\n\t" \
  "ds_write_b64 v206, v[204:205] offset:0\n\t" \
  "s_waitcnt vmcnt(1)\n\t" \
  "v_xor_b32 v216, 128, v227\n\t" \
  "v_xor_b32 v217, 128, v228\n\t" \
  "v_xor_b32 v218, 0, v227\n\t" \
  "v_xor_b32_e64 v219, 0, v228\n\t" \
  "ds_read_b64_tr_b16 v[56:57], v216 offset:8192\n\t" \
  "ds_read_b64_tr_b16 v[58:59], v217 offset:10240\n\t" \
  "ds_read_b64_tr_b16 v[64:65], v218 offset:8192\n\t" \
  "ds_read_b64_tr_b16 v[66:67], v219 offset:10240\n\t" \
  "ds_read_b64_tr_b16 v[60:61], v216 offset:12288\n\t" \
  "ds_read_b64_tr_b16 v[62:63], v217 offset:14336\n\t" \
  "ds_read_b64_tr_b16 v[68:69], v218 offset:12288\n\t" \
  "ds_read_b64_tr_b16 v[70:71], v219 offset:14336\n\t" \
  "v_xor_b32 v216, 192, v227\n\t" \
  "v_xor_b32 v217, 192, v228\n\t" \
  "v_xor_b32 v218, 64, v227\n\t" \
  "v_xor_b32 v219, 64, v228\n\t" \
  "ds_read_b64_tr_b16 v[72:73], v216 offset:8192\n\t" \
  "ds_read_b64_tr_b16 v[74:75], v217 offset:10240\n\t" \
  "ds_read_b64_tr_b16 v[80:81], v218 offset:8192\n\t" \
  "ds_read_b64_tr_b16 v[82:83], v219 offset:10240\n\t" \
  "ds_read_b64_tr_b16 v[76:77], v216 offset:12288\n\t" \
  "ds_read_b64_tr_b16 v[78:79], v217 offset:14336\n\t" \
  "ds_read_b64_tr_b16 v[84:85], v218 offset:12288\n\t" \
  "ds_read_b64_tr_b16 v[86:87], v219 offset:14336\n\t" \
  "v_mul_f32 v112, v172, v90\n\t" \
  "v_mul_f32 v116, v188, v90\n\t" \
  "v_mul_f32 v113, v173, v90\n\t" \
  "v_mul_f32 v117, v189, v90\n\t" \
  "v_fmac_f32_e64 v112, v188, v94\n\t" \
  "v_fma_f32 v116, -v172, v94, v116\n\t" \
  "v_fmac_f32_e64 v113, v189, v94\n\t" \
  "v_fma_f32 v117, -v173, v94, v117\n\t" \
  "v_mul_f32 v114, v174, v90\n\t" \
  "v_mul_f32 v118, v190, v90\n\t" \
  "v_mul_f32 v115, v175, v90\n\t" \
  "v_mul_f32 v119, v191, v90\n\t" \
  "v_fmac_f32_e64 v114, v190, v94\n\t" \
  "v_fma_f32 v118, -v174, v94, v118\n\t" \
  "v_fmac_f32_e64 v115, v191, v94\n\t" \
  "v_fma_f32 v119, -v175, v94, v119\n\t" \
  "v_cvt_pk_bf16_f32 v208, v112, v113\n\t" \
  "v_cvt_pk_bf16_f32 v209, v114, v115\n\t" \
  "v_xor_b32 v210, 80, v229\n\t" \
  "ds_write_b64 v210, v[208:209] offset:0\n\t" \
  "v_cvt_pk_bf16_f32 v212, v116, v117\n\t" \
  "v_cvt_pk_bf16_f32 v213, v118, v119\n\t" \
  "v_xor_b32 v214, 208, v229\n\t" \
  "ds_write_b64 v214, v[212:213] offset:0\n\t" \
  "s_waitcnt lgkmcnt(14)\n\t" \
  "v_mfma_f32_32x32x16_bf16 v[136:151], v[56:59], v[0:3], 0\n\t" \
  "v_mul_f32 v104, v176, v90\n\t" \
  "v_mul_f32 v108, v192, v90\n\t" \
  "v_mul_f32 v105, v177, v90\n\t" \
  "v_mul_f32 v109, v193, v90\n\t" \
  "v_fmac_f32 v104, v192, v94\n\t" \
  "v_fma_f32 v108, -v176, v94, v108\n\t" \
  "v_mfma_f32_32x32x16_bf16 v[152:167], v[64:67], v[0:3], 0\n\t" \
  "v_fmac_f32_e64 v105, v193, v94\n\t" \
  "v_fma_f32 v109, -v177, v94, v109\n\t" \
  "v_mul_f32 v106, v178, v90\n\t" \
  "v_mul_f32 v110, v194, v90\n\t" \
  "v_mul_f32 v107, v179, v90\n\t" \
  "v_mul_f32 v111, v195, v90\n\t" \
  "v_mfma_f32_32x32x16_bf16 v[136:151], v[64:67], v[8:11], v[136:151]\n\t" \
  "v_fmac_f32_e64 v106, v194, v94\n\t" \
  "v_fma_f32 v110, -v178, v94, v110\n\t" \
  "v_fmac_f32_e64 v107, v195, v94\n\t" \
  "v_fma_f32 v111, -v179, v94, v111\n\t" \
  "v_cvt_pk_bf16_f32 v200, v104, v105\n\t" \
  "v_cvt_pk_bf16_f32 v201, v106, v107\n\t" \
  "v_mfma_f32_32x32x16_bf16 v[152:167], v[56:59], v[16:19], v[152:167]\n\t" \
  "v_xor_b32 v202, 96, v229\n\t" \
  "ds_write_b64 v202, v[200:201] offset:0\n\t" \
  "v_cvt_pk_bf16_f32 v204, v108, v109\n\t" \
  "v_cvt_pk_bf16_f32 v205, v110, v111\n\t" \
  "v_xor_b32 v206, 224, v229\n\t" \
  "ds_write_b64 v206, v[204:205] offset:0\n\t" \
  "s_waitcnt lgkmcnt(14)\n\t" \
  "v_mfma_f32_32x32x16_bf16 v[136:151], v[60:63], v[4:7], v[136:151]\n\t" \
  "v_mul_f32 v112, v180, v90\n\t" \
  "v_mul_f32 v116, v196, v90\n\t" \
  "v_mul_f32 v113, v181, v90\n\t" \
  "v_mul_f32 v117, v197, v90\n\t" \
  "v_fmac_f32 v112, v196, v94\n\t" \
  "v_fma_f32 v116, -v180, v94, v116\n\t" \
  "s_waitcnt lgkmcnt(12)\n\t" \
  "v_mfma_f32_32x32x16_bf16 v[152:167], v[68:71], v[4:7], v[152:167]\n\t" \
  "v_fmac_f32 v113, v197, v94\n\t" \
  "v_fma_f32 v117, -v181, v94, v117\n\t" \
  "v_mul_f32 v114, v182, v90\n\t" \
  "v_mul_f32 v118, v198, v90\n\t" \
  "v_mul_f32 v115, v183, v90\n\t" \
  "v_mul_f32 v119, v199, v90\n\t" \
  "v_mfma_f32_32x32x16_bf16 v[136:151], v[68:71], v[12:15], v[136:151]\n\t" \
  "v_fmac_f32_e64 v114, v198, v94\n\t" \
  "v_fma_f32 v118, -v182, v94, v118\n\t" \
  "v_fmac_f32_e64 v115, v199, v94\n\t" \
  "v_fma_f32 v119, -v183, v94, v119\n\t" \
  "v_cvt_pk_bf16_f32 v208, v112, v113\n\t" \
  "v_cvt_pk_bf16_f32 v209, v114, v115\n\t" \
  "v_mfma_f32_32x32x16_bf16 v[152:167], v[60:63], v[20:23], v[152:167]\n\t" \
  "v_xor_b32 v210, 112, v229\n\t" \
  "ds_write_b64 v210, v[208:209] offset:0\n\t" \
  "v_cvt_pk_bf16_f32 v212, v116, v117\n\t" \
  "v_cvt_pk_bf16_f32 v213, v118, v119\n\t" \
  "v_xor_b32 v214, 240, v229\n\t" \
  "ds_write_b64 v214, v[212:213] offset:0\n\t" \
  "s_waitcnt lgkmcnt(12)\n\t" \
  "v_mfma_f32_32x32x16_bf16 v[168:183], v[72:75], v[0:3], 0\n\t" \
  "s_waitcnt lgkmcnt(10)\n\t" \
  "v_mfma_f32_32x32x16_bf16 v[184:199], v[80:83], v[0:3], 0\n\t" \
  "v_mul_f32 v104, v136, v89\n\t" \
  "s_nop 0\n\t" \
  "v_mul_f32 v108, v152, v89\n\t" \
  "v_mul_f32 v105, v137, v89\n\t" \
  "v_mul_f32 v109, v153, v89\n\t" \
  "v_fmac_f32 v104, v152, v93\n\t" \
  "v_fma_f32 v108, -v136, v93, v108\n\t" \
  "v_fmac_f32_e64 v105, v153, v93\n\t" \
  "v_fma_f32 v109, -v137, v93, v109\n\t" \
  "v_mul_f32 v106, v138, v89\n\t" \
  "v_mul_f32 v110, v154, v89\n\t" \
  "v_mul_f32 v107, v139, v89\n\t" \
  "v_mul_f32 v111, v155, v89\n\t" \
  "v_fmac_f32_e64 v106, v154, v93\n\t" \
  "v_mfma_f32_32x32x16_bf16 v[168:183], v[80:83], v[8:11], v[168:183]\n\t" \
  "v_fma_f32 v110, -v138, v93, v110\n\t" \
  "v_fmac_f32_e64 v107, v155, v93\n\t" \
  "v_fma_f32 v111, -v139, v93, v111\n\t" \
  "v_cvt_pk_bf16_f32 v200, v104, v105\n\t" \
  "v_cvt_pk_bf16_f32 v201, v106, v107\n\t" \
  "v_xor_b32 v202, 128, v229\n\t" \
  "ds_write_b64 v202, v[200:201] offset:8192\n\t" \
  "v_cvt_pk_bf16_f32 v204, v108, v109\n\t" \
  "v_cvt_pk_bf16_f32 v205, v110, v111\n\t" \
  "v_xor_b32_e64 v206, 0, v229\n\t" \
  "ds_write_b64 v206, v[204:205] offset:8192\n\t" \
  "v_mul_f32 v112, v140, v89\n\t" \
  "v_mul_f32 v116, v156, v89\n\t" \
  "v_mul_f32_e64 v113, v141, v89\n\t" \
  "v_mfma_f32_32x32x16_bf16 v[184:199], v[72:75], v[16:19], v[184:199]\n\t" \
  "v_mul_f32 v117, v157, v89\n\t" \
  "v_fmac_f32 v112, v156, v93\n\t" \
  "v_fma_f32 v116, -v140, v93, v116\n\t" \
  "v_fmac_f32_e64 v113, v157, v93\n\t" \
  "v_fma_f32 v117, -v141, v93, v117\n\t" \
  "v_mul_f32 v114, v142, v89\n\t" \
  "v_mul_f32 v118, v158, v89\n\t" \
  "v_mul_f32 v115, v143, v89\n\t" \
  "v_mul_f32 v119, v159, v89\n\t" \
  "v_fmac_f32_e64 v114, v158, v93\n\t" \
  "v_fma_f32 v118, -v142, v93, v118\n\t" \
  "v_fmac_f32_e64 v115, v159, v93\n\t" \
  "v_fma_f32 v119, -v143, v93, v119\n\t" \
  "v_cvt_pk_bf16_f32 v208, v112, v113\n\t" \
  "s_waitcnt lgkmcnt(10)\n\t" \
  "s_nop 0\n\t" \
  "v_mfma_f32_32x32x16_bf16 v[168:183], v[76:79], v[4:7], v[168:183]\n\t" \
  "v_cvt_pk_bf16_f32 v209, v114, v115\n\t" \
  "v_xor_b32 v210, 144, v229\n\t" \
  "ds_write_b64 v210, v[208:209] offset:8192\n\t" \
  "v_cvt_pk_bf16_f32 v212, v116, v117\n\t" \
  "v_cvt_pk_bf16_f32 v213, v118, v119\n\t" \
  "v_xor_b32_e64 v214, 16, v229\n\t" \
  "ds_write_b64 v214, v[212:213] offset:8192\n\t" \
  "v_mul_f32 v104, v144, v89\n\t" \
  "v_mul_f32 v108, v160, v89\n\t" \
  "v_mul_f32 v105, v145, v89\n\t" \
  "v_mul_f32 v109, v161, v89\n\t" \
  "v_fmac_f32_e64 v104, v160, v93\n\t" \
  "v_fma_f32 v108, -v144, v93, v108\n\t" \
  "s_waitcnt lgkmcnt(10)\n\t" \
  "v_mfma_f32_32x32x16_bf16 v[184:199], v[84:87], v[4:7], v[184:199]\n\t" \
  "v_fmac_f32 v105, v161, v93\n\t" \
  "v_fma_f32 v109, -v145, v93, v109\n\t" \
  "v_mul_f32 v106, v146, v89\n\t" \
  "v_mul_f32 v110, v162, v89\n\t" \
  "v_mul_f32 v107, v147, v89\n\t" \
  "v_mul_f32 v111, v163, v89\n\t" \
  "v_fmac_f32_e64 v106, v162, v93\n\t" \
  "v_fma_f32 v110, -v146, v93, v110\n\t" \
  "v_fmac_f32_e64 v107, v163, v93\n\t" \
  "v_fma_f32 v111, -v147, v93, v111\n\t" \
  "v_cvt_pk_bf16_f32 v200, v104, v105\n\t" \
  "v_cvt_pk_bf16_f32 v201, v106, v107\n\t" \
  "v_xor_b32 v202, 160, v229\n\t" \
  "ds_write_b64 v202, v[200:201] offset:8192\n\t" \
  "v_mfma_f32_32x32x16_bf16 v[168:183], v[84:87], v[12:15], v[168:183]\n\t" \
  "v_cvt_pk_bf16_f32 v204, v108, v109\n\t" \
  "v_cvt_pk_bf16_f32 v205, v110, v111\n\t" \
  "v_xor_b32_e64 v206, 32, v229\n\t" \
  "ds_write_b64 v206, v[204:205] offset:8192\n\t" \
  "v_mul_f32 v112, v148, v89\n\t" \
  "v_mul_f32 v116, v164, v89\n\t" \
  "v_mul_f32 v113, v149, v89\n\t" \
  "v_mul_f32 v117, v165, v89\n\t" \
  "v_fmac_f32_e64 v112, v164, v93\n\t" \
  "v_fma_f32 v116, -v148, v93, v116\n\t" \
  "v_fmac_f32_e64 v113, v165, v93\n\t" \
  "v_fma_f32 v117, -v149, v93, v117\n\t" \
  "v_mul_f32 v114, v150, v89\n\t" \
  "v_mul_f32 v118, v166, v89\n\t" \
  "v_mfma_f32_32x32x16_bf16 v[184:199], v[76:79], v[20:23], v[184:199]\n\t" \
  "v_mul_f32 v115, v151, v89\n\t" \
  "v_mul_f32 v119, v167, v89\n\t" \
  "v_fmac_f32_e64 v114, v166, v93\n\t" \
  "v_fma_f32 v118, -v150, v93, v118\n\t" \
  "v_fmac_f32_e64 v115, v167, v93\n\t" \
  "v_fma_f32 v119, -v151, v93, v119\n\t" \
  "v_cvt_pk_bf16_f32 v208, v112, v113\n\t" \
  "v_cvt_pk_bf16_f32 v209, v114, v115\n\t" \
  "v_xor_b32 v210, 176, v229\n\t" \
  "ds_write_b64 v210, v[208:209] offset:8192\n\t" \
  "v_cvt_pk_bf16_f32 v212, v116, v117\n\t" \
  "v_cvt_pk_bf16_f32 v213, v118, v119\n\t" \
  "v_xor_b32_e64 v214, 48, v229\n\t" \
  "ds_write_b64 v214, v[212:213] offset:8192\n\t" \
  "v_xor_b32_e64 v216, 0, v230\n\t" \
  "v_add_u32 v220, 81920, v216\n\t" \
  "v_xor_b32_e64 v217, 64, v230\n\t" \
  "v_add_u32 v221, 81920, v217\n\t" \
  "v_xor_b32 v218, 128, v230\n\t" \
  "v_add_u32 v222, 81920, v218\n\t" \
  "v_xor_b32 v219, 192, v230\n\t" \
  "v_add_u32 v223, 81920, v219\n\t" \
  "v_mul_f32 v104, v168, v91\n\t" \
  "v_mul_f32 v108, v184, v91\n\t" \
  "v_mul_f32 v105, v169, v91\n\t" \
  "v_mul_f32 v109, v185, v91\n\t" \
  "v_fmac_f32_e64 v104, v184, v95\n\t" \
  "v_fma_f32 v108, -v168, v95, v108\n\t" \
  "v_fmac_f32_e64 v105, v185, v95\n\t" \
  "v_fma_f32 v109, -v169, v95, v109\n\t" \
  "v_mul_f32 v106, v170, v91\n\t" \
  "v_mul_f32 v110, v186, v91\n\t" \
  "v_mul_f32 v107, v171, v91\n\t" \
  "v_mul_f32 v111, v187, v91\n\t" \
  "v_fmac_f32_e64 v106, v186, v95\n\t" \
  "v_fma_f32 v110, -v170, v95, v110\n\t" \
  "v_fmac_f32_e64 v107, v187, v95\n\t" \
  "v_fma_f32 v111, -v171, v95, v111\n\t" \
  "v_cvt_pk_bf16_f32 v200, v104, v105\n\t" \
  "v_cvt_pk_bf16_f32 v201, v106, v107\n\t" \
  "v_xor_b32 v202, 192, v229\n\t" \
  "ds_write_b64 v202, v[200:201] offset:8192\n\t" \
  "v_cvt_pk_bf16_f32 v204, v108, v109\n\t" \
  "v_cvt_pk_bf16_f32 v205, v110, v111\n\t" \
  "v_xor_b32_e64 v206, 64, v229\n\t" \
  "ds_write_b64 v206, v[204:205] offset:8192\n\t" \
  "v_mul_f32 v112, v172, v91\n\t" \
  "v_mul_f32 v116, v188, v91\n\t" \
  "v_mul_f32 v113, v173, v91\n\t" \
  "v_mul_f32 v117, v189, v91\n\t" \
  "v_fmac_f32_e64 v112, v188, v95\n\t" \
  "v_fma_f32 v116, -v172, v95, v116\n\t" \
  "v_fmac_f32_e64 v113, v189, v95\n\t" \
  "v_fma_f32 v117, -v173, v95, v117\n\t" \
  "v_mul_f32 v114, v174, v91\n\t" \
  "v_mul_f32 v118, v190, v91\n\t" \
  "v_mul_f32 v115, v175, v91\n\t" \
  "v_mul_f32 v119, v191, v91\n\t" \
  "v_fmac_f32_e64 v114, v190, v95\n\t" \
  "v_fma_f32 v118, -v174, v95, v118\n\t" \
  "v_fmac_f32_e64 v115, v191, v95\n\t" \
  "v_fma_f32 v119, -v175, v95, v119\n\t" \
  "v_cvt_pk_bf16_f32 v208, v112, v113\n\t" \
  "v_cvt_pk_bf16_f32 v209, v114, v115\n\t" \
  "v_xor_b32 v210, 208, v229\n\t" \
  "ds_write_b64 v210, v[208:209] offset:8192\n\t" \
  "v_cvt_pk_bf16_f32 v212, v116, v117\n\t" \
  "v_cvt_pk_bf16_f32 v213, v118, v119\n\t" \
  "v_xor_b32 v214, 80, v229\n\t" \
  "ds_write_b64 v214, v[212:213] offset:8192\n\t" \
  "v_mul_f32 v104, v176, v91\n\t" \
  "v_mul_f32 v108, v192, v91\n\t" \
  "v_mul_f32 v105, v177, v91\n\t" \
  "v_mul_f32 v109, v193, v91\n\t" \
  "v_fmac_f32_e64 v104, v192, v95\n\t" \
  "v_fma_f32 v108, -v176, v95, v108\n\t" \
  "v_fmac_f32_e64 v105, v193, v95\n\t" \
  "v_fma_f32 v109, -v177, v95, v109\n\t" \
  "v_mul_f32 v106, v178, v91\n\t" \
  "v_mul_f32 v110, v194, v91\n\t" \
  "v_mul_f32 v107, v179, v91\n\t" \
  "v_mul_f32 v111, v195, v91\n\t" \
  "v_fmac_f32_e64 v106, v194, v95\n\t" \
  "v_fma_f32 v110, -v178, v95, v110\n\t" \
  "v_fmac_f32_e64 v107, v195, v95\n\t" \
  "v_fma_f32 v111, -v179, v95, v111\n\t" \
  "v_cvt_pk_bf16_f32 v200, v104, v105\n\t" \
  "v_cvt_pk_bf16_f32 v201, v106, v107\n\t" \
  "v_xor_b32 v202, 224, v229\n\t" \
  "ds_write_b64 v202, v[200:201] offset:8192\n\t" \
  "v_cvt_pk_bf16_f32 v204, v108, v109\n\t" \
  "v_cvt_pk_bf16_f32 v205, v110, v111\n\t" \
  "v_xor_b32 v206, 96, v229\n\t" \
  "ds_write_b64 v206, v[204:205] offset:8192\n\t" \
  "v_mul_f32 v112, v180, v91\n\t" \
  "v_mul_f32 v116, v196, v91\n\t" \
  "v_mul_f32 v113, v181, v91\n\t" \
  "v_mul_f32 v117, v197, v91\n\t" \
  "v_fmac_f32_e64 v112, v196, v95\n\t" \
  "v_fma_f32 v116, -v180, v95, v116\n\t" \
  "v_fmac_f32_e64 v113, v197, v95\n\t" \
  "v_fma_f32 v117, -v181, v95, v117\n\t" \
  "v_mul_f32 v114, v182, v91\n\t" \
  "v_mul_f32 v118, v198, v91\n\t" \
  "v_mul_f32 v115, v183, v91\n\t" \
  "v_mul_f32 v119, v199, v91\n\t" \
  "v_fmac_f32_e64 v114, v198, v95\n\t" \
  "v_fma_f32 v118, -v182, v95, v118\n\t" \
  "v_fmac_f32_e64 v115, v199, v95\n\t" \
  "v_fma_f32 v119, -v183, v95, v119\n\t" \
  "v_cvt_pk_bf16_f32 v208, v112, v113\n\t" \
  "v_cvt_pk_bf16_f32 v209, v114, v115\n\t" \
  "v_xor_b32 v210, 240, v229\n\t" \
  "ds_write_b64 v210, v[208:209] offset:8192\n\t" \
  "v_cvt_pk_bf16_f32 v212, v116, v117\n\t" \
  "v_cvt_pk_bf16_f32 v213, v118, v119\n\t" \
  "v_xor_b32 v214, 112, v229\n\t" \
  "ds_write_b64 v214, v[212:213] offset:8192\n\t" \
  "s_waitcnt lgkmcnt(0)\n\t" \
  "s_barrier\n\t" \
  "s_waitcnt vmcnt(0)\n\t" \
  "s_nop 0\n\t" \
  "buffer_load_dwordx4 v[104:107], v231, s[68:71], 0 offen sc1\n\t" \
  "buffer_load_dwordx4 v[108:111], v231, s[68:71], 0 offen offset:16 sc1\n\t" \
  "ds_read_b64_tr_b16 v[24:25], v216 offset:0\n\t" \
  "ds_read_b64_tr_b16 v[26:27], v216 offset:40960\n\t" \
  "ds_read_b64_tr_b16 v[32:33], v218 offset:0\n\t" \
  "ds_read_b64_tr_b16 v[34:35], v218 offset:40960\n\t" \
  "ds_read_b64_tr_b16 v[28:29], v220 offset:0\n\t" \
  "ds_read_b64_tr_b16 v[30:31], v220 offset:40960\n\t" \
  "ds_read_b64_tr_b16 v[36:37], v222 offset:0\n\t" \
  "ds_read_b64_tr_b16 v[38:39], v222 offset:40960\n\t" \
  "ds_read_b64_tr_b16 v[40:41], v217 offset:256\n\t" \
  "ds_read_b64_tr_b16 v[42:43], v217 offset:41216\n\t" \
  "ds_read_b64_tr_b16 v[48:49], v219 offset:256\n\t" \
  "ds_read_b64_tr_b16 v[50:51], v219 offset:41216\n\t" \
  "ds_read_b64_tr_b16 v[44:45], v221 offset:256\n\t" \
  "ds_read_b64_tr_b16 v[46:47], v221 offset:41216\n\t" \
  "ds_read_b64_tr_b16 v[52:53], v223 offset:256\n\t" \
  "ds_read_b64_tr_b16 v[54:55], v223 offset:41216\n\t" \
  "ds_read_b64_tr_b16 v[56:57], v218 offset:512\n\t" \
  "ds_read_b64_tr_b16 v[58:59], v218 offset:41472\n\t" \
  "ds_read_b64_tr_b16 v[64:65], v216 offset:512\n\t" \
  "ds_read_b64_tr_b16 v[66:67], v216 offset:41472\n\t" \
  "ds_read_b64_tr_b16 v[60:61], v222 offset:512\n\t" \
  "ds_read_b64_tr_b16 v[62:63], v222 offset:41472\n\t" \
  "ds_read_b64_tr_b16 v[68:69], v220 offset:512\n\t" \
  "ds_read_b64_tr_b16 v[70:71], v220 offset:41472\n\t" \
  "ds_read_b64_tr_b16 v[72:73], v219 offset:768\n\t" \
  "ds_read_b64_tr_b16 v[74:75], v219 offset:41728\n\t" \
  "ds_read_b64_tr_b16 v[80:81], v217 offset:768\n\t" \
  "ds_read_b64_tr_b16 v[82:83], v217 offset:41728\n\t" \
  "ds_read_b64_tr_b16 v[76:77], v223 offset:768\n\t" \
  "ds_read_b64_tr_b16 v[78:79], v223 offset:41728\n\t" \
  "ds_read_b64_tr_b16 v[84:85], v221 offset:768\n\t" \
  "ds_read_b64_tr_b16 v[86:87], v221 offset:41728\n\t" \
  "s_waitcnt lgkmcnt(14)\n\t" \
  "s_nop 0\n\t" \
  "v_mfma_f32_32x32x16_bf16 v[136:151], v[24:27], v[0:3], 0\n\t" \
  "v_mfma_f32_32x32x16_bf16 v[152:167], v[32:35], v[0:3], 0\n\t" \
  "v_mfma_f32_32x32x16_bf16 v[136:151], v[32:35], v[8:11], v[136:151]\n\t" \
  "v_mfma_f32_32x32x16_bf16 v[152:167], v[24:27], v[16:19], v[152:167]\n\t" \
  "v_mfma_f32_32x32x16_bf16 v[136:151], v[28:31], v[4:7], v[136:151]\n\t" \
  "v_mfma_f32_32x32x16_bf16 v[152:167], v[36:39], v[4:7], v[152:167]\n\t" \
  "v_mfma_f32_32x32x16_bf16 v[136:151], v[36:39], v[12:15], v[136:151]\n\t" \
  "v_mfma_f32_32x32x16_bf16 v[152:167], v[28:31], v[20:23], v[152:167]\n\t" \
  "v_mfma_f32_32x32x16_bf16 v[168:183], v[40:43], v[0:3], 0\n\t" \
  "v_mfma_f32_32x32x16_bf16 v[184:199], v[48:51], v[0:3], 0\n\t" \
  "s_add_u32 s52, s48, 0\n\t" \
  "s_add_u32 s53, s48, 1048576\n\t" \
  "s_add_u32 s54, s48, 2097152\n\t" \
  "s_add_u32 s55, s48, 3145728\n\t" \
  "s_nop 4\n\t" \
  "v_cvt_pk_bf16_f32 v200, v136, v137\n\t" \
  "v_cvt_pk_bf16_f32 v201, v138, v139\n\t" \
  "v_mfma_f32_32x32x16_bf16 v[168:183], v[48:51], v[8:11], v[168:183]\n\t" \
  "ds_write_b64 v232, v[200:201]\n\t" \
  "v_cvt_pk_bf16_f32 v202, v152, v153\n\t" \
  "v_cvt_pk_bf16_f32 v203, v154, v155\n\t" \
  "ds_write_b64 v236, v[202:203]\n\t" \
  "v_cvt_pk_bf16_f32 v204, v140, v141\n\t" \
  "v_cvt_pk_bf16_f32 v205, v142, v143\n\t" \
  "v_mfma_f32_32x32x16_bf16 v[184:199], v[40:43], v[16:19], v[184:199]\n\t" \
  "ds_write_b64 v233, v[204:205]\n\t" \
  "v_cvt_pk_bf16_f32 v206, v156, v157\n\t" \
  "v_cvt_pk_bf16_f32 v207, v158, v159\n\t" \
  "ds_write_b64 v237, v[206:207]\n\t" \
  "v_cvt_pk_bf16_f32 v208, v144, v145\n\t" \
  "v_cvt_pk_bf16_f32 v209, v146, v147\n\t" \
  "ds_write_b64 v234, v[208:209]\n\t" \
  "v_mfma_f32_32x32x16_bf16 v[168:183], v[44:47], v[4:7], v[168:183]\n\t" \
  "v_cvt_pk_bf16_f32 v210, v160, v161\n\t" \
  "v_cvt_pk_bf16_f32 v211, v162, v163\n\t" \
  "ds_write_b64 v238, v[210:211]\n\t" \
  "v_cvt_pk_bf16_f32 v212, v148, v149\n\t" \
  "v_cvt_pk_bf16_f32 v213, v150, v151\n\t" \
  "ds_write_b64 v235, v[212:213]\n\t" \
  "v_mfma_f32_32x32x16_bf16 v[184:199], v[52:55], v[4:7], v[184:199]\n\t" \
  "v_cvt_pk_bf16_f32 v214, v164, v165\n\t" \
  "v_cvt_pk_bf16_f32 v215, v166, v167\n\t" \
  "ds_write_b64 v239, v[214:215]\n\t" \
  "ds_read_b128 v[88:91], v240 offset:0\n\t" \
  "ds_read_b128 v[92:95], v240 offset:1024\n\t" \
  "ds_read_b128 v[96:99], v240 offset:2048\n\t" \
  "ds_read_b128 v[100:103], v240 offset:3072\n\t" \
  "v_mfma_f32_32x32x16_bf16 v[168:183], v[52:55], v[12:15], v[168:183]\n\t" \
  "v_mfma_f32_32x32x16_bf16 v[184:199], v[44:47], v[20:23], v[184:199]\n\t" \
  "s_waitcnt lgkmcnt(3)\n\t" \
  "buffer_store_dwordx4 v[88:91], v241, s[40:43], s52 offen sc1\n\t" \
  "s_waitcnt lgkmcnt(2)\n\t" \
  "buffer_store_dwordx4 v[92:95], v241, s[40:43], s53 offen sc1\n\t" \
  "s_waitcnt lgkmcnt(1)\n\t" \
  "buffer_store_dwordx4 v[96:99], v241, s[40:43], s54 offen sc1\n\t" \
  "s_waitcnt lgkmcnt(0)\n\t" \
  "buffer_store_dwordx4 v[100:103], v241, s[40:43], s55 offen sc1\n\t" \
  "s_waitcnt vmcnt(4)\n\t" \
  "s_nop 0\n\t" \
  "buffer_store_dword v104, v231, s[68:71], 0 offen offset:4 sc1\n\t" \
  "buffer_store_dword v106, v231, s[68:71], 0 offen offset:12 sc1\n\t" \
  "buffer_store_dword v108, v231, s[68:71], 0 offen offset:20 sc1\n\t" \
  "buffer_store_dword v110, v231, s[68:71], 0 offen offset:28 sc1\n\t" \
  "v_mfma_f32_32x32x16_bf16 v[136:151], v[56:59], v[0:3], 0\n\t" \
  "v_mfma_f32_32x32x16_bf16 v[152:167], v[64:67], v[0:3], 0\n\t" \
  "s_add_u32 s52, s48, 4096\n\t" \
  "s_add_u32 s53, s48, 1052672\n\t" \
  "s_add_u32 s54, s48, 2101248\n\t" \
  "s_add_u32 s55, s48, 3149824\n\t" \
  "v_cvt_pk_bf16_f32 v200, v168, v169\n\t" \
  "v_cvt_pk_bf16_f32 v201, v170, v171\n\t" \
  "v_mfma_f32_32x32x16_bf16 v[136:151], v[64:67], v[8:11], v[136:151]\n\t" \
  "ds_write_b64 v232, v[200:201]\n\t" \
  "v_cvt_pk_bf16_f32 v202, v184, v185\n\t" \
  "v_cvt_pk_bf16_f32 v203, v186, v187\n\t" \
  "ds_write_b64 v236, v[202:203]\n\t" \
  "v_cvt_pk_bf16_f32 v204, v172, v173\n\t" \
  "v_cvt_pk_bf16_f32 v205, v174, v175\n\t" \
  "v_mfma_f32_32x32x16_bf16 v[152:167], v[56:59], v[16:19], v[152:167]\n\t" \
  "ds_write_b64 v233, v[204:205]\n\t" \
  "v_cvt_pk_bf16_f32 v206, v188, v189\n\t" \
  "v_cvt_pk_bf16_f32 v207, v190, v191\n\t" \
  "ds_write_b64 v237, v[206:207]\n\t" \
  "v_cvt_pk_bf16_f32 v208, v176, v177\n\t" \
  "v_cvt_pk_bf16_f32 v209, v178, v179\n\t" \
  "ds_write_b64 v234, v[208:209]\n\t" \
  "v_mfma_f32_32x32x16_bf16 v[136:151], v[60:63], v[4:7], v[136:151]\n\t" \
  "v_cvt_pk_bf16_f32 v210, v192, v193\n\t" \
  "v_cvt_pk_bf16_f32 v211, v194, v195\n\t" \
  "ds_write_b64 v238, v[210:211]\n\t" \
  "v_cvt_pk_bf16_f32 v212, v180, v181\n\t" \
  "v_cvt_pk_bf16_f32 v213, v182, v183\n\t" \
  "ds_write_b64 v235, v[212:213]\n\t" \
  "v_mfma_f32_32x32x16_bf16 v[152:167], v[68:71], v[4:7], v[152:167]\n\t" \
  "v_cvt_pk_bf16_f32 v214, v196, v197\n\t" \
  "v_cvt_pk_bf16_f32 v215, v198, v199\n\t" \
  "ds_write_b64 v239, v[214:215]\n\t" \
  "ds_read_b128 v[88:91], v240 offset:0\n\t" \
  "ds_read_b128 v[92:95], v240 offset:1024\n\t" \
  "ds_read_b128 v[96:99], v240 offset:2048\n\t" \
  "ds_read_b128 v[100:103], v240 offset:3072\n\t" \
  "v_mfma_f32_32x32x16_bf16 v[136:151], v[68:71], v[12:15], v[136:151]\n\t" \
  "v_mfma_f32_32x32x16_bf16 v[152:167], v[60:63], v[20:23], v[152:167]\n\t" \
  "s_waitcnt lgkmcnt(3)\n\t" \
  "buffer_store_dwordx4 v[88:91], v241, s[40:43], s52 offen sc1\n\t" \
  "s_waitcnt lgkmcnt(2)\n\t" \
  "buffer_store_dwordx4 v[92:95], v241, s[40:43], s53 offen sc1\n\t" \
  "s_waitcnt lgkmcnt(1)\n\t" \
  "buffer_store_dwordx4 v[96:99], v241, s[40:43], s54 offen sc1\n\t" \
  "s_waitcnt lgkmcnt(0)\n\t" \
  "buffer_store_dwordx4 v[100:103], v241, s[40:43], s55 offen sc1\n\t" \
  "v_mfma_f32_32x32x16_bf16 v[168:183], v[72:75], v[0:3], 0\n\t" \
  "v_mfma_f32_32x32x16_bf16 v[184:199], v[80:83], v[0:3], 0\n\t" \
  "s_add_u32 s52, s48, 8192\n\t" \
  "s_add_u32 s53, s48, 1056768\n\t" \
  "s_add_u32 s54, s48, 2105344\n\t" \
  "s_add_u32 s55, s48, 3153920\n\t" \
  "v_cvt_pk_bf16_f32 v200, v136, v137\n\t" \
  "v_cvt_pk_bf16_f32 v201, v138, v139\n\t" \
  "v_mfma_f32_32x32x16_bf16 v[168:183], v[80:83], v[8:11], v[168:183]\n\t" \
  "ds_write_b64 v232, v[200:201]\n\t" \
  "v_cvt_pk_bf16_f32 v202, v152, v153\n\t" \
  "v_cvt_pk_bf16_f32 v203, v154, v155\n\t" \
  "ds_write_b64 v236, v[202:203]\n\t" \
  "v_cvt_pk_bf16_f32 v204, v140, v141\n\t" \
  "v_cvt_pk_bf16_f32 v205, v142, v143\n\t" \
  "v_mfma_f32_32x32x16_bf16 v[184:199], v[72:75], v[16:19], v[184:199]\n\t" \
  "ds_write_b64 v233, v[204:205]\n\t" \
  "v_cvt_pk_bf16_f32 v206, v156, v157\n\t" \
  "v_cvt_pk_bf16_f32 v207, v158, v159\n\t" \
  "ds_write_b64 v237, v[206:207]\n\t" \
  "v_cvt_pk_bf16_f32 v208, v144, v145\n\t" \
  "v_cvt_pk_bf16_f32 v209, v146, v147\n\t" \
  "ds_write_b64 v234, v[208:209]\n\t" \
  "v_mfma_f32_32x32x16_bf16 v[168:183], v[76:79], v[4:7], v[168:183]\n\t" \
  "v_cvt_pk_bf16_f32 v210, v160, v161\n\t" \
  "v_cvt_pk_bf16_f32 v211, v162, v163\n\t" \
  "ds_write_b64 v238, v[210:211]\n\t" \
  "v_cvt_pk_bf16_f32 v212, v148, v149\n\t" \
  "v_cvt_pk_bf16_f32 v213, v150, v151\n\t" \
  "ds_write_b64 v235, v[212:213]\n\t" \
  "v_mfma_f32_32x32x16_bf16 v[184:199], v[84:87], v[4:7], v[184:199]\n\t" \
  "v_cvt_pk_bf16_f32 v214, v164, v165\n\t" \
  "v_cvt_pk_bf16_f32 v215, v166, v167\n\t" \
  "ds_write_b64 v239, v[214:215]\n\t" \
  "ds_read_b128 v[88:91], v240 offset:0\n\t" \
  "ds_read_b128 v[92:95], v240 offset:1024\n\t" \
  "ds_read_b128 v[96:99], v240 offset:2048\n\t" \
  "ds_read_b128 v[100:103], v240 offset:3072\n\t" \
  "v_mfma_f32_32x32x16_bf16 v[168:183], v[84:87], v[12:15], v[168:183]\n\t" \
  "v_mfma_f32_32x32x16_bf16 v[184:199], v[76:79], v[20:23], v[184:199]\n\t" \
  "s_waitcnt lgkmcnt(3)\n\t" \
  "buffer_store_dwordx4 v[88:91], v241, s[40:43], s52 offen sc1\n\t" \
  "s_waitcnt lgkmcnt(2)\n\t" \
  "buffer_store_dwordx4 v[92:95], v241, s[40:43], s53 offen sc1\n\t" \
  "s_waitcnt lgkmcnt(1)\n\t" \
  "buffer_store_dwordx4 v[96:99], v241, s[40:43], s54 offen sc1\n\t" \
  "s_waitcnt lgkmcnt(0)\n\t" \
  "buffer_store_dwordx4 v[100:103], v241, s[40:43], s55 offen sc1\n\t" \
  "s_add_u32 s52, s48, 12288\n\t" \
  "s_add_u32 s53, s48, 1060864\n\t" \
  "s_add_u32 s54, s48, 2109440\n\t" \
  "s_add_u32 s55, s48, 3158016\n\t" \
  "v_cvt_pk_bf16_f32 v200, v168, v169\n\t" \
  "v_cvt_pk_bf16_f32 v201, v170, v171\n\t" \
  "ds_write_b64 v232, v[200:201]\n\t" \
  "v_cvt_pk_bf16_f32 v202, v184, v185\n\t" \
  "v_cvt_pk_bf16_f32 v203, v186, v187\n\t" \
  "ds_write_b64 v236, v[202:203]\n\t" \
  "v_cvt_pk_bf16_f32 v204, v172, v173\n\t" \
  "v_cvt_pk_bf16_f32 v205, v174, v175\n\t" \
  "ds_write_b64 v233, v[204:205]\n\t" \
  "v_cvt_pk_bf16_f32 v206, v188, v189\n\t" \
  "v_cvt_pk_bf16_f32 v207, v190, v191\n\t" \
  "ds_write_b64 v237, v[206:207]\n\t" \
  "v_cvt_pk_bf16_f32 v208, v176, v177\n\t" \
  "v_cvt_pk_bf16_f32 v209, v178, v179\n\t" \
  "ds_write_b64 v234, v[208:209]\n\t" \
  "v_cvt_pk_bf16_f32 v210, v192, v193\n\t" \
  "v_cvt_pk_bf16_f32 v211, v194, v195\n\t" \
  "ds_write_b64 v238, v[210:211]\n\t" \
  "v_cvt_pk_bf16_f32 v212, v180, v181\n\t" \
  "v_cvt_pk_bf16_f32 v213, v182, v183\n\t" \
  "ds_write_b64 v235, v[212:213]\n\t" \
  "v_cvt_pk_bf16_f32 v214, v196, v197\n\t" \
  "v_cvt_pk_bf16_f32 v215, v198, v199\n\t" \
  "ds_write_b64 v239, v[214:215]\n\t" \
  "ds_read_b128 v[88:91], v240 offset:0\n\t" \
  "ds_read_b128 v[92:95], v240 offset:1024\n\t" \
  "ds_read_b128 v[96:99], v240 offset:2048\n\t" \
  "ds_read_b128 v[100:103], v240 offset:3072\n\t" \
  "s_waitcnt lgkmcnt(3)\n\t" \
  "buffer_store_dwordx4 v[88:91], v241, s[40:43], s52 offen sc1\n\t" \
  "s_waitcnt lgkmcnt(2)\n\t" \
  "buffer_store_dwordx4 v[92:95], v241, s[40:43], s53 offen sc1\n\t" \
  "s_waitcnt lgkmcnt(1)\n\t" \
  "buffer_store_dwordx4 v[96:99], v241, s[40:43], s54 offen sc1\n\t" \
  "s_waitcnt lgkmcnt(0)\n\t" \
  "buffer_store_dwordx4 v[100:103], v241, s[40:43], s55 offen sc1\n\t" \
  "buffer_load_dword v249, v249, s[68:71], 0 offen offset:4 sc1\n\t" \
  "s_waitcnt vmcnt(0)\n\t" \
  "s_barrier\n\t"
#define FFT_W \
  "s_nop 4\n\t" \
  "s_mul_i32 s64, s49, 1026\n\t" \
  "s_sub_u32 s65, 2048, s49\n\t" \
  "s_and_b32 s65, s65, 2047\n\t" \
  "s_mul_i32 s65, s65, 1026\n\t" \
  "s_add_u32 s66, s64, 1050624\n\t" \
  "s_sub_u32 s67, 1050624, s64\n\t" \
  "s_add_u32 s64, s64, 2\n\t" \
  "s_add_u32 s65, s65, 2\n\t" \
  "s_add_u32 s66, s66, 2\n\t" \
  "s_add_u32 s67, s67, 2\n\t" \
  "s_and_b32 s64, s64, -4\n\t" \
  "s_and_b32 s65, s65, -4\n\t" \
  "s_and_b32 s66, s66, -4\n\t" \
  "s_and_b32 s67, s67, -4\n\t" \
  "s_cmp_eq_u32 s49, 512\n\t" \
  "s_cselect_b32 s64, 0, s64\n\t" \
  "s_cselect_b32 s65, 1050624, s65\n\t" \
  "s_add_u32 m0, s50, 0\n\t" \
  "s_nop 0\n\t" \
  "buffer_load_dwordx4 v243, s[56:59], s64 offen lds\n\t" \
  "s_add_u32 m0, s50, 1024\n\t" \
  "s_nop 0\n\t" \
  "buffer_load_dwordx4 v243, s[60:63], s64 offen lds\n\t" \
  "s_add_u32 m0, s50, 2048\n\t" \
  "s_nop 0\n\t" \
  "buffer_load_dwordx4 v243, s[56:59], s65 offen lds\n\t" \
  "s_add_u32 m0, s50, 3072\n\t" \
  "s_nop 0\n\t" \
  "buffer_load_dwordx4 v243, s[60:63], s65 offen lds\n\t" \
  "s_add_u32 m0, s50, 4096\n\t" \
  "s_nop 0\n\t" \
  "buffer_load_dwordx4 v243, s[56:59], s66 offen lds\n\t" \
  "s_add_u32 m0, s50, 5120\n\t" \
  "s_nop 0\n\t" \
  "buffer_load_dwordx4 v243, s[60:63], s66 offen lds\n\t" \
  "s_add_u32 m0, s50, 6144\n\t" \
  "s_nop 0\n\t" \
  "buffer_load_dwordx4 v243, s[56:59], s67 offen lds\n\t" \
  "s_add_u32 m0, s50, 7168\n\t" \
  "s_nop 0\n\t" \
  "buffer_load_dwordx4 v243, s[60:63], s67 offen lds\n\t"
#define FFT_P2 \
  ".p2align 3\n\t" \
  "s_nop 4\n\t" \
  "s_mov_b32 s69, 0\n\t" \
  "s_lshl_b32 s64, s48, 12\n\t" \
  "s_sub_u32 s65, 1024, s48\n\t" \
  "s_and_b32 s65, s65, 511\n\t" \
  "s_cmp_lg_u32 s65, 0\n\t" \
  "s_cselect_b32 s66, 512, 0\n\t" \
  "s_or_b32 s65, s65, s66\n\t" \
  "s_lshl_b32 s65, s65, 12\n\t" \
  "s_add_u32 m0, s51, 0\n\t" \
  "s_nop 0\n\t" \
  "buffer_load_dwordx4 v242, s[44:47], s64 offen offset:0 sc1 lds\n\t" \
  "buffer_load_dwordx4 v242, s[44:47], s64 offen offset:1024 sc1 lds\n\t" \
  "buffer_load_dwordx4 v242, s[44:47], s64 offen offset:2048 sc1 lds\n\t" \
  "buffer_load_dwordx4 v242, s[44:47], s64 offen offset:3072 sc1 lds\n\t" \
  "s_add_u32 m0, s51, 4096\n\t" \
  "s_nop 0\n\t" \
  "buffer_load_dwordx4 v242, s[44:47], s65 offen offset:0 sc1 lds\n\t" \
  "buffer_load_dwordx4 v242, s[44:47], s65 offen offset:1024 sc1 lds\n\t" \
  "buffer_load_dwordx4 v242, s[44:47], s65 offen offset:2048 sc1 lds\n\t" \
  "buffer_load_dwordx4 v242, s[44:47], s65 offen offset:3072 sc1 lds\n\t" \
  "s_waitcnt vmcnt(0)\n\t" \
  "s_nop 0\n\t" \
  "1:\n\t" \
  "s_waitcnt vmcnt(16)\n\t" \
  "ds_read_b64_tr_b16 v[152:153], v228 offset:0\n\t" \
  "ds_read_b64_tr_b16 v[154:155], v228 offset:1024\n\t" \
  "ds_read_b64_tr_b16 v[160:161], v229 offset:0\n\t" \
  "ds_read_b64_tr_b16 v[162:163], v229 offset:1024\n\t" \
  "ds_read_b64_tr_b16 v[156:157], v228 offset:2048\n\t" \
  "ds_read_b64_tr_b16 v[158:159], v228 offset:3072\n\t" \
  "ds_read_b64_tr_b16 v[164:165], v229 offset:2048\n\t" \
  "ds_read_b64_tr_b16 v[166:167], v229 offset:3072\n\t" \
  "ds_read_b64_tr_b16 v[168:169], v228 offset:4096\n\t" \
  "ds_read_b64_tr_b16 v[170:171], v228 offset:5120\n\t" \
  "ds_read_b64_tr_b16 v[176:177], v229 offset:4096\n\t" \
  "ds_read_b64_tr_b16 v[178:179], v229 offset:5120\n\t" \
  "ds_read_b64_tr_b16 v[172:173], v228 offset:6144\n\t" \
  "ds_read_b64_tr_b16 v[174:175], v228 offset:7168\n\t" \
  "ds_read_b64_tr_b16 v[180:181], v229 offset:6144\n\t" \
  "ds_read_b64_tr_b16 v[182:183], v229 offset:7168\n\t" \
  "s_mov_b32 m0, s69\n\t" \
  "s_add_u32 s71, s69, 1\n\t" \
  "s_cmp_eq_u32 s71, s70\n\t" \
  "v_readlane_b32 s72, v244, m0\n\t" \
  "s_cselect_b32 s46, 0, s46\n\t" \
  "s_cselect_b32 s58, 0, s58\n\t" \
  "s_cselect_b32 s62, 0, s62\n\t" \
  "s_add_u32 m0, s69, 4\n\t" \
  "s_nop 0\n\t" \
  "v_readlane_b32 s74, v244, m0\n\t" \
  "s_and_b32 s75, s48, 1\n\t" \
  "s_lshl_b32 s75, s75, 1\n\t" \
  "v_subrev_u32 v246, s75, v230\n\t" \
  "v_subrev_u32 v247, s75, v231\n\t" \
  "s_waitcnt lgkmcnt(14)\n\t" \
  "v_mfma_f32_32x32x16_bf16 v[56:71], v[152:155], v[0:3], 0\n\t" \
  "v_mul_f32 v24, s74, v24\n\t" \
  "v_mul_f32 v25, s74, v25\n\t" \
  "v_mul_f32 v26, s74, v26\n\t" \
  "v_mul_f32 v27, s74, v27\n\t" \
  "s_waitcnt lgkmcnt(12)\n\t" \
  "v_mfma_f32_32x32x16_bf16 v[72:87], v[160:163], v[0:3], 0\n\t" \
  "v_mul_f32 v28, s74, v28\n\t" \
  "v_mul_f32 v29, s74, v29\n\t" \
  "v_mul_f32 v30, s74, v30\n\t" \
  "v_mul_f32 v31, s74, v31\n\t" \
  "v_mfma_f32_32x32x16_bf16 v[56:71], v[160:163], v[8:11], v[56:71]\n\t" \
  "v_mul_f32 v32, s74, v32\n\t" \
  "v_mul_f32 v33, s74, v33\n\t" \
  "v_mul_f32 v34, s74, v34\n\t" \
  "v_mul_f32 v35, s74, v35\n\t" \
  "v_mfma_f32_32x32x16_bf16 v[72:87], v[152:155], v[16:19], v[72:87]\n\t" \
  "v_mul_f32 v36, s74, v36\n\t" \
  "v_mul_f32 v37, s74, v37\n\t" \
  "v_mul_f32 v38, s74, v38\n\t" \
  "v_mul_f32 v39, s74, v39\n\t" \
  "s_waitcnt lgkmcnt(10)\n\t" \
  "v_mfma_f32_32x32x16_bf16 v[56:71], v[156:159], v[4:7], v[56:71]\n\t" \
  "v_mul_f32 v40, s74, v40\n\t" \
  "v_mul_f32 v41, s74, v41\n\t" \
  "v_mul_f32 v42, s74, v42\n\t" \
  "v_mul_f32 v43, s74, v43\n\t" \
  "s_waitcnt lgkmcnt(8)\n\t" \
  "v_mfma_f32_32x32x16_bf16 v[72:87], v[164:167], v[4:7], v[72:87]\n\t" \
  "v_mul_f32 v44, s74, v44\n\t" \
  "v_mul_f32 v45, s74, v45\n\t" \
  "v_mul_f32 v46, s74, v46\n\t" \
  "v_mul_f32 v47, s74, v47\n\t" \
  "v_mfma_f32_32x32x16_bf16 v[56:71], v[164:167], v[12:15], v[56:71]\n\t" \
  "v_mul_f32 v48, s74, v48\n\t" \
  "v_mul_f32 v49, s74, v49\n\t" \
  "v_mul_f32 v50, s74, v50\n\t" \
  "v_mul_f32 v51, s74, v51\n\t" \
  "v_mfma_f32_32x32x16_bf16 v[72:87], v[156:159], v[20:23], v[72:87]\n\t" \
  "v_mul_f32 v52, s74, v52\n\t" \
  "v_mul_f32 v53, s74, v53\n\t" \
  "v_mul_f32 v54, s74, v54\n\t" \
  "v_mul_f32 v55, s74, v55\n\t" \
  "s_waitcnt lgkmcnt(6)\n\t" \
  "v_mfma_f32_32x32x16_bf16 v[88:103], v[168:171], v[0:3], 0\n\t" \
  "s_waitcnt lgkmcnt(0)\n\t" \
  "v_mfma_f32_32x32x16_bf16 v[104:119], v[176:179], v[0:3], 0\n\t" \
  "v_mul_f32 v216, v56, v24\n\t" \
  "s_nop 2\n\t" \
  "v_mul_f32 v218, v72, v24\n\t" \
  "v_mul_f32 v217, v57, v25\n\t" \
  "v_mul_f32 v219, v73, v25\n\t" \
  "v_fmac_f32 v216, v72, v40\n\t" \
  "s_lshl_b32 s64, s49, 12\n\t" \
  "v_fma_f32 v218, -v56, v40, v218\n\t" \
  "v_fmac_f32 v217, v73, v41\n\t" \
  "v_fma_f32 v219, -v57, v41, v219\n\t" \
  "v_cvt_pk_bf16_f32 v184, v216, v217\n\t" \
  "v_cvt_pk_bf16_f32 v192, v218, v219\n\t" \
  "s_sub_u32 s65, 1024, s49\n\t" \
  "v_mul_f32_e64 v220, v58, v26\n\t" \
  "v_mfma_f32_32x32x16_bf16 v[88:103], v[176:179], v[16:19], v[88:103]\n\t" \
  "v_mul_f32 v222, v74, v26\n\t" \
  "v_mul_f32 v221, v59, v27\n\t" \
  "v_mul_f32 v223, v75, v27\n\t" \
  "v_fmac_f32 v220, v74, v42\n\t" \
  "s_and_b32 s65, s65, 511\n\t" \
  "v_fma_f32 v222, -v58, v42, v222\n\t" \
  "v_fmac_f32_e64 v221, v75, v43\n\t" \
  "v_fma_f32 v223, -v59, v43, v223\n\t" \
  "v_cvt_pk_bf16_f32 v185, v220, v221\n\t" \
  "s_cmp_lg_u32 s65, 0\n\t" \
  "v_cvt_pk_bf16_f32 v193, v222, v223\n\t" \
  "v_mul_f32 v224, v60, v28\n\t" \
  "v_mul_f32 v226, v76, v28\n\t" \
  "v_mul_f32 v225, v61, v29\n\t" \
  "v_mfma_f32_32x32x16_bf16 v[104:119], v[168:171], v[8:11], v[104:119]\n\t" \
  "v_mul_f32_e64 v227, v77, v29\n\t" \
  "s_cselect_b32 s66, 512, 0\n\t" \
  "v_fmac_f32_e64 v224, v76, v44\n\t" \
  "v_fma_f32 v226, -v60, v44, v226\n\t" \
  "v_fmac_f32_e64 v225, v77, v45\n\t" \
  "v_fma_f32 v227, -v61, v45, v227\n\t" \
  "v_cvt_pk_bf16_f32 v186, v224, v225\n\t" \
  "s_or_b32 s65, s65, s66\n\t" \
  "v_cvt_pk_bf16_f32 v194, v226, v227\n\t" \
  "v_mul_f32 v216, v62, v30\n\t" \
  "v_mul_f32 v218, v78, v30\n\t" \
  "v_mul_f32 v217, v63, v31\n\t" \
  "s_lshl_b32 s65, s65, 12\n\t" \
  "v_mul_f32 v219, v79, v31\n\t" \
  "v_mfma_f32_32x32x16_bf16 v[88:103], v[172:175], v[4:7], v[88:103]\n\t" \
  "v_fmac_f32_e64 v216, v78, v46\n\t" \
  "v_fma_f32 v218, -v62, v46, v218\n\t" \
  "v_fmac_f32_e64 v217, v79, v47\n\t" \
  "v_fma_f32 v219, -v63, v47, v219\n\t" \
  "s_add_u32 m0, s51, 0\n\t" \
  "v_cvt_pk_bf16_f32 v187, v216, v217\n\t" \
  "v_cvt_pk_bf16_f32 v195, v218, v219\n\t" \
  "v_mul_f32 v220, v64, v32\n\t" \
  "v_mul_f32 v222, v80, v32\n\t" \
  "v_mul_f32 v221, v65, v33\n\t" \
  "buffer_load_dwordx4 v242, s[44:47], s64 offen offset:0 sc1 lds\n\t" \
  "v_mul_f32 v223, v81, v33\n\t" \
  "v_fmac_f32 v220, v80, v48\n\t" \
  "v_fma_f32 v222, -v64, v48, v222\n\t" \
  "v_mfma_f32_32x32x16_bf16 v[104:119], v[180:183], v[4:7], v[104:119]\n\t" \
  "v_fmac_f32_e64 v221, v81, v49\n\t" \
  "v_fma_f32 v223, -v65, v49, v223\n\t" \
  "buffer_load_dwordx4 v242, s[44:47], s64 offen offset:1024 sc1 lds\n\t" \
  "v_cvt_pk_bf16_f32 v188, v220, v221\n\t" \
  "v_cvt_pk_bf16_f32 v196, v222, v223\n\t" \
  "v_mul_f32 v224, v66, v34\n\t" \
  "v_mul_f32 v226, v82, v34\n\t" \
  "buffer_load_dwordx4 v242, s[44:47], s64 offen offset:2048 sc1 lds\n\t" \
  "v_mul_f32 v225, v67, v35\n\t" \
  "v_mul_f32 v227, v83, v35\n\t" \
  "v_fmac_f32_e64 v224, v82, v50\n\t" \
  "v_fma_f32 v226, -v66, v50, v226\n\t" \
  "v_fmac_f32_e64 v225, v83, v51\n\t" \
  "buffer_load_dwordx4 v242, s[44:47], s64 offen offset:3072 sc1 lds\n\t" \
  "v_mfma_f32_32x32x16_bf16 v[88:103], v[180:183], v[20:23], v[88:103]\n\t" \
  "v_fma_f32 v227, -v67, v51, v227\n\t" \
  "v_cvt_pk_bf16_f32 v189, v224, v225\n\t" \
  "v_cvt_pk_bf16_f32 v197, v226, v227\n\t" \
  "v_mul_f32 v216, v68, v36\n\t" \
  "v_mul_f32 v218, v84, v36\n\t" \
  "s_add_u32 m0, s51, 4096\n\t" \
  "v_mul_f32 v217, v69, v37\n\t" \
  "v_mul_f32 v219, v85, v37\n\t" \
  "v_fmac_f32_e64 v216, v84, v52\n\t" \
  "v_fma_f32 v218, -v68, v52, v218\n\t" \
  "buffer_load_dwordx4 v242, s[44:47], s65 offen offset:0 sc1 lds\n\t" \
  "v_fmac_f32_e64 v217, v85, v53\n\t" \
  "v_fma_f32 v219, -v69, v53, v219\n\t" \
  "v_cvt_pk_bf16_f32 v190, v216, v217\n\t" \
  "v_mfma_f32_32x32x16_bf16 v[104:119], v[172:175], v[12:15], v[104:119]\n\t" \
  "v_cvt_pk_bf16_f32 v198, v218, v219\n\t" \
  "v_mul_f32_e64 v220, v70, v38\n\t" \
  "buffer_load_dwordx4 v242, s[44:47], s65 offen offset:1024 sc1 lds\n\t" \
  "v_mul_f32 v222, v86, v38\n\t" \
  "v_mul_f32 v221, v71, v39\n\t" \
  "v_mul_f32 v223, v87, v39\n\t" \
  "v_fmac_f32 v220, v86, v54\n\t" \
  "v_fma_f32 v222, -v70, v54, v222\n\t" \
  "buffer_load_dwordx4 v242, s[44:47], s65 offen offset:2048 sc1 lds\n\t" \
  "v_fmac_f32_e64 v221, v87, v55\n\t" \
  "v_fma_f32 v223, -v71, v55, v223\n\t" \
  "v_cvt_pk_bf16_f32 v191, v220, v221\n\t" \
  "v_cvt_pk_bf16_f32 v199, v222, v223\n\t" \
  "buffer_load_dwordx4 v242, s[44:47], s65 offen offset:3072 sc1 lds\n\t" \
  "v_mfma_f32_32x32x16_bf16 v[120:135], v[0:3], v[184:187], 0\n\t" \
  "v_mfma_f32_32x32x16_bf16 v[136:151], v[0:3], v[192:195], 0\n\t" \
  "v_mul_f32 v216, v88, v24\n\t" \
  "v_mul_f32 v218, v104, v24\n\t" \
  "v_mul_f32 v217, v89, v25\n\t" \
  "v_mul_f32 v219, v105, v25\n\t" \
  "v_fma_f32 v216, -v104, v40, v216\n\t" \
  "v_fmac_f32_e64 v218, v88, v40\n\t" \
  "v_fma_f32 v217, -v105, v41, v217\n\t" \
  "v_fmac_f32_e64 v219, v89, v41\n\t" \
  "v_cvt_pk_bf16_f32 v200, v216, v217\n\t" \
  "v_cvt_pk_bf16_f32 v208, v218, v219\n\t" \
  "v_mul_f32_e64 v220, v90, v26\n\t" \
  "v_mfma_f32_32x32x16_bf16 v[120:135], v[8:11], v[192:195], v[120:135]\n\t" \
  "v_mul_f32 v222, v106, v26\n\t" \
  "v_mul_f32 v221, v91, v27\n\t" \
  "v_mul_f32_e64 v223, v107, v27\n\t" \
  "v_fma_f32 v220, -v106, v42, v220\n\t" \
  "v_fmac_f32_e64 v222, v90, v42\n\t" \
  "v_fma_f32 v221, -v107, v43, v221\n\t" \
  "v_fmac_f32_e64 v223, v91, v43\n\t" \
  "v_cvt_pk_bf16_f32 v201, v220, v221\n\t" \
  "v_cvt_pk_bf16_f32 v209, v222, v223\n\t" \
  "v_mul_f32 v224, v92, v28\n\t" \
  "v_mul_f32 v226, v108, v28\n\t" \
  "v_mfma_f32_32x32x16_bf16 v[136:151], v[16:19], v[184:187], v[136:151]\n\t" \
  "v_mul_f32 v225, v93, v29\n\t" \
  "v_mul_f32 v227, v109, v29\n\t" \
  "v_fma_f32 v224, -v108, v44, v224\n\t" \
  "v_fmac_f32_e64 v226, v92, v44\n\t" \
  "v_fma_f32 v225, -v109, v45, v225\n\t" \
  "v_fmac_f32_e64 v227, v93, v45\n\t" \
  "v_cvt_pk_bf16_f32 v202, v224, v225\n\t" \
  "v_cvt_pk_bf16_f32 v210, v226, v227\n\t" \
  "v_mul_f32 v216, v94, v30\n\t" \
  "v_mul_f32 v218, v110, v30\n\t" \
  "v_mul_f32 v217, v95, v31\n\t" \
  "v_mul_f32 v219, v111, v31\n\t" \
  "v_mfma_f32_32x32x16_bf16 v[120:135], v[4:7], v[188:191], v[120:135]\n\t" \
  "v_fma_f32 v216, -v110, v46, v216\n\t" \
  "v_fmac_f32_e64 v218, v94, v46\n\t" \
  "v_fma_f32 v217, -v111, v47, v217\n\t" \
  "v_fmac_f32_e64 v219, v95, v47\n\t" \
  "v_cvt_pk_bf16_f32 v203, v216, v217\n\t" \
  "v_cvt_pk_bf16_f32 v211, v218, v219\n\t" \
  "v_mul_f32 v220, v96, v32\n\t" \
  "v_mul_f32 v222, v112, v32\n\t" \
  "v_mul_f32 v221, v97, v33\n\t" \
  "v_mul_f32 v223, v113, v33\n\t" \
  "v_fma_f32 v220, -v112, v48, v220\n\t" \
  "v_mfma_f32_32x32x16_bf16 v[136:151], v[4:7], v[196:199], v[136:151]\n\t" \
  "v_fmac_f32_e64 v222, v96, v48\n\t" \
  "v_fma_f32 v221, -v113, v49, v221\n\t" \
  "v_fmac_f32_e64 v223, v97, v49\n\t" \
  "v_cvt_pk_bf16_f32 v204, v220, v221\n\t" \
  "v_cvt_pk_bf16_f32 v212, v222, v223\n\t" \
  "v_mul_f32 v224, v98, v34\n\t" \
  "v_mul_f32 v226, v114, v34\n\t" \
  "v_mul_f32 v225, v99, v35\n\t" \
  "v_mul_f32 v227, v115, v35\n\t" \
  "v_fma_f32 v224, -v114, v50, v224\n\t" \
  "v_fmac_f32_e64 v226, v98, v50\n\t" \
  "v_fma_f32 v225, -v115, v51, v225\n\t" \
  "v_mfma_f32_32x32x16_bf16 v[120:135], v[12:15], v[196:199], v[120:135]\n\t" \
  "v_fmac_f32_e64 v227, v99, v51\n\t" \
  "v_cvt_pk_bf16_f32 v205, v224, v225\n\t" \
  "v_cvt_pk_bf16_f32 v213, v226, v227\n\t" \
  "v_mul_f32 v216, v100, v36\n\t" \
  "v_mul_f32 v218, v116, v36\n\t" \
  "v_mul_f32 v217, v101, v37\n\t" \
  "v_mul_f32 v219, v117, v37\n\t" \
  "v_fma_f32 v216, -v116, v52, v216\n\t" \
  "v_fmac_f32_e64 v218, v100, v52\n\t" \
  "v_fma_f32 v217, -v117, v53, v217\n\t" \
  "v_fmac_f32_e64 v219, v101, v53\n\t" \
  "v_mfma_f32_32x32x16_bf16 v[136:151], v[20:23], v[188:191], v[136:151]\n\t" \
  "v_cvt_pk_bf16_f32 v206, v216, v217\n\t" \
  "v_cvt_pk_bf16_f32 v214, v218, v219\n\t" \
  "v_mul_f32 v220, v102, v38\n\t" \
  "v_mul_f32 v222, v118, v38\n\t" \
  "v_mul_f32 v221, v103, v39\n\t" \
  "v_mul_f32 v223, v119, v39\n\t" \
  "v_fma_f32 v220, -v118, v54, v220\n\t" \
  "v_fmac_f32_e64 v222, v102, v54\n\t" \
  "v_fma_f32 v221, -v119, v55, v221\n\t" \
  "v_fmac_f32_e64 v223, v103, v55\n\t" \
  "v_cvt_pk_bf16_f32 v207, v220, v221\n\t" \
  "v_cvt_pk_bf16_f32 v215, v222, v223\n\t" \
  "s_waitcnt vmcnt(16)\n\t" \
  "s_nop 0\n\t" \
  "v_mfma_f32_32x32x16_bf16 v[56:71], v[0:3], v[200:203], 0\n\t" \
  "ds_read_u16_d16_hi v88, v246 offset:0\n\t" \
  "ds_read_u16_d16_hi v96, v246 offset:1024\n\t" \
  "ds_read_u16_d16_hi v104, v246 offset:4096\n\t" \
  "ds_read_u16_d16_hi v112, v246 offset:5120\n\t" \
  "ds_read_u16_d16_hi v89, v246 offset:64\n\t" \
  "v_mfma_f32_32x32x16_bf16 v[72:87], v[0:3], v[208:211], 0\n\t" \
  "ds_read_u16_d16_hi v97, v246 offset:1088\n\t" \
  "ds_read_u16_d16_hi v105, v246 offset:4160\n\t" \
  "ds_read_u16_d16_hi v113, v246 offset:5184\n\t" \
  "ds_read_u16_d16_hi v90, v246 offset:128\n\t" \
  "ds_read_u16_d16_hi v98, v246 offset:1152\n\t" \
  "ds_read_u16_d16_hi v106, v246 offset:4224\n\t" \
  "v_mfma_f32_32x32x16_bf16 v[56:71], v[16:19], v[208:211], v[56:71]\n\t" \
  "ds_read_u16_d16_hi v114, v246 offset:5248\n\t" \
  "ds_read_u16_d16_hi v91, v246 offset:192\n\t" \
  "ds_read_u16_d16_hi v99, v246 offset:1216\n\t" \
  "ds_read_u16_d16_hi v107, v246 offset:4288\n\t" \
  "ds_read_u16_d16_hi v115, v246 offset:5312\n\t" \
  "v_mfma_f32_32x32x16_bf16 v[72:87], v[8:11], v[200:203], v[72:87]\n\t" \
  "ds_read_u16_d16_hi v92, v246 offset:512\n\t" \
  "ds_read_u16_d16_hi v100, v246 offset:1536\n\t" \
  "ds_read_u16_d16_hi v108, v246 offset:4608\n\t" \
  "ds_read_u16_d16_hi v116, v246 offset:5632\n\t" \
  "ds_read_u16_d16_hi v93, v246 offset:576\n\t" \
  "ds_read_u16_d16_hi v101, v246 offset:1600\n\t" \
  "v_mfma_f32_32x32x16_bf16 v[56:71], v[4:7], v[204:207], v[56:71]\n\t" \
  "ds_read_u16_d16_hi v109, v246 offset:4672\n\t" \
  "ds_read_u16_d16_hi v117, v246 offset:5696\n\t" \
  "ds_read_u16_d16_hi v94, v246 offset:640\n\t" \
  "ds_read_u16_d16_hi v102, v246 offset:1664\n\t" \
  "ds_read_u16_d16_hi v110, v246 offset:4736\n\t" \
  "ds_read_u16_d16_hi v118, v246 offset:5760\n\t" \
  "v_mfma_f32_32x32x16_bf16 v[72:87], v[4:7], v[212:215], v[72:87]\n\t" \
  "ds_read_u16_d16_hi v95, v246 offset:704\n\t" \
  "ds_read_u16_d16_hi v103, v246 offset:1728\n\t" \
  "ds_read_u16_d16_hi v111, v246 offset:4800\n\t" \
  "ds_read_u16_d16_hi v119, v246 offset:5824\n\t" \
  "s_lshl_b32 m0, s69, 3\n\t" \
  "v_mfma_f32_32x32x16_bf16 v[56:71], v[20:23], v[212:215], v[56:71]\n\t" \
  "v_readlane_b32 s76, v245, m0\n\t" \
  "s_add_u32 m0, m0, 1\n\t" \
  "v_readlane_b32 s77, v245, m0\n\t" \
  "s_add_u32 m0, m0, 1\n\t" \
  "v_readlane_b32 s78, v245, m0\n\t" \
  "s_add_u32 m0, m0, 1\n\t" \
  "v_mfma_f32_32x32x16_bf16 v[72:87], v[12:15], v[204:207], v[72:87]\n\t" \
  "v_readlane_b32 s79, v245, m0\n\t" \
  "s_add_u32 m0, m0, 1\n\t" \
  "s_waitcnt lgkmcnt(14)\n\t" \
  "v_writelane_b32 v88, s76, 0\n\t" \
  "v_writelane_b32 v96, s77, 0\n\t" \
  "v_writelane_b32 v104, s78, 0\n\t" \
  "v_writelane_b32 v112, s79, 0\n\t" \
  "s_cmp_eq_u32 s48, 512\n\t" \
  "s_waitcnt lgkmcnt(0)\n\t" \
  "s_nop 1\n\t" \
  "s_cbranch_scc1 7f\n\t" \
  "ds_read_u16_d16_hi v184, v247 offset:704\n\t" \
  "v_pk_fma_f32 v[216:217], v[136:137], s[72:73], v[56:57] op_sel_hi:[1,0,1]\n\t" \
  "v_pk_fma_f32 v[218:219], v[120:121], s[72:73], v[72:73] op_sel_hi:[1,0,1]\n\t" \
  "ds_read_u16_d16_hi v192, v247 offset:1728\n\t" \
  "v_pk_fma_f32 v[220:221], v[72:73], s[72:73], v[120:121] op_sel_hi:[1,0,1] neg_lo:[1,0,0] neg_hi:[1,0,0]\n\t" \
  "v_pk_fma_f32 v[222:223], v[56:57], s[72:73], v[136:137] op_sel_hi:[1,0,1] neg_lo:[1,0,0] neg_hi:[1,0,0]\n\t" \
  "ds_read_u16_d16_hi v200, v247 offset:4800\n\t" \
  "v_pk_mul_f32 v[120:121], v[216:217], v[88:89]\n\t" \
  "v_pk_mul_f32 v[136:137], v[216:217], v[96:97]\n\t" \
  "ds_read_u16_d16_hi v208, v247 offset:5824\n\t" \
  "v_pk_mul_f32 v[56:57], v[220:221], v[104:105]\n\t" \
  "v_pk_mul_f32 v[72:73], v[220:221], v[112:113]\n\t" \
  "ds_read_u16_d16_hi v185, v247 offset:640\n\t" \
  "v_pk_fma_f32 v[120:121], v[218:219], v[96:97], v[120:121]\n\t" \
  "v_pk_fma_f32 v[136:137], v[218:219], v[88:89], v[136:137] neg_lo:[1,0,0] neg_hi:[1,0,0]\n\t" \
  "ds_read_u16_d16_hi v193, v247 offset:1664\n\t" \
  "v_pk_fma_f32 v[56:57], v[222:223], v[112:113], v[56:57] neg_lo:[1,0,0] neg_hi:[1,0,0]\n\t" \
  "v_pk_fma_f32 v[72:73], v[222:223], v[104:105], v[72:73]\n\t" \
  "ds_read_u16_d16_hi v201, v247 offset:4736\n\t" \
  "v_pk_fma_f32 v[88:89], v[136:137], s[72:73], v[56:57] op_sel_hi:[1,0,1] neg_lo:[1,0,0] neg_hi:[1,0,0]\n\t" \
  "v_pk_fma_f32 v[96:97], v[120:121], s[72:73], v[72:73] op_sel_hi:[1,0,1]\n\t" \
  "ds_read_u16_d16_hi v209, v247 offset:5760\n\t" \
  "v_pk_fma_f32 v[104:105], v[72:73], s[72:73], v[120:121] op_sel_hi:[1,0,1] neg_lo:[1,0,0] neg_hi:[1,0,0]\n\t" \
  "v_pk_fma_f32 v[112:113], v[56:57], s[72:73], v[136:137] op_sel_hi:[1,0,1] neg_lo:[1,0,1] neg_hi:[1,0,1]\n\t" \
  "ds_read_u16_d16_hi v186, v247 offset:576\n\t" \
  "v_cvt_pk_bf16_f32 v152, v88, v89\n\t" \
  "v_cvt_pk_bf16_f32 v160, v96, v97\n\t" \
  "ds_read_u16_d16_hi v194, v247 offset:1600\n\t" \
  "v_cvt_pk_bf16_f32 v168, v104, v105\n\t" \
  "v_cvt_pk_bf16_f32 v176, v112, v113\n\t" \
  "ds_read_u16_d16_hi v202, v247 offset:4672\n\t" \
  "v_pk_fma_f32 v[216:217], v[138:139], s[72:73], v[58:59] op_sel_hi:[1,0,1]\n\t" \
  "v_pk_fma_f32 v[218:219], v[122:123], s[72:73], v[74:75] op_sel_hi:[1,0,1]\n\t" \
  "ds_read_u16_d16_hi v210, v247 offset:5696\n\t" \
  "v_pk_fma_f32 v[220:221], v[74:75], s[72:73], v[122:123] op_sel_hi:[1,0,1] neg_lo:[1,0,0] neg_hi:[1,0,0]\n\t" \
  "v_pk_fma_f32 v[222:223], v[58:59], s[72:73], v[138:139] op_sel_hi:[1,0,1] neg_lo:[1,0,0] neg_hi:[1,0,0]\n\t" \
  "ds_read_u16_d16_hi v187, v247 offset:512\n\t" \
  "v_pk_mul_f32 v[122:123], v[216:217], v[90:91]\n\t" \
  "v_pk_mul_f32 v[138:139], v[216:217], v[98:99]\n\t" \
  "ds_read_u16_d16_hi v195, v247 offset:1536\n\t" \
  "v_pk_mul_f32 v[58:59], v[220:221], v[106:107]\n\t" \
  "v_pk_mul_f32 v[74:75], v[220:221], v[114:115]\n\t" \
  "ds_read_u16_d16_hi v203, v247 offset:4608\n\t" \
  "v_pk_fma_f32 v[122:123], v[218:219], v[98:99], v[122:123]\n\t" \
  "v_pk_fma_f32 v[138:139], v[218:219], v[90:91], v[138:139] neg_lo:[1,0,0] neg_hi:[1,0,0]\n\t" \
  "ds_read_u16_d16_hi v211, v247 offset:5632\n\t" \
  "v_pk_fma_f32 v[58:59], v[222:223], v[114:115], v[58:59] neg_lo:[1,0,0] neg_hi:[1,0,0]\n\t" \
  "v_pk_fma_f32 v[74:75], v[222:223], v[106:107], v[74:75]\n\t" \
  "ds_read_u16_d16_hi v188, v247 offset:192\n\t" \
  "v_pk_fma_f32 v[90:91], v[138:139], s[72:73], v[58:59] op_sel_hi:[1,0,1] neg_lo:[1,0,0] neg_hi:[1,0,0]\n\t" \
  "v_pk_fma_f32 v[98:99], v[122:123], s[72:73], v[74:75] op_sel_hi:[1,0,1]\n\t" \
  "ds_read_u16_d16_hi v196, v247 offset:1216\n\t" \
  "v_pk_fma_f32 v[106:107], v[74:75], s[72:73], v[122:123] op_sel_hi:[1,0,1] neg_lo:[1,0,0] neg_hi:[1,0,0]\n\t" \
  "v_pk_fma_f32 v[114:115], v[58:59], s[72:73], v[138:139] op_sel_hi:[1,0,1] neg_lo:[1,0,1] neg_hi:[1,0,1]\n\t" \
  "ds_read_u16_d16_hi v204, v247 offset:4288\n\t" \
  "v_cvt_pk_bf16_f32 v153, v90, v91\n\t" \
  "v_cvt_pk_bf16_f32 v161, v98, v99\n\t" \
  "ds_read_u16_d16_hi v212, v247 offset:5312\n\t" \
  "v_cvt_pk_bf16_f32 v169, v106, v107\n\t" \
  "v_cvt_pk_bf16_f32 v177, v114, v115\n\t" \
  "ds_read_u16_d16_hi v189, v247 offset:128\n\t" \
  "v_pk_fma_f32 v[216:217], v[140:141], s[72:73], v[60:61] op_sel_hi:[1,0,1]\n\t" \
  "v_pk_fma_f32 v[218:219], v[124:125], s[72:73], v[76:77] op_sel_hi:[1,0,1]\n\t" \
  "ds_read_u16_d16_hi v197, v247 offset:1152\n\t" \
  "v_pk_fma_f32 v[220:221], v[76:77], s[72:73], v[124:125] op_sel_hi:[1,0,1] neg_lo:[1,0,0] neg_hi:[1,0,0]\n\t" \
  "v_pk_fma_f32 v[222:223], v[60:61], s[72:73], v[140:141] op_sel_hi:[1,0,1] neg_lo:[1,0,0] neg_hi:[1,0,0]\n\t" \
  "ds_read_u16_d16_hi v205, v247 offset:4224\n\t" \
  "v_pk_mul_f32 v[124:125], v[216:217], v[92:93]\n\t" \
  "v_pk_mul_f32 v[140:141], v[216:217], v[100:101]\n\t" \
  "ds_read_u16_d16_hi v213, v247 offset:5248\n\t" \
  "v_pk_mul_f32 v[60:61], v[220:221], v[108:109]\n\t" \
  "v_pk_mul_f32 v[76:77], v[220:221], v[116:117]\n\t" \
  "ds_read_u16_d16_hi v190, v247 offset:64\n\t" \
  "v_pk_fma_f32 v[124:125], v[218:219], v[100:101], v[124:125]\n\t" \
  "v_pk_fma_f32 v[140:141], v[218:219], v[92:93], v[140:141] neg_lo:[1,0,0] neg_hi:[1,0,0]\n\t" \
  "ds_read_u16_d16_hi v198, v247 offset:1088\n\t" \
  "v_pk_fma_f32 v[60:61], v[222:223], v[116:117], v[60:61] neg_lo:[1,0,0] neg_hi:[1,0,0]\n\t" \
  "v_pk_fma_f32 v[76:77], v[222:223], v[108:109], v[76:77]\n\t" \
  "ds_read_u16_d16_hi v206, v247 offset:4160\n\t" \
  "v_pk_fma_f32 v[92:93], v[140:141], s[72:73], v[60:61] op_sel_hi:[1,0,1] neg_lo:[1,0,0] neg_hi:[1,0,0]\n\t" \
  "v_pk_fma_f32 v[100:101], v[124:125], s[72:73], v[76:77] op_sel_hi:[1,0,1]\n\t" \
  "ds_read_u16_d16_hi v214, v247 offset:5184\n\t" \
  "v_pk_fma_f32 v[108:109], v[76:77], s[72:73], v[124:125] op_sel_hi:[1,0,1] neg_lo:[1,0,0] neg_hi:[1,0,0]\n\t" \
  "v_pk_fma_f32 v[116:117], v[60:61], s[72:73], v[140:141] op_sel_hi:[1,0,1] neg_lo:[1,0,1] neg_hi:[1,0,1]\n\t" \
  "ds_read_u16_d16_hi v191, v247 offset:0\n\t" \
  "v_cvt_pk_bf16_f32 v154, v92, v93\n\t" \
  "v_cvt_pk_bf16_f32 v162, v100, v101\n\t" \
  "ds_read_u16_d16_hi v199, v247 offset:1024\n\t" \
  "v_cvt_pk_bf16_f32 v170, v108, v109\n\t" \
  "v_cvt_pk_bf16_f32 v178, v116, v117\n\t" \
  "ds_read_u16_d16_hi v207, v247 offset:4096\n\t" \
  "v_pk_fma_f32 v[216:217], v[142:143], s[72:73], v[62:63] op_sel_hi:[1,0,1]\n\t" \
  "v_pk_fma_f32 v[218:219], v[126:127], s[72:73], v[78:79] op_sel_hi:[1,0,1]\n\t" \
  "ds_read_u16_d16_hi v215, v247 offset:5120\n\t" \
  "v_pk_fma_f32 v[220:221], v[78:79], s[72:73], v[126:127] op_sel_hi:[1,0,1] neg_lo:[1,0,0] neg_hi:[1,0,0]\n\t" \
  "v_pk_fma_f32 v[222:223], v[62:63], s[72:73], v[142:143] op_sel_hi:[1,0,1] neg_lo:[1,0,0] neg_hi:[1,0,0]\n\t" \
  "v_pk_mul_f32 v[126:127], v[216:217], v[94:95]\n\t" \
  "v_pk_mul_f32 v[142:143], v[216:217], v[102:103]\n\t" \
  "v_pk_mul_f32 v[62:63], v[220:221], v[110:111]\n\t" \
  "v_pk_mul_f32 v[78:79], v[220:221], v[118:119]\n\t" \
  "v_pk_fma_f32 v[126:127], v[218:219], v[102:103], v[126:127]\n\t" \
  "v_pk_fma_f32 v[142:143], v[218:219], v[94:95], v[142:143] neg_lo:[1,0,0] neg_hi:[1,0,0]\n\t" \
  "v_pk_fma_f32 v[62:63], v[222:223], v[118:119], v[62:63] neg_lo:[1,0,0] neg_hi:[1,0,0]\n\t" \
  "v_pk_fma_f32 v[78:79], v[222:223], v[110:111], v[78:79]\n\t" \
  "v_pk_fma_f32 v[94:95], v[142:143], s[72:73], v[62:63] op_sel_hi:[1,0,1] neg_lo:[1,0,0] neg_hi:[1,0,0]\n\t" \
  "v_pk_fma_f32 v[102:103], v[126:127], s[72:73], v[78:79] op_sel_hi:[1,0,1]\n\t" \
  "v_pk_fma_f32 v[110:111], v[78:79], s[72:73], v[126:127] op_sel_hi:[1,0,1] neg_lo:[1,0,0] neg_hi:[1,0,0]\n\t" \
  "v_pk_fma_f32 v[118:119], v[62:63], s[72:73], v[142:143] op_sel_hi:[1,0,1] neg_lo:[1,0,1] neg_hi:[1,0,1]\n\t" \
  "v_cvt_pk_bf16_f32 v155, v94, v95\n\t" \
  "v_cvt_pk_bf16_f32 v163, v102, v103\n\t" \
  "v_cvt_pk_bf16_f32 v171, v110, v111\n\t" \
  "v_cvt_pk_bf16_f32 v179, v118, v119\n\t" \
  "v_readlane_b32 s76, v245, m0\n\t" \
  "s_add_u32 m0, m0, 1\n\t" \
  "v_readlane_b32 s77, v245, m0\n\t" \
  "s_add_u32 m0, m0, 1\n\t" \
  "v_readlane_b32 s78, v245, m0\n\t" \
  "s_add_u32 m0, m0, 1\n\t" \
  "v_readlane_b32 s79, v245, m0\n\t" \
  "s_add_u32 m0, m0, 1\n\t" \
  "s_waitcnt lgkmcnt(14)\n\t" \
  "v_writelane_b32 v184, s76, 0\n\t" \
  "v_writelane_b32 v192, s77, 0\n\t" \
  "v_writelane_b32 v200, s78, 0\n\t" \
  "v_writelane_b32 v208, s79, 0\n\t" \
  "v_mfma_f32_32x32x16_bf16 v[88:103], v[152:155], v[0:3], 0\n\t" \
  "v_pk_fma_f32 v[216:217], v[144:145], s[72:73], v[64:65] op_sel_hi:[1,0,1]\n\t" \
  "v_pk_fma_f32 v[218:219], v[128:129], s[72:73], v[80:81] op_sel_hi:[1,0,1]\n\t" \
  "v_pk_fma_f32 v[220:221], v[80:81], s[72:73], v[128:129] op_sel_hi:[1,0,1] neg_lo:[1,0,0] neg_hi:[1,0,0]\n\t" \
  "v_pk_fma_f32 v[222:223], v[64:65], s[72:73], v[144:145] op_sel_hi:[1,0,1] neg_lo:[1,0,0] neg_hi:[1,0,0]\n\t" \
  "v_pk_mul_f32 v[128:129], v[216:217], v[184:185]\n\t" \
  "v_pk_mul_f32 v[144:145], v[216:217], v[192:193]\n\t" \
  "v_pk_mul_f32 v[64:65], v[220:221], v[200:201]\n\t" \
  "v_pk_mul_f32 v[80:81], v[222:223], v[200:201]\n\t" \
  "v_pk_fma_f32 v[128:129], v[218:219], v[192:193], v[128:129] neg_lo:[1,0,0] neg_hi:[1,0,0]\n\t" \
  "v_pk_fma_f32 v[144:145], v[218:219], v[184:185], v[144:145] neg_lo:[1,0,1] neg_hi:[1,0,1]\n\t" \
  "v_pk_fma_f32 v[64:65], v[222:223], v[208:209], v[64:65]\n\t" \
  "v_pk_fma_f32 v[80:81], v[220:221], v[208:209], v[80:81] neg_lo:[1,0,0] neg_hi:[1,0,0]\n\t" \
  "v_pk_fma_f32 v[184:185], v[144:145], s[72:73], v[64:65] op_sel_hi:[1,0,1] neg_lo:[1,0,0] neg_hi:[1,0,0]\n\t" \
  "v_pk_fma_f32 v[192:193], v[128:129], s[72:73], v[80:81] op_sel_hi:[1,0,1]\n\t" \
  "v_pk_fma_f32 v[200:201], v[80:81], s[72:73], v[128:129] op_sel_hi:[1,0,1] neg_lo:[1,0,0] neg_hi:[1,0,0]\n\t" \
  "v_mfma_f32_32x32x16_bf16 v[104:119], v[160:163], v[0:3], 0\n\t" \
  "v_pk_fma_f32 v[208:209], v[64:65], s[72:73], v[144:145] op_sel_hi:[1,0,1] neg_lo:[1,0,1] neg_hi:[1,0,1]\n\t" \
  "v_cvt_pk_bf16_f32 v156, v184, v185\n\t" \
  "v_cvt_pk_bf16_f32 v164, v192, v193\n\t" \
  "v_cvt_pk_bf16_f32 v172, v200, v201\n\t" \
  "v_cvt_pk_bf16_f32 v180, v208, v209\n\t" \
  "v_pk_fma_f32 v[216:217], v[146:147], s[72:73], v[66:67] op_sel_hi:[1,0,1]\n\t" \
  "v_pk_fma_f32 v[218:219], v[130:131], s[72:73], v[82:83] op_sel_hi:[1,0,1]\n\t" \
  "v_pk_fma_f32 v[220:221], v[82:83], s[72:73], v[130:131] op_sel_hi:[1,0,1] neg_lo:[1,0,0] neg_hi:[1,0,0]\n\t" \
  "v_pk_fma_f32 v[222:223], v[66:67], s[72:73], v[146:147] op_sel_hi:[1,0,1] neg_lo:[1,0,0] neg_hi:[1,0,0]\n\t" \
  "v_pk_mul_f32 v[130:131], v[216:217], v[186:187]\n\t" \
  "v_pk_mul_f32 v[146:147], v[216:217], v[194:195]\n\t" \
  "v_pk_mul_f32 v[66:67], v[220:221], v[202:203]\n\t" \
  "v_pk_mul_f32 v[82:83], v[222:223], v[202:203]\n\t" \
  "v_pk_fma_f32 v[130:131], v[218:219], v[194:195], v[130:131] neg_lo:[1,0,0] neg_hi:[1,0,0]\n\t" \
  "v_pk_fma_f32 v[146:147], v[218:219], v[186:187], v[146:147] neg_lo:[1,0,1] neg_hi:[1,0,1]\n\t" \
  "v_mfma_f32_32x32x16_bf16 v[88:103], v[160:163], v[16:19], v[88:103]\n\t" \
  "v_pk_fma_f32 v[66:67], v[222:223], v[210:211], v[66:67]\n\t" \
  "v_pk_fma_f32 v[82:83], v[220:221], v[210:211], v[82:83] neg_lo:[1,0,0] neg_hi:[1,0,0]\n\t" \
  "v_pk_fma_f32 v[186:187], v[146:147], s[72:73], v[66:67] op_sel_hi:[1,0,1] neg_lo:[1,0,0] neg_hi:[1,0,0]\n\t" \
  "v_pk_fma_f32 v[194:195], v[130:131], s[72:73], v[82:83] op_sel_hi:[1,0,1]\n\t" \
  "v_pk_fma_f32 v[202:203], v[82:83], s[72:73], v[130:131] op_sel_hi:[1,0,1] neg_lo:[1,0,0] neg_hi:[1,0,0]\n\t" \
  "v_pk_fma_f32 v[210:211], v[66:67], s[72:73], v[146:147] op_sel_hi:[1,0,1] neg_lo:[1,0,1] neg_hi:[1,0,1]\n\t" \
  "v_cvt_pk_bf16_f32 v157, v186, v187\n\t" \
  "v_cvt_pk_bf16_f32 v165, v194, v195\n\t" \
  "v_cvt_pk_bf16_f32 v173, v202, v203\n\t" \
  "v_cvt_pk_bf16_f32 v181, v210, v211\n\t" \
  "v_pk_fma_f32 v[216:217], v[148:149], s[72:73], v[68:69] op_sel_hi:[1,0,1]\n\t" \
  "v_pk_fma_f32 v[218:219], v[132:133], s[72:73], v[84:85] op_sel_hi:[1,0,1]\n\t" \
  "v_pk_fma_f32 v[220:221], v[84:85], s[72:73], v[132:133] op_sel_hi:[1,0,1] neg_lo:[1,0,0] neg_hi:[1,0,0]\n\t" \
  "v_pk_fma_f32 v[222:223], v[68:69], s[72:73], v[148:149] op_sel_hi:[1,0,1] neg_lo:[1,0,0] neg_hi:[1,0,0]\n\t" \
  "s_waitcnt lgkmcnt(11)\n\t" \
  "v_pk_mul_f32 v[132:133], v[216:217], v[188:189]\n\t" \
  "v_mfma_f32_32x32x16_bf16 v[104:119], v[152:155], v[8:11], v[104:119]\n\t" \
  "s_waitcnt lgkmcnt(10)\n\t" \
  "v_pk_mul_f32 v[148:149], v[216:217], v[196:197]\n\t" \
  "s_waitcnt lgkmcnt(9)\n\t" \
  "v_pk_mul_f32 v[68:69], v[220:221], v[204:205]\n\t" \
  "v_pk_mul_f32 v[84:85], v[222:223], v[204:205]\n\t" \
  "v_pk_fma_f32 v[132:133], v[218:219], v[196:197], v[132:133] neg_lo:[1,0,0] neg_hi:[1,0,0]\n\t" \
  "v_pk_fma_f32 v[148:149], v[218:219], v[188:189], v[148:149] neg_lo:[1,0,1] neg_hi:[1,0,1]\n\t" \
  "s_waitcnt lgkmcnt(8)\n\t" \
  "v_pk_fma_f32 v[68:69], v[222:223], v[212:213], v[68:69]\n\t" \
  "v_pk_fma_f32 v[84:85], v[220:221], v[212:213], v[84:85] neg_lo:[1,0,0] neg_hi:[1,0,0]\n\t" \
  "v_pk_fma_f32 v[188:189], v[148:149], s[72:73], v[68:69] op_sel_hi:[1,0,1] neg_lo:[1,0,0] neg_hi:[1,0,0]\n\t" \
  "v_pk_fma_f32 v[196:197], v[132:133], s[72:73], v[84:85] op_sel_hi:[1,0,1]\n\t" \
  "v_pk_fma_f32 v[204:205], v[84:85], s[72:73], v[132:133] op_sel_hi:[1,0,1] neg_lo:[1,0,0] neg_hi:[1,0,0]\n\t" \
  "v_pk_fma_f32 v[212:213], v[68:69], s[72:73], v[148:149] op_sel_hi:[1,0,1] neg_lo:[1,0,1] neg_hi:[1,0,1]\n\t" \
  "v_cvt_pk_bf16_f32 v158, v188, v189\n\t" \
  "v_cvt_pk_bf16_f32 v166, v196, v197\n\t" \
  "v_cvt_pk_bf16_f32 v174, v204, v205\n\t" \
  "v_cvt_pk_bf16_f32 v182, v212, v213\n\t" \
  "v_pk_fma_f32 v[216:217], v[150:151], s[72:73], v[70:71] op_sel_hi:[1,0,1]\n\t" \
  "v_pk_fma_f32 v[218:219], v[134:135], s[72:73], v[86:87] op_sel_hi:[1,0,1]\n\t" \
  "v_pk_fma_f32 v[220:221], v[86:87], s[72:73], v[134:135] op_sel_hi:[1,0,1] neg_lo:[1,0,0] neg_hi:[1,0,0]\n\t" \
  "v_pk_fma_f32 v[222:223], v[70:71], s[72:73], v[150:151] op_sel_hi:[1,0,1] neg_lo:[1,0,0] neg_hi:[1,0,0]\n\t" \
  "s_waitcnt lgkmcnt(3)\n\t" \
  "v_pk_mul_f32 v[134:135], v[216:217], v[190:191]\n\t" \
  "s_waitcnt lgkmcnt(2)\n\t" \
  "v_pk_mul_f32 v[150:151], v[216:217], v[198:199]\n\t" \
  "s_waitcnt lgkmcnt(1)\n\t" \
  "v_pk_mul_f32 v[70:71], v[220:221], v[206:207]\n\t" \
  "v_pk_mul_f32 v[86:87], v[222:223], v[206:207]\n\t" \
  "v_pk_fma_f32 v[134:135], v[218:219], v[198:199], v[134:135] neg_lo:[1,0,0] neg_hi:[1,0,0]\n\t" \
  "v_pk_fma_f32 v[150:151], v[218:219], v[190:191], v[150:151] neg_lo:[1,0,1] neg_hi:[1,0,1]\n\t" \
  "s_waitcnt lgkmcnt(0)\n\t" \
  "v_pk_fma_f32 v[70:71], v[222:223], v[214:215], v[70:71]\n\t" \
  "v_pk_fma_f32 v[86:87], v[220:221], v[214:215], v[86:87] neg_lo:[1,0,0] neg_hi:[1,0,0]\n\t" \
  "v_pk_fma_f32 v[190:191], v[150:151], s[72:73], v[70:71] op_sel_hi:[1,0,1] neg_lo:[1,0,0] neg_hi:[1,0,0]\n\t" \
  "v_pk_fma_f32 v[198:199], v[134:135], s[72:73], v[86:87] op_sel_hi:[1,0,1]\n\t" \
  "v_pk_fma_f32 v[206:207], v[86:87], s[72:73], v[134:135] op_sel_hi:[1,0,1] neg_lo:[1,0,0] neg_hi:[1,0,0]\n\t" \
  "v_pk_fma_f32 v[214:215], v[70:71], s[72:73], v[150:151] op_sel_hi:[1,0,1] neg_lo:[1,0,1] neg_hi:[1,0,1]\n\t" \
  "v_cvt_pk_bf16_f32 v159, v190, v191\n\t" \
  "v_cvt_pk_bf16_f32 v167, v198, v199\n\t" \
  "v_cvt_pk_bf16_f32 v175, v206, v207\n\t" \
  "v_cvt_pk_bf16_f32 v183, v214, v215\n\t" \
  "s_nop 1\n\t" \
  "s_nop 0\n\t" \
  "8:\n\t" \
  "v_mfma_f32_32x32x16_bf16 v[88:103], v[156:159], v[4:7], v[88:103]\n\t" \
  "v_mfma_f32_32x32x16_bf16 v[104:119], v[164:167], v[4:7], v[104:119]\n\t" \
  "v_mfma_f32_32x32x16_bf16 v[88:103], v[164:167], v[20:23], v[88:103]\n\t" \
  "v_mfma_f32_32x32x16_bf16 v[104:119], v[156:159], v[12:15], v[104:119]\n\t" \
  "v_mfma_f32_32x32x16_bf16 v[56:71], v[168:171], v[0:3], 0\n\t" \
  "v_mfma_f32_32x32x16_bf16 v[72:87], v[176:179], v[0:3], 0\n\t" \
  "s_nop 8\n\t" \
  "v_mul_f32 v216, v88, v24\n\t" \
  "v_mul_f32 v218, v104, v24\n\t" \
  "v_mul_f32 v217, v89, v25\n\t" \
  "s_mul_i32 s64, s49, 1026\n\t" \
  "v_mul_f32_e64 v219, v105, v25\n\t" \
  "v_fma_f32 v216, -v104, v40, v216\n\t" \
  "s_sub_u32 s65, 2048, s49\n\t" \
  "v_fmac_f32_e64 v218, v88, v40\n\t" \
  "v_fma_f32 v217, -v105, v41, v217\n\t" \
  "v_fmac_f32_e64 v219, v89, v41\n\t" \
  "s_and_b32 s65, s65, 2047\n\t" \
  "v_cvt_pk_bf16_f32 v184, v216, v217\n\t" \
  "v_cvt_pk_bf16_f32 v192, v218, v219\n\t" \
  "s_mul_i32 s65, s65, 1026\n\t" \
  "v_mul_f32 v220, v90, v26\n\t" \
  "v_mul_f32 v222, v106, v26\n\t" \
  "v_mfma_f32_32x32x16_bf16 v[56:71], v[176:179], v[8:11], v[56:71]\n\t" \
  "v_mul_f32_e64 v221, v91, v27\n\t" \
  "s_add_u32 s66, s64, 1050624\n\t" \
  "v_mul_f32_e64 v223, v107, v27\n\t" \
  "v_fma_f32 v220, -v106, v42, v220\n\t" \
  "s_sub_u32 s67, 1050624, s64\n\t" \
  "v_fmac_f32_e64 v222, v90, v42\n\t" \
  "v_fma_f32 v221, -v107, v43, v221\n\t" \
  "s_add_u32 s64, s64, 2\n\t" \
  "v_fmac_f32 v223, v91, v43\n\t" \
  "v_cvt_pk_bf16_f32 v185, v220, v221\n\t" \
  "v_cvt_pk_bf16_f32 v193, v222, v223\n\t" \
  "s_add_u32 s65, s65, 2\n\t" \
  "v_mul_f32 v224, v92, v28\n\t" \
  "v_mul_f32 v226, v108, v28\n\t" \
  "s_add_u32 s66, s66, 2\n\t" \
  "v_mul_f32_e64 v225, v93, v29\n\t" \
  "v_mfma_f32_32x32x16_bf16 v[72:87], v[168:171], v[16:19], v[72:87]\n\t" \
  "v_mul_f32_e64 v227, v109, v29\n\t" \
  "v_fma_f32 v224, -v108, v44, v224\n\t" \
  "s_add_u32 s67, s67, 2\n\t" \
  "v_fmac_f32 v226, v92, v44\n\t" \
  "v_fma_f32 v225, -v109, v45, v225\n\t" \
  "s_and_b32 s64, s64, -4\n\t" \
  "v_fmac_f32 v227, v93, v45\n\t" \
  "v_cvt_pk_bf16_f32 v186, v224, v225\n\t" \
  "v_cvt_pk_bf16_f32 v194, v226, v227\n\t" \
  "s_and_b32 s65, s65, -4\n\t" \
  "v_mul_f32 v216, v94, v30\n\t" \
  "v_mul_f32 v218, v110, v30\n\t" \
  "s_and_b32 s66, s66, -4\n\t" \
  "v_mul_f32 v217, v95, v31\n\t" \
  "v_mul_f32 v219, v111, v31\n\t" \
  "s_and_b32 s67, s67, -4\n\t" \
  "v_mfma_f32_32x32x16_bf16 v[56:71], v[172:175], v[4:7], v[56:71]\n\t" \
  "v_fma_f32 v216, -v110, v46, v216\n\t" \
  "v_fmac_f32 v218, v94, v46\n\t" \
  "v_fma_f32 v217, -v111, v47, v217\n\t" \
  "s_cmp_eq_u32 s49, 512\n\t" \
  "v_fmac_f32_e64 v219, v95, v47\n\t" \
  "v_cvt_pk_bf16_f32 v187, v216, v217\n\t" \
  "s_cselect_b32 s64, 0, s64\n\t" \
  "v_cvt_pk_bf16_f32 v195, v218, v219\n\t" \
  "v_mul_f32 v220, v96, v32\n\t" \
  "v_mul_f32_e64 v222, v112, v32\n\t" \
  "s_cselect_b32 s65, 1050624, s65\n\t" \
  "v_mul_f32 v221, v97, v33\n\t" \
  "v_mul_f32 v223, v113, v33\n\t" \
  "s_add_u32 m0, s50, 0\n\t" \
  "v_fma_f32 v220, -v112, v48, v220\n\t" \
  "v_fmac_f32 v222, v96, v48\n\t" \
  "v_mfma_f32_32x32x16_bf16 v[72:87], v[180:183], v[4:7], v[72:87]\n\t" \
  "v_fma_f32 v221, -v113, v49, v221\n\t" \
  "buffer_load_dwordx4 v243, s[56:59], s64 offen lds\n\t" \
  "v_fmac_f32_e64 v223, v97, v49\n\t" \
  "v_cvt_pk_bf16_f32 v188, v220, v221\n\t" \
  "s_add_u32 m0, s50, 1024\n\t" \
  "v_cvt_pk_bf16_f32 v196, v222, v223\n\t" \
  "v_mul_f32_e64 v224, v98, v34\n\t" \
  "buffer_load_dwordx4 v243, s[60:63], s64 offen lds\n\t" \
  "v_mul_f32 v226, v114, v34\n\t" \
  "v_mul_f32 v225, v99, v35\n\t" \
  "v_mul_f32_e64 v227, v115, v35\n\t" \
  "s_add_u32 m0, s50, 2048\n\t" \
  "v_fma_f32 v224, -v114, v50, v224\n\t" \
  "v_fmac_f32_e64 v226, v98, v50\n\t" \
  "buffer_load_dwordx4 v243, s[56:59], s65 offen lds\n\t" \
  "v_fma_f32 v225, -v115, v51, v225\n\t" \
  "v_mfma_f32_32x32x16_bf16 v[56:71], v[180:183], v[12:15], v[56:71]\n\t" \
  "v_fmac_f32_e64 v227, v99, v51\n\t" \
  "v_cvt_pk_bf16_f32 v189, v224, v225\n\t" \
  "s_add_u32 m0, s50, 3072\n\t" \
  "v_cvt_pk_bf16_f32 v197, v226, v227\n\t" \
  "v_mul_f32_e64 v216, v100, v36\n\t" \
  "buffer_load_dwordx4 v243, s[60:63], s65 offen lds\n\t" \
  "v_mul_f32 v218, v116, v36\n\t" \
  "v_mul_f32 v217, v101, v37\n\t" \
  "v_mul_f32_e64 v219, v117, v37\n\t" \
  "s_add_u32 m0, s50, 4096\n\t" \
  "v_fma_f32 v216, -v116, v52, v216\n\t" \
  "v_fmac_f32_e64 v218, v100, v52\n\t" \
  "buffer_load_dwordx4 v243, s[56:59], s66 offen lds\n\t" \
  "v_fma_f32 v217, -v117, v53, v217\n\t" \
  "v_fmac_f32_e64 v219, v101, v53\n\t" \
  "s_add_u32 m0, s50, 5120\n\t" \
  "v_mfma_f32_32x32x16_bf16 v[72:87], v[172:175], v[20:23], v[72:87]\n\t" \
  "v_cvt_pk_bf16_f32 v190, v216, v217\n\t" \
  "v_cvt_pk_bf16_f32 v198, v218, v219\n\t" \
  "v_mul_f32_e64 v220, v102, v38\n\t" \
  "buffer_load_dwordx4 v243, s[60:63], s66 offen lds\n\t" \
  "v_mul_f32 v222, v118, v38\n\t" \
  "v_mul_f32 v221, v103, v39\n\t" \
  "s_add_u32 m0, s50, 6144\n\t" \
  "v_mul_f32_e64 v223, v119, v39\n\t" \
  "v_fma_f32 v220, -v118, v54, v220\n\t" \
  "v_fmac_f32_e64 v222, v102, v54\n\t" \
  "buffer_load_dwordx4 v243, s[56:59], s67 offen lds\n\t" \
  "v_fma_f32 v221, -v119, v55, v221\n\t" \
  "v_fmac_f32_e64 v223, v103, v55\n\t" \
  "s_add_u32 m0, s50, 7168\n\t" \
  "v_cvt_pk_bf16_f32 v191, v220, v221\n\t" \
  "v_cvt_pk_bf16_f32 v199, v222, v223\n\t" \
  "buffer_load_dwordx4 v243, s[60:63], s67 offen lds\n\t" \
  "v_mfma_f32_32x32x16_bf16 v[120:135], v[184:187], v[0:3], 0\n\t" \
  "v_mfma_f32_32x32x16_bf16 v[136:151], v[192:195], v[0:3], 0\n\t" \
  "v_mul_f32 v216, v56, v24\n\t" \
  "v_mul_f32 v218, v72, v24\n\t" \
  "v_mul_f32 v217, v57, v25\n\t" \
  "v_mul_f32 v219, v73, v25\n\t" \
  "v_fmac_f32_e64 v216, v72, v40\n\t" \
  "v_fma_f32 v218, -v56, v40, v218\n\t" \
  "v_fmac_f32_e64 v217, v73, v41\n\t" \
  "v_fma_f32 v219, -v57, v41, v219\n\t" \
  "v_cvt_pk_bf16_f32 v200, v216, v217\n\t" \
  "v_cvt_pk_bf16_f32 v208, v218, v219\n\t" \
  "v_mul_f32_e64 v220, v58, v26\n\t" \
  "v_mfma_f32_32x32x16_bf16 v[120:135], v[192:195], v[16:19], v[120:135]\n\t" \
  "v_mul_f32 v222, v74, v26\n\t" \
  "v_mul_f32 v221, v59, v27\n\t" \
  "v_mul_f32 v223, v75, v27\n\t" \
  "v_fmac_f32 v220, v74, v42\n\t" \
  "v_fma_f32 v222, -v58, v42, v222\n\t" \
  "v_fmac_f32_e64 v221, v75, v43\n\t" \
  "v_fma_f32 v223, -v59, v43, v223\n\t" \
  "v_cvt_pk_bf16_f32 v201, v220, v221\n\t" \
  "v_cvt_pk_bf16_f32 v209, v222, v223\n\t" \
  "v_mul_f32 v224, v60, v28\n\t" \
  "v_mul_f32 v226, v76, v28\n\t" \
  "v_mfma_f32_32x32x16_bf16 v[136:151], v[184:187], v[8:11], v[136:151]\n\t" \
  "v_mul_f32 v225, v61, v29\n\t" \
  "v_mul_f32 v227, v77, v29\n\t" \
  "v_fmac_f32_e64 v224, v76, v44\n\t" \
  "v_fma_f32 v226, -v60, v44, v226\n\t" \
  "v_fmac_f32_e64 v225, v77, v45\n\t" \
  "v_fma_f32 v227, -v61, v45, v227\n\t" \
  "v_cvt_pk_bf16_f32 v202, v224, v225\n\t" \
  "v_cvt_pk_bf16_f32 v210, v226, v227\n\t" \
  "v_mul_f32 v216, v62, v30\n\t" \
  "v_mul_f32 v218, v78, v30\n\t" \
  "v_mul_f32 v217, v63, v31\n\t" \
  "v_mul_f32 v219, v79, v31\n\t" \
  "v_mfma_f32_32x32x16_bf16 v[120:135], v[188:191], v[4:7], v[120:135]\n\t" \
  "v_fmac_f32_e64 v216, v78, v46\n\t" \
  "v_fma_f32 v218, -v62, v46, v218\n\t" \
  "v_fmac_f32_e64 v217, v79, v47\n\t" \
  "v_fma_f32 v219, -v63, v47, v219\n\t" \
  "v_cvt_pk_bf16_f32 v203, v216, v217\n\t" \
  "v_cvt_pk_bf16_f32 v211, v218, v219\n\t" \
  "v_mul_f32 v220, v64, v32\n\t" \
  "v_mul_f32 v222, v80, v32\n\t" \
  "v_mul_f32 v221, v65, v33\n\t" \
  "v_mul_f32 v223, v81, v33\n\t" \
  "v_fmac_f32_e64 v220, v80, v48\n\t" \
  "v_mfma_f32_32x32x16_bf16 v[136:151], v[196:199], v[4:7], v[136:151]\n\t" \
  "v_fma_f32 v222, -v64, v48, v222\n\t" \
  "v_fmac_f32_e64 v221, v81, v49\n\t" \
  "v_fma_f32 v223, -v65, v49, v223\n\t" \
  "v_cvt_pk_bf16_f32 v204, v220, v221\n\t" \
  "v_cvt_pk_bf16_f32 v212, v222, v223\n\t" \
  "v_mul_f32 v224, v66, v34\n\t" \
  "v_mul_f32 v226, v82, v34\n\t" \
  "v_mul_f32 v225, v67, v35\n\t" \
  "v_mul_f32 v227, v83, v35\n\t" \
  "v_fmac_f32_e64 v224, v82, v50\n\t" \
  "v_fma_f32 v226, -v66, v50, v226\n\t" \
  "v_fmac_f32_e64 v225, v83, v51\n\t" \
  "v_mfma_f32_32x32x16_bf16 v[120:135], v[196:199], v[20:23], v[120:135]\n\t" \
  "v_fma_f32 v227, -v67, v51, v227\n\t" \
  "v_cvt_pk_bf16_f32 v205, v224, v225\n\t" \
  "v_cvt_pk_bf16_f32 v213, v226, v227\n\t" \
  "v_mul_f32 v216, v68, v36\n\t" \
  "v_mul_f32 v218, v84, v36\n\t" \
  "v_mul_f32 v217, v69, v37\n\t" \
  "v_mul_f32 v219, v85, v37\n\t" \
  "v_fmac_f32_e64 v216, v84, v52\n\t" \
  "v_fma_f32 v218, -v68, v52, v218\n\t" \
  "v_fmac_f32_e64 v217, v85, v53\n\t" \
  "v_fma_f32 v219, -v69, v53, v219\n\t" \
  "v_mfma_f32_32x32x16_bf16 v[136:151], v[188:191], v[12:15], v[136:151]\n\t" \
  "v_cvt_pk_bf16_f32 v206, v216, v217\n\t" \
  "v_cvt_pk_bf16_f32 v214, v218, v219\n\t" \
  "v_mul_f32 v220, v70, v38\n\t" \
  "v_mul_f32 v222, v86, v38\n\t" \
  "v_mul_f32 v221, v71, v39\n\t" \
  "v_mul_f32 v223, v87, v39\n\t" \
  "v_fmac_f32_e64 v220, v86, v54\n\t" \
  "v_fma_f32 v222, -v70, v54, v222\n\t" \
  "v_fmac_f32_e64 v221, v87, v55\n\t" \
  "v_fma_f32 v223, -v71, v55, v223\n\t" \
  "v_cvt_pk_bf16_f32 v207, v220, v221\n\t" \
  "v_cvt_pk_bf16_f32 v215, v222, v223\n\t" \
  "v_mfma_f32_32x32x16_bf16 v[88:103], v[200:203], v[0:3], 0\n\t" \
  "v_mfma_f32_32x32x16_bf16 v[104:119], v[208:211], v[0:3], 0\n\t" \
  "s_lshl_b32 s52, s48, 7\n\t" \
  "s_nop 0\n\t" \
  "s_add_u32 s53, s52, 1048576\n\t" \
  "s_add_u32 s54, s52, 2097152\n\t" \
  "s_add_u32 s55, s52, 3145728\n\t" \
  "v_cvt_pk_bf16_f32 v152, v120, v121\n\t" \
  "v_cvt_pk_bf16_f32 v153, v122, v123\n\t" \
  "v_mfma_f32_32x32x16_bf16 v[88:103], v[208:211], v[8:11], v[88:103]\n\t" \
  "ds_write_b64 v232, v[152:153]\n\t" \
  "v_cvt_pk_bf16_f32 v154, v136, v137\n\t" \
  "v_cvt_pk_bf16_f32 v155, v138, v139\n\t" \
  "ds_write_b64 v236, v[154:155]\n\t" \
  "v_cvt_pk_bf16_f32 v156, v124, v125\n\t" \
  "v_cvt_pk_bf16_f32 v157, v126, v127\n\t" \
  "v_mfma_f32_32x32x16_bf16 v[104:119], v[200:203], v[16:19], v[104:119]\n\t" \
  "ds_write_b64 v233, v[156:157]\n\t" \
  "v_cvt_pk_bf16_f32 v158, v140, v141\n\t" \
  "v_cvt_pk_bf16_f32 v159, v142, v143\n\t" \
  "ds_write_b64 v237, v[158:159]\n\t" \
  "v_cvt_pk_bf16_f32 v160, v128, v129\n\t" \
  "v_cvt_pk_bf16_f32 v161, v130, v131\n\t" \
  "ds_write_b64 v234, v[160:161]\n\t" \
  "v_mfma_f32_32x32x16_bf16 v[88:103], v[204:207], v[4:7], v[88:103]\n\t" \
  "v_cvt_pk_bf16_f32 v162, v144, v145\n\t" \
  "v_cvt_pk_bf16_f32 v163, v146, v147\n\t" \
  "ds_write_b64 v238, v[162:163]\n\t" \
  "v_cvt_pk_bf16_f32 v164, v132, v133\n\t" \
  "v_cvt_pk_bf16_f32 v165, v134, v135\n\t" \
  "ds_write_b64 v235, v[164:165]\n\t" \
  "v_mfma_f32_32x32x16_bf16 v[104:119], v[212:215], v[4:7], v[104:119]\n\t" \
  "v_cvt_pk_bf16_f32 v166, v148, v149\n\t" \
  "v_cvt_pk_bf16_f32 v167, v150, v151\n\t" \
  "ds_write_b64 v239, v[166:167]\n\t" \
  "ds_read_b128 v[168:171], v240 offset:0\n\t" \
  "ds_read_b128 v[172:175], v240 offset:1024\n\t" \
  "ds_read_b128 v[176:179], v240 offset:2048\n\t" \
  "ds_read_b128 v[180:183], v240 offset:3072\n\t" \
  "v_mfma_f32_32x32x16_bf16 v[88:103], v[212:215], v[12:15], v[88:103]\n\t" \
  "v_mfma_f32_32x32x16_bf16 v[104:119], v[204:207], v[20:23], v[104:119]\n\t" \
  "s_waitcnt lgkmcnt(3)\n\t" \
  "buffer_store_dwordx4 v[168:171], v241, s[40:43], s52 offen sc1\n\t" \
  "s_waitcnt lgkmcnt(2)\n\t" \
  "buffer_store_dwordx4 v[172:175], v241, s[40:43], s53 offen sc1\n\t" \
  "s_waitcnt lgkmcnt(1)\n\t" \
  "buffer_store_dwordx4 v[176:179], v241, s[40:43], s54 offen sc1\n\t" \
  "s_waitcnt lgkmcnt(0)\n\t" \
  "buffer_store_dwordx4 v[180:183], v241, s[40:43], s55 offen sc1\n\t" \
  "s_sub_u32 s52, 1024, s48\n\t" \
  "s_cmp_eq_u32 s48, 512\n\t" \
  "s_cselect_b32 s52, 0, s52\n\t" \
  "s_lshl_b32 s52, s52, 7\n\t" \
  "s_add_u32 s53, s52, 1048576\n\t" \
  "s_add_u32 s54, s52, 2097152\n\t" \
  "s_add_u32 s55, s52, 3145728\n\t" \
  "v_cvt_pk_bf16_f32 v152, v88, v89\n\t" \
  "v_cvt_pk_bf16_f32 v153, v90, v91\n\t" \
  "ds_write_b64 v232, v[152:153]\n\t" \
  "v_cvt_pk_bf16_f32 v154, v104, v105\n\t" \
  "v_cvt_pk_bf16_f32 v155, v106, v107\n\t" \
  "ds_write_b64 v236, v[154:155]\n\t" \
  "v_cvt_pk_bf16_f32 v156, v92, v93\n\t" \
  "v_cvt_pk_bf16_f32 v157, v94, v95\n\t" \
  "ds_write_b64 v233, v[156:157]\n\t" \
  "v_cvt_pk_bf16_f32 v158, v108, v109\n\t" \
  "v_cvt_pk_bf16_f32 v159, v110, v111\n\t" \
  "ds_write_b64 v237, v[158:159]\n\t" \
  "v_cvt_pk_bf16_f32 v160, v96, v97\n\t" \
  "v_cvt_pk_bf16_f32 v161, v98, v99\n\t" \
  "ds_write_b64 v234, v[160:161]\n\t" \
  "v_cvt_pk_bf16_f32 v162, v112, v113\n\t" \
  "v_cvt_pk_bf16_f32 v163, v114, v115\n\t" \
  "ds_write_b64 v238, v[162:163]\n\t" \
  "v_cvt_pk_bf16_f32 v164, v100, v101\n\t" \
  "v_cvt_pk_bf16_f32 v165, v102, v103\n\t" \
  "ds_write_b64 v235, v[164:165]\n\t" \
  "v_cvt_pk_bf16_f32 v166, v116, v117\n\t" \
  "v_cvt_pk_bf16_f32 v167, v118, v119\n\t" \
  "ds_write_b64 v239, v[166:167]\n\t" \
  "ds_read_b128 v[168:171], v240 offset:0\n\t" \
  "ds_read_b128 v[172:175], v240 offset:1024\n\t" \
  "ds_read_b128 v[176:179], v240 offset:2048\n\t" \
  "ds_read_b128 v[180:183], v240 offset:3072\n\t" \
  "s_mov_b32 s48, s49\n\t" \
  "s_mov_b32 s49, s68\n\t" \
  "s_mov_b32 s69, s71\n\t" \
  "s_waitcnt lgkmcnt(3)\n\t" \
  "buffer_store_dwordx4 v[168:171], v241, s[40:43], s52 offen sc1\n\t" \
  "s_waitcnt lgkmcnt(2)\n\t" \
  "buffer_store_dwordx4 v[172:175], v241, s[40:43], s53 offen sc1\n\t" \
  "s_waitcnt lgkmcnt(1)\n\t" \
  "buffer_store_dwordx4 v[176:179], v241, s[40:43], s54 offen sc1\n\t" \
  "s_waitcnt lgkmcnt(0)\n\t" \
  "buffer_store_dwordx4 v[180:183], v241, s[40:43], s55 offen sc1\n\t" \
  "s_cmp_lg_u32 s69, s70\n\t" \
  "s_cbranch_scc1 1b\n\t" \
  "s_waitcnt vmcnt(0)\n\t" \
  "s_barrier\n\t" \
  "s_branch 9f\n\t" \
  "7:\n\t" \
  "s_waitcnt vmcnt(0)\n\t" \
  "v_subrev_u32 v224, s50, v230\n\t" \
  "v_lshlrev_b32_e64 v224, 1, v224\n\t" \
  "v_sub_u32 v225, 640, v224\n\t" \
  "v_add_u32 v224, s51, v224\n\t" \
  "v_add_u32 v225, s51, v225\n\t" \
  "ds_write_b32 v224, v56 offset:0\n\t" \
  "ds_write_b32 v224, v57 offset:128\n\t" \
  "ds_write_b32 v224, v58 offset:256\n\t" \
  "ds_write_b32 v224, v59 offset:384\n\t" \
  "ds_write_b32 v224, v60 offset:1024\n\t" \
  "ds_write_b32 v224, v61 offset:1152\n\t" \
  "ds_write_b32 v224, v62 offset:1280\n\t" \
  "ds_write_b32 v224, v63 offset:1408\n\t" \
  "ds_write_b32 v224, v64 offset:2048\n\t" \
  "ds_write_b32 v224, v65 offset:2176\n\t" \
  "ds_write_b32 v224, v66 offset:2304\n\t" \
  "ds_write_b32 v224, v67 offset:2432\n\t" \
  "ds_write_b32 v224, v68 offset:3072\n\t" \
  "ds_write_b32 v224, v69 offset:3200\n\t" \
  "ds_write_b32 v224, v70 offset:3328\n\t" \
  "ds_write_b32 v224, v71 offset:3456\n\t" \
  "ds_write_b32 v224, v72 offset:4096\n\t" \
  "ds_write_b32 v224, v73 offset:4224\n\t" \
  "ds_write_b32 v224, v74 offset:4352\n\t" \
  "ds_write_b32 v224, v75 offset:4480\n\t" \
  "ds_write_b32 v224, v76 offset:5120\n\t" \
  "ds_write_b32 v224, v77 offset:5248\n\t" \
  "ds_write_b32 v224, v78 offset:5376\n\t" \
  "ds_write_b32 v224, v79 offset:5504\n\t" \
  "ds_write_b32 v224, v80 offset:6144\n\t" \
  "ds_write_b32 v224, v81 offset:6272\n\t" \
  "ds_write_b32 v224, v82 offset:6400\n\t" \
  "ds_write_b32 v224, v83 offset:6528\n\t" \
  "ds_write_b32 v224, v84 offset:7168\n\t" \
  "ds_write_b32 v224, v85 offset:7296\n\t" \
  "ds_write_b32 v224, v86 offset:7424\n\t" \
  "ds_write_b32 v224, v87 offset:7552\n\t" \
  "ds_read_u16_d16_hi v184, v247 offset:704\n\t" \
  "ds_read_u16_d16_hi v192, v247 offset:1728\n\t" \
  "ds_read_u16_d16_hi v200, v247 offset:4800\n\t" \
  "ds_read_u16_d16_hi v208, v247 offset:5824\n\t" \
  "ds_read_u16_d16_hi v185, v247 offset:640\n\t" \
  "ds_read_u16_d16_hi v193, v247 offset:1664\n\t" \
  "ds_read_u16_d16_hi v201, v247 offset:4736\n\t" \
  "ds_read_u16_d16_hi v209, v247 offset:5760\n\t" \
  "ds_read_u16_d16_hi v186, v247 offset:576\n\t" \
  "ds_read_u16_d16_hi v194, v247 offset:1600\n\t" \
  "ds_read_u16_d16_hi v202, v247 offset:4672\n\t" \
  "ds_read_u16_d16_hi v210, v247 offset:5696\n\t" \
  "ds_read_u16_d16_hi v187, v247 offset:512\n\t" \
  "ds_read_u16_d16_hi v195, v247 offset:1536\n\t" \
  "ds_read_u16_d16_hi v203, v247 offset:4608\n\t" \
  "ds_read_u16_d16_hi v211, v247 offset:5632\n\t" \
  "ds_read_u16_d16_hi v188, v247 offset:192\n\t" \
  "ds_read_u16_d16_hi v196, v247 offset:1216\n\t" \
  "ds_read_u16_d16_hi v204, v247 offset:4288\n\t" \
  "ds_read_u16_d16_hi v212, v247 offset:5312\n\t" \
  "ds_read_u16_d16_hi v189, v247 offset:128\n\t" \
  "ds_read_u16_d16_hi v197, v247 offset:1152\n\t" \
  "ds_read_u16_d16_hi v205, v247 offset:4224\n\t" \
  "ds_read_u16_d16_hi v213, v247 offset:5248\n\t" \
  "ds_read_u16_d16_hi v190, v247 offset:64\n\t" \
  "ds_read_u16_d16_hi v198, v247 offset:1088\n\t" \
  "ds_read_u16_d16_hi v206, v247 offset:4160\n\t" \
  "ds_read_u16_d16_hi v214, v247 offset:5184\n\t" \
  "ds_read_u16_d16_hi v191, v247 offset:0\n\t" \
  "ds_read_u16_d16_hi v199, v247 offset:1024\n\t" \
  "ds_read_u16_d16_hi v207, v247 offset:4096\n\t" \
  "ds_read_u16_d16_hi v215, v247 offset:5120\n\t" \
  "v_pk_fma_f32 v[220:221], v[120:121], s[72:73], v[120:121] op_sel_hi:[1,0,1]\n\t" \
  "v_pk_fma_f32 v[222:223], v[136:137], s[72:73], v[136:137] op_sel_hi:[1,0,1]\n\t" \
  "v_pk_mul_f32 v[120:121], v[222:223], v[112:113]\n\t" \
  "v_pk_mul_f32 v[136:137], v[220:221], v[112:113]\n\t" \
  "v_pk_fma_f32 v[88:89], v[220:221], v[104:105], v[120:121] neg_lo:[0,0,1] neg_hi:[0,0,1]\n\t" \
  "v_pk_fma_f32 v[96:97], v[222:223], v[104:105], v[136:137]\n\t" \
  "v_cvt_pk_bf16_f32 v152, v88, v89\n\t" \
  "v_cvt_pk_bf16_f32 v160, v96, v97\n\t" \
  "v_pk_fma_f32 v[220:221], v[122:123], s[72:73], v[122:123] op_sel_hi:[1,0,1]\n\t" \
  "v_pk_fma_f32 v[222:223], v[138:139], s[72:73], v[138:139] op_sel_hi:[1,0,1]\n\t" \
  "v_pk_mul_f32 v[122:123], v[222:223], v[114:115]\n\t" \
  "v_pk_mul_f32 v[138:139], v[220:221], v[114:115]\n\t" \
  "v_pk_fma_f32 v[90:91], v[220:221], v[106:107], v[122:123] neg_lo:[0,0,1] neg_hi:[0,0,1]\n\t" \
  "v_pk_fma_f32 v[98:99], v[222:223], v[106:107], v[138:139]\n\t" \
  "v_cvt_pk_bf16_f32 v153, v90, v91\n\t" \
  "v_cvt_pk_bf16_f32 v161, v98, v99\n\t" \
  "v_pk_fma_f32 v[220:221], v[124:125], s[72:73], v[124:125] op_sel_hi:[1,0,1]\n\t" \
  "v_pk_fma_f32 v[222:223], v[140:141], s[72:73], v[140:141] op_sel_hi:[1,0,1]\n\t" \
  "v_pk_mul_f32 v[124:125], v[222:223], v[116:117]\n\t" \
  "v_pk_mul_f32 v[140:141], v[220:221], v[116:117]\n\t" \
  "v_pk_fma_f32 v[92:93], v[220:221], v[108:109], v[124:125] neg_lo:[0,0,1] neg_hi:[0,0,1]\n\t" \
  "v_pk_fma_f32 v[100:101], v[222:223], v[108:109], v[140:141]\n\t" \
  "v_cvt_pk_bf16_f32 v154, v92, v93\n\t" \
  "v_cvt_pk_bf16_f32 v162, v100, v101\n\t" \
  "v_pk_fma_f32 v[220:221], v[126:127], s[72:73], v[126:127] op_sel_hi:[1,0,1]\n\t" \
  "v_pk_fma_f32 v[222:223], v[142:143], s[72:73], v[142:143] op_sel_hi:[1,0,1]\n\t" \
  "v_pk_mul_f32 v[126:127], v[222:223], v[118:119]\n\t" \
  "v_pk_mul_f32 v[142:143], v[220:221], v[118:119]\n\t" \
  "v_pk_fma_f32 v[94:95], v[220:221], v[110:111], v[126:127] neg_lo:[0,0,1] neg_hi:[0,0,1]\n\t" \
  "v_pk_fma_f32 v[102:103], v[222:223], v[110:111], v[142:143]\n\t" \
  "v_cvt_pk_bf16_f32 v155, v94, v95\n\t" \
  "v_cvt_pk_bf16_f32 v163, v102, v103\n\t" \
  "v_readlane_b32 s76, v245, m0\n\t" \
  "s_add_u32 m0, m0, 1\n\t" \
  "v_readlane_b32 s77, v245, m0\n\t" \
  "s_add_u32 m0, m0, 1\n\t" \
  "v_readlane_b32 s78, v245, m0\n\t" \
  "s_add_u32 m0, m0, 1\n\t" \
  "v_readlane_b32 s79, v245, m0\n\t" \
  "s_add_u32 m0, m0, 1\n\t" \
  "s_waitcnt lgkmcnt(14)\n\t" \
  "s_nop 0\n\t" \
  "v_writelane_b32 v184, s76, 0\n\t" \
  "v_writelane_b32 v192, s77, 0\n\t" \
  "v_writelane_b32 v200, s78, 0\n\t" \
  "v_writelane_b32 v208, s79, 0\n\t" \
  "v_pk_fma_f32 v[220:221], v[128:129], s[72:73], v[128:129] op_sel_hi:[1,0,1]\n\t" \
  "v_pk_fma_f32 v[222:223], v[144:145], s[72:73], v[144:145] op_sel_hi:[1,0,1]\n\t" \
  "v_pk_mul_f32 v[128:129], v[222:223], v[208:209]\n\t" \
  "v_pk_mul_f32 v[144:145], v[220:221], v[208:209]\n\t" \
  "v_pk_fma_f32 v[184:185], v[220:221], v[200:201], v[128:129]\n\t" \
  "v_pk_fma_f32 v[192:193], v[222:223], v[200:201], v[144:145] neg_lo:[0,0,1] neg_hi:[0,0,1]\n\t" \
  "v_cvt_pk_bf16_f32 v156, v184, v185\n\t" \
  "v_cvt_pk_bf16_f32 v164, v192, v193\n\t" \
  "v_pk_fma_f32 v[220:221], v[130:131], s[72:73], v[130:131] op_sel_hi:[1,0,1]\n\t" \
  "v_pk_fma_f32 v[222:223], v[146:147], s[72:73], v[146:147] op_sel_hi:[1,0,1]\n\t" \
  "v_pk_mul_f32 v[130:131], v[222:223], v[210:211]\n\t" \
  "v_pk_mul_f32 v[146:147], v[220:221], v[210:211]\n\t" \
  "v_pk_fma_f32 v[186:187], v[220:221], v[202:203], v[130:131]\n\t" \
  "v_pk_fma_f32 v[194:195], v[222:223], v[202:203], v[146:147] neg_lo:[0,0,1] neg_hi:[0,0,1]\n\t" \
  "v_cvt_pk_bf16_f32 v157, v186, v187\n\t" \
  "v_cvt_pk_bf16_f32 v165, v194, v195\n\t" \
  "v_pk_fma_f32 v[220:221], v[132:133], s[72:73], v[132:133] op_sel_hi:[1,0,1]\n\t" \
  "v_pk_fma_f32 v[222:223], v[148:149], s[72:73], v[148:149] op_sel_hi:[1,0,1]\n\t" \
  "s_waitcnt lgkmcnt(8)\n\t" \
  "s_nop 0\n\t" \
  "v_pk_mul_f32 v[132:133], v[222:223], v[212:213]\n\t" \
  "v_pk_mul_f32 v[148:149], v[220:221], v[212:213]\n\t" \
  "v_pk_fma_f32 v[188:189], v[220:221], v[204:205], v[132:133]\n\t" \
  "v_pk_fma_f32 v[196:197], v[222:223], v[204:205], v[148:149] neg_lo:[0,0,1] neg_hi:[0,0,1]\n\t" \
  "v_cvt_pk_bf16_f32 v158, v188, v189\n\t" \
  "v_cvt_pk_bf16_f32 v166, v196, v197\n\t" \
  "v_pk_fma_f32 v[220:221], v[134:135], s[72:73], v[134:135] op_sel_hi:[1,0,1]\n\t" \
  "v_pk_fma_f32 v[222:223], v[150:151], s[72:73], v[150:151] op_sel_hi:[1,0,1]\n\t" \
  "s_waitcnt lgkmcnt(0)\n\t" \
  "s_nop 0\n\t" \
  "v_pk_mul_f32 v[134:135], v[222:223], v[214:215]\n\t" \
  "v_pk_mul_f32 v[150:151], v[220:221], v[214:215]\n\t" \
  "v_pk_fma_f32 v[190:191], v[220:221], v[206:207], v[134:135]\n\t" \
  "v_pk_fma_f32 v[198:199], v[222:223], v[206:207], v[150:151] neg_lo:[0,0,1] neg_hi:[0,0,1]\n\t" \
  "v_cvt_pk_bf16_f32 v159, v190, v191\n\t" \
  "v_cvt_pk_bf16_f32 v167, v198, v199\n\t" \
  "ds_read_b32 v120, v225 offset:3456\n\t" \
  "ds_read_b32 v121, v225 offset:3328\n\t" \
  "ds_read_b32 v122, v225 offset:3200\n\t" \
  "ds_read_b32 v123, v225 offset:3072\n\t" \
  "ds_read_b32 v124, v225 offset:2432\n\t" \
  "ds_read_b32 v125, v225 offset:2304\n\t" \
  "ds_read_b32 v126, v225 offset:2176\n\t" \
  "ds_read_b32 v127, v225 offset:2048\n\t" \
  "ds_read_b32 v128, v225 offset:1408\n\t" \
  "ds_read_b32 v129, v225 offset:1280\n\t" \
  "ds_read_b32 v130, v225 offset:1152\n\t" \
  "ds_read_b32 v131, v225 offset:1024\n\t" \
  "ds_read_b32 v132, v225 offset:384\n\t" \
  "ds_read_b32 v133, v225 offset:256\n\t" \
  "ds_read_b32 v134, v225 offset:128\n\t" \
  "ds_read_b32 v135, v225 offset:0\n\t" \
  "ds_read_b32 v136, v225 offset:7552\n\t" \
  "ds_read_b32 v137, v225 offset:7424\n\t" \
  "ds_read_b32 v138, v225 offset:7296\n\t" \
  "ds_read_b32 v139, v225 offset:7168\n\t" \
  "ds_read_b32 v140, v225 offset:6528\n\t" \
  "ds_read_b32 v141, v225 offset:6400\n\t" \
  "ds_read_b32 v142, v225 offset:6272\n\t" \
  "ds_read_b32 v143, v225 offset:6144\n\t" \
  "ds_read_b32 v144, v225 offset:5504\n\t" \
  "ds_read_b32 v145, v225 offset:5376\n\t" \
  "ds_read_b32 v146, v225 offset:5248\n\t" \
  "ds_read_b32 v147, v225 offset:5120\n\t" \
  "ds_read_b32 v148, v225 offset:4480\n\t" \
  "ds_read_b32 v149, v225 offset:4352\n\t" \
  "ds_read_b32 v150, v225 offset:4224\n\t" \
  "ds_read_b32 v151, v225 offset:4096\n\t" \
  "v_subrev_u32 v247, 2048, v247\n\t" \
  "ds_read_u16_d16_hi v88, v246 offset:0\n\t" \
  "ds_read_u16_d16_hi v96, v246 offset:1024\n\t" \
  "ds_read_u16_d16_hi v104, v246 offset:2048\n\t" \
  "ds_read_u16_d16_hi v112, v246 offset:3072\n\t" \
  "ds_read_u16_d16_hi v89, v246 offset:64\n\t" \
  "ds_read_u16_d16_hi v97, v246 offset:1088\n\t" \
  "ds_read_u16_d16_hi v105, v246 offset:2112\n\t" \
  "ds_read_u16_d16_hi v113, v246 offset:3136\n\t" \
  "ds_read_u16_d16_hi v90, v246 offset:128\n\t" \
  "ds_read_u16_d16_hi v98, v246 offset:1152\n\t" \
  "ds_read_u16_d16_hi v106, v246 offset:2176\n\t" \
  "ds_read_u16_d16_hi v114, v246 offset:3200\n\t" \
  "ds_read_u16_d16_hi v91, v246 offset:192\n\t" \
  "ds_read_u16_d16_hi v99, v246 offset:1216\n\t" \
  "ds_read_u16_d16_hi v107, v246 offset:2240\n\t" \
  "ds_read_u16_d16_hi v115, v246 offset:3264\n\t" \
  "ds_read_u16_d16_hi v92, v246 offset:512\n\t" \
  "ds_read_u16_d16_hi v100, v246 offset:1536\n\t" \
  "ds_read_u16_d16_hi v108, v246 offset:2560\n\t" \
  "ds_read_u16_d16_hi v116, v246 offset:3584\n\t" \
  "ds_read_u16_d16_hi v93, v246 offset:576\n\t" \
  "ds_read_u16_d16_hi v101, v246 offset:1600\n\t" \
  "ds_read_u16_d16_hi v109, v246 offset:2624\n\t" \
  "ds_read_u16_d16_hi v117, v246 offset:3648\n\t" \
  "ds_read_u16_d16_hi v94, v246 offset:640\n\t" \
  "ds_read_u16_d16_hi v102, v246 offset:1664\n\t" \
  "ds_read_u16_d16_hi v110, v246 offset:2688\n\t" \
  "ds_read_u16_d16_hi v118, v246 offset:3712\n\t" \
  "ds_read_u16_d16_hi v95, v246 offset:704\n\t" \
  "ds_read_u16_d16_hi v103, v246 offset:1728\n\t" \
  "ds_read_u16_d16_hi v111, v246 offset:2752\n\t" \
  "ds_read_u16_d16_hi v119, v246 offset:3776\n\t" \
  "ds_read_u16_d16_hi v184, v247 offset:704\n\t" \
  "ds_read_u16_d16_hi v192, v247 offset:1728\n\t" \
  "ds_read_u16_d16_hi v200, v247 offset:2752\n\t" \
  "ds_read_u16_d16_hi v208, v247 offset:3776\n\t" \
  "ds_read_u16_d16_hi v185, v247 offset:640\n\t" \
  "ds_read_u16_d16_hi v193, v247 offset:1664\n\t" \
  "ds_read_u16_d16_hi v201, v247 offset:2688\n\t" \
  "ds_read_u16_d16_hi v209, v247 offset:3712\n\t" \
  "ds_read_u16_d16_hi v186, v247 offset:576\n\t" \
  "ds_read_u16_d16_hi v194, v247 offset:1600\n\t" \
  "ds_read_u16_d16_hi v202, v247 offset:2624\n\t" \
  "ds_read_u16_d16_hi v210, v247 offset:3648\n\t" \
  "ds_read_u16_d16_hi v187, v247 offset:512\n\t" \
  "ds_read_u16_d16_hi v195, v247 offset:1536\n\t" \
  "ds_read_u16_d16_hi v203, v247 offset:2560\n\t" \
  "ds_read_u16_d16_hi v211, v247 offset:3584\n\t" \
  "ds_read_u16_d16_hi v188, v247 offset:192\n\t" \
  "ds_read_u16_d16_hi v196, v247 offset:1216\n\t" \
  "ds_read_u16_d16_hi v204, v247 offset:2240\n\t" \
  "ds_read_u16_d16_hi v212, v247 offset:3264\n\t" \
  "ds_read_u16_d16_hi v189, v247 offset:128\n\t" \
  "ds_read_u16_d16_hi v197, v247 offset:1152\n\t" \
  "ds_read_u16_d16_hi v205, v247 offset:2176\n\t" \
  "ds_read_u16_d16_hi v213, v247 offset:3200\n\t" \
  "ds_read_u16_d16_hi v190, v247 offset:64\n\t" \
  "ds_read_u16_d16_hi v198, v247 offset:1088\n\t" \
  "ds_read_u16_d16_hi v206, v247 offset:2112\n\t" \
  "ds_read_u16_d16_hi v214, v247 offset:3136\n\t" \
  "ds_read_u16_d16_hi v191, v247 offset:0\n\t" \
  "ds_read_u16_d16_hi v199, v247 offset:1024\n\t" \
  "ds_read_u16_d16_hi v207, v247 offset:2048\n\t" \
  "ds_read_u16_d16_hi v215, v247 offset:3072\n\t" \
  "v_readlane_b32 s76, v56, 0\n\t" \
  "v_readlane_b32 s77, v72, 0\n\t" \
  "s_waitcnt lgkmcnt(14)\n\t" \
  "s_nop 1\n\t" \
  "v_writelane_b32 v120, s76, 0\n\t" \
  "v_writelane_b32 v136, s77, 0\n\t" \
  "s_mov_b32 m0, 16\n\t" \
  "v_readlane_b32 s76, v245, m0\n\t" \
  "s_add_u32 m0, m0, 1\n\t" \
  "v_readlane_b32 s77, v245, m0\n\t" \
  "s_add_u32 m0, m0, 1\n\t" \
  "v_readlane_b32 s78, v245, m0\n\t" \
  "s_add_u32 m0, m0, 1\n\t" \
  "v_readlane_b32 s79, v245, m0\n\t" \
  "s_add_u32 m0, m0, 1\n\t" \
  "s_nop 0\n\t" \
  "v_writelane_b32 v88, s76, 0\n\t" \
  "v_writelane_b32 v96, s77, 0\n\t" \
  "v_writelane_b32 v104, s78, 0\n\t" \
  "v_writelane_b32 v112, s79, 0\n\t" \
  "v_readlane_b32 s76, v245, m0\n\t" \
  "s_add_u32 m0, m0, 1\n\t" \
  "v_readlane_b32 s77, v245, m0\n\t" \
  "s_add_u32 m0, m0, 1\n\t" \
  "v_readlane_b32 s78, v245, m0\n\t" \
  "s_add_u32 m0, m0, 1\n\t" \
  "v_readlane_b32 s79, v245, m0\n\t" \
  "s_add_u32 m0, m0, 1\n\t" \
  "v_writelane_b32 v184, s76, 0\n\t" \
  "v_writelane_b32 v192, s77, 0\n\t" \
  "v_writelane_b32 v200, s78, 0\n\t" \
  "v_writelane_b32 v208, s79, 0\n\t" \
  "v_pk_fma_f32 v[216:217], v[136:137], s[72:73], v[56:57] op_sel_hi:[1,0,1]\n\t" \
  "v_pk_fma_f32 v[218:219], v[120:121], s[72:73], v[72:73] op_sel_hi:[1,0,1]\n\t" \
  "v_pk_fma_f32 v[220:221], v[72:73], s[72:73], v[120:121] op_sel_hi:[1,0,1] neg_lo:[1,0,0] neg_hi:[1,0,0]\n\t" \
  "v_pk_fma_f32 v[222:223], v[56:57], s[72:73], v[136:137] op_sel_hi:[1,0,1] neg_lo:[1,0,0] neg_hi:[1,0,0]\n\t" \
  "v_pk_mul_f32 v[120:121], v[216:217], v[88:89]\n\t" \
  "v_pk_mul_f32 v[136:137], v[216:217], v[96:97]\n\t" \
  "v_pk_mul_f32 v[56:57], v[220:221], v[104:105]\n\t" \
  "v_pk_mul_f32 v[72:73], v[220:221], v[112:113]\n\t" \
  "v_pk_fma_f32 v[120:121], v[218:219], v[96:97], v[120:121]\n\t" \
  "v_pk_fma_f32 v[136:137], v[218:219], v[88:89], v[136:137] neg_lo:[1,0,0] neg_hi:[1,0,0]\n\t" \
  "v_pk_fma_f32 v[56:57], v[222:223], v[112:113], v[56:57] neg_lo:[1,0,0] neg_hi:[1,0,0]\n\t" \
  "v_pk_fma_f32 v[72:73], v[222:223], v[104:105], v[72:73]\n\t" \
  "v_pk_fma_f32 v[104:105], v[72:73], s[72:73], v[120:121] op_sel_hi:[1,0,1] neg_lo:[1,0,0] neg_hi:[1,0,0]\n\t" \
  "v_pk_fma_f32 v[112:113], v[56:57], s[72:73], v[136:137] op_sel_hi:[1,0,1] neg_lo:[1,0,1] neg_hi:[1,0,1]\n\t" \
  "v_cvt_pk_bf16_f32 v168, v104, v105\n\t" \
  "v_cvt_pk_bf16_f32 v176, v112, v113\n\t" \
  "v_pk_fma_f32 v[216:217], v[138:139], s[72:73], v[58:59] op_sel_hi:[1,0,1]\n\t" \
  "v_pk_fma_f32 v[218:219], v[122:123], s[72:73], v[74:75] op_sel_hi:[1,0,1]\n\t" \
  "v_pk_fma_f32 v[220:221], v[74:75], s[72:73], v[122:123] op_sel_hi:[1,0,1] neg_lo:[1,0,0] neg_hi:[1,0,0]\n\t" \
  "v_pk_fma_f32 v[222:223], v[58:59], s[72:73], v[138:139] op_sel_hi:[1,0,1] neg_lo:[1,0,0] neg_hi:[1,0,0]\n\t" \
  "v_pk_mul_f32 v[122:123], v[216:217], v[90:91]\n\t" \
  "v_pk_mul_f32 v[138:139], v[216:217], v[98:99]\n\t" \
  "v_pk_mul_f32 v[58:59], v[220:221], v[106:107]\n\t" \
  "v_pk_mul_f32 v[74:75], v[220:221], v[114:115]\n\t" \
  "v_pk_fma_f32 v[122:123], v[218:219], v[98:99], v[122:123]\n\t" \
  "v_pk_fma_f32 v[138:139], v[218:219], v[90:91], v[138:139] neg_lo:[1,0,0] neg_hi:[1,0,0]\n\t" \
  "v_pk_fma_f32 v[58:59], v[222:223], v[114:115], v[58:59] neg_lo:[1,0,0] neg_hi:[1,0,0]\n\t" \
  "v_pk_fma_f32 v[74:75], v[222:223], v[106:107], v[74:75]\n\t" \
  "v_pk_fma_f32 v[106:107], v[74:75], s[72:73], v[122:123] op_sel_hi:[1,0,1] neg_lo:[1,0,0] neg_hi:[1,0,0]\n\t" \
  "v_pk_fma_f32 v[114:115], v[58:59], s[72:73], v[138:139] op_sel_hi:[1,0,1] neg_lo:[1,0,1] neg_hi:[1,0,1]\n\t" \
  "v_cvt_pk_bf16_f32 v169, v106, v107\n\t" \
  "v_cvt_pk_bf16_f32 v177, v114, v115\n\t" \
  "v_pk_fma_f32 v[216:217], v[140:141], s[72:73], v[60:61] op_sel_hi:[1,0,1]\n\t" \
  "v_pk_fma_f32 v[218:219], v[124:125], s[72:73], v[76:77] op_sel_hi:[1,0,1]\n\t" \
  "v_pk_fma_f32 v[220:221], v[76:77], s[72:73], v[124:125] op_sel_hi:[1,0,1] neg_lo:[1,0,0] neg_hi:[1,0,0]\n\t" \
  "v_pk_fma_f32 v[222:223], v[60:61], s[72:73], v[140:141] op_sel_hi:[1,0,1] neg_lo:[1,0,0] neg_hi:[1,0,0]\n\t" \
  "v_pk_mul_f32 v[124:125], v[216:217], v[92:93]\n\t" \
  "v_pk_mul_f32 v[140:141], v[216:217], v[100:101]\n\t" \
  "v_pk_mul_f32 v[60:61], v[220:221], v[108:109]\n\t" \
  "v_pk_mul_f32 v[76:77], v[220:221], v[116:117]\n\t" \
  "v_pk_fma_f32 v[124:125], v[218:219], v[100:101], v[124:125]\n\t" \
  "v_pk_fma_f32 v[140:141], v[218:219], v[92:93], v[140:141] neg_lo:[1,0,0] neg_hi:[1,0,0]\n\t" \
  "v_pk_fma_f32 v[60:61], v[222:223], v[116:117], v[60:61] neg_lo:[1,0,0] neg_hi:[1,0,0]\n\t" \
  "v_pk_fma_f32 v[76:77], v[222:223], v[108:109], v[76:77]\n\t" \
  "v_pk_fma_f32 v[108:109], v[76:77], s[72:73], v[124:125] op_sel_hi:[1,0,1] neg_lo:[1,0,0] neg_hi:[1,0,0]\n\t" \
  "v_pk_fma_f32 v[116:117], v[60:61], s[72:73], v[140:141] op_sel_hi:[1,0,1] neg_lo:[1,0,1] neg_hi:[1,0,1]\n\t" \
  "v_cvt_pk_bf16_f32 v170, v108, v109\n\t" \
  "v_cvt_pk_bf16_f32 v178, v116, v117\n\t" \
  "v_pk_fma_f32 v[216:217], v[142:143], s[72:73], v[62:63] op_sel_hi:[1,0,1]\n\t" \
  "v_pk_fma_f32 v[218:219], v[126:127], s[72:73], v[78:79] op_sel_hi:[1,0,1]\n\t" \
  "v_pk_fma_f32 v[220:221], v[78:79], s[72:73], v[126:127] op_sel_hi:[1,0,1] neg_lo:[1,0,0] neg_hi:[1,0,0]\n\t" \
  "v_pk_fma_f32 v[222:223], v[62:63], s[72:73], v[142:143] op_sel_hi:[1,0,1] neg_lo:[1,0,0] neg_hi:[1,0,0]\n\t" \
  "v_pk_mul_f32 v[126:127], v[216:217], v[94:95]\n\t" \
  "v_pk_mul_f32 v[142:143], v[216:217], v[102:103]\n\t" \
  "v_pk_mul_f32 v[62:63], v[220:221], v[110:111]\n\t" \
  "v_pk_mul_f32 v[78:79], v[220:221], v[118:119]\n\t" \
  "v_pk_fma_f32 v[126:127], v[218:219], v[102:103], v[126:127]\n\t" \
  "v_pk_fma_f32 v[142:143], v[218:219], v[94:95], v[142:143] neg_lo:[1,0,0] neg_hi:[1,0,0]\n\t" \
  "v_pk_fma_f32 v[62:63], v[222:223], v[118:119], v[62:63] neg_lo:[1,0,0] neg_hi:[1,0,0]\n\t" \
  "v_pk_fma_f32 v[78:79], v[222:223], v[110:111], v[78:79]\n\t" \
  "v_pk_fma_f32 v[110:111], v[78:79], s[72:73], v[126:127] op_sel_hi:[1,0,1] neg_lo:[1,0,0] neg_hi:[1,0,0]\n\t" \
  "v_pk_fma_f32 v[118:119], v[62:63], s[72:73], v[142:143] op_sel_hi:[1,0,1] neg_lo:[1,0,1] neg_hi:[1,0,1]\n\t" \
  "v_cvt_pk_bf16_f32 v171, v110, v111\n\t" \
  "v_cvt_pk_bf16_f32 v179, v118, v119\n\t" \
  "v_pk_fma_f32 v[216:217], v[144:145], s[72:73], v[64:65] op_sel_hi:[1,0,1]\n\t" \
  "v_pk_fma_f32 v[218:219], v[128:129], s[72:73], v[80:81] op_sel_hi:[1,0,1]\n\t" \
  "v_pk_fma_f32 v[220:221], v[80:81], s[72:73], v[128:129] op_sel_hi:[1,0,1] neg_lo:[1,0,0] neg_hi:[1,0,0]\n\t" \
  "v_pk_fma_f32 v[222:223], v[64:65], s[72:73], v[144:145] op_sel_hi:[1,0,1] neg_lo:[1,0,0] neg_hi:[1,0,0]\n\t" \
  "v_pk_mul_f32 v[128:129], v[216:217], v[184:185]\n\t" \
  "v_pk_mul_f32 v[144:145], v[216:217], v[192:193]\n\t" \
  "v_pk_mul_f32 v[64:65], v[220:221], v[200:201]\n\t" \
  "v_pk_mul_f32 v[80:81], v[222:223], v[200:201]\n\t" \
  "v_pk_fma_f32 v[128:129], v[218:219], v[192:193], v[128:129] neg_lo:[1,0,0] neg_hi:[1,0,0]\n\t" \
  "v_pk_fma_f32 v[144:145], v[218:219], v[184:185], v[144:145] neg_lo:[1,0,1] neg_hi:[1,0,1]\n\t" \
  "v_pk_fma_f32 v[64:65], v[222:223], v[208:209], v[64:65]\n\t" \
  "v_pk_fma_f32 v[80:81], v[220:221], v[208:209], v[80:81] neg_lo:[1,0,0] neg_hi:[1,0,0]\n\t" \
  "v_pk_fma_f32 v[200:201], v[80:81], s[72:73], v[128:129] op_sel_hi:[1,0,1] neg_lo:[1,0,0] neg_hi:[1,0,0]\n\t" \
  "v_pk_fma_f32 v[208:209], v[64:65], s[72:73], v[144:145] op_sel_hi:[1,0,1] neg_lo:[1,0,1] neg_hi:[1,0,1]\n\t" \
  "v_cvt_pk_bf16_f32 v172, v200, v201\n\t" \
  "v_cvt_pk_bf16_f32 v180, v208, v209\n\t" \
  "v_pk_fma_f32 v[216:217], v[146:147], s[72:73], v[66:67] op_sel_hi:[1,0,1]\n\t" \
  "v_pk_fma_f32 v[218:219], v[130:131], s[72:73], v[82:83] op_sel_hi:[1,0,1]\n\t" \
  "v_pk_fma_f32 v[220:221], v[82:83], s[72:73], v[130:131] op_sel_hi:[1,0,1] neg_lo:[1,0,0] neg_hi:[1,0,0]\n\t" \
  "v_pk_fma_f32 v[222:223], v[66:67], s[72:73], v[146:147] op_sel_hi:[1,0,1] neg_lo:[1,0,0] neg_hi:[1,0,0]\n\t" \
  "v_pk_mul_f32 v[130:131], v[216:217], v[186:187]\n\t" \
  "v_pk_mul_f32 v[146:147], v[216:217], v[194:195]\n\t" \
  "v_pk_mul_f32 v[66:67], v[220:221], v[202:203]\n\t" \
  "v_pk_mul_f32 v[82:83], v[222:223], v[202:203]\n\t" \
  "v_pk_fma_f32 v[130:131], v[218:219], v[194:195], v[130:131] neg_lo:[1,0,0] neg_hi:[1,0,0]\n\t" \
  "v_pk_fma_f32 v[146:147], v[218:219], v[186:187], v[146:147] neg_lo:[1,0,1] neg_hi:[1,0,1]\n\t" \
  "v_pk_fma_f32 v[66:67], v[222:223], v[210:211], v[66:67]\n\t" \
  "v_pk_fma_f32 v[82:83], v[220:221], v[210:211], v[82:83] neg_lo:[1,0,0] neg_hi:[1,0,0]\n\t" \
  "v_pk_fma_f32 v[202:203], v[82:83], s[72:73], v[130:131] op_sel_hi:[1,0,1] neg_lo:[1,0,0] neg_hi:[1,0,0]\n\t" \
  "v_pk_fma_f32 v[210:211], v[66:67], s[72:73], v[146:147] op_sel_hi:[1,0,1] neg_lo:[1,0,1] neg_hi:[1,0,1]\n\t" \
  "v_cvt_pk_bf16_f32 v173, v202, v203\n\t" \
  "v_cvt_pk_bf16_f32 v181, v210, v211\n\t" \
  "v_pk_fma_f32 v[216:217], v[148:149], s[72:73], v[68:69] op_sel_hi:[1,0,1]\n\t" \
  "v_pk_fma_f32 v[218:219], v[132:133], s[72:73], v[84:85] op_sel_hi:[1,0,1]\n\t" \
  "v_pk_fma_f32 v[220:221], v[84:85], s[72:73], v[132:133] op_sel_hi:[1,0,1] neg_lo:[1,0,0] neg_hi:[1,0,0]\n\t" \
  "v_pk_fma_f32 v[222:223], v[68:69], s[72:73], v[148:149] op_sel_hi:[1,0,1] neg_lo:[1,0,0] neg_hi:[1,0,0]\n\t" \
  "s_waitcnt lgkmcnt(11)\n\t" \
  "v_pk_mul_f32 v[132:133], v[216:217], v[188:189]\n\t" \
  "s_waitcnt lgkmcnt(10)\n\t" \
  "v_pk_mul_f32 v[148:149], v[216:217], v[196:197]\n\t" \
  "s_waitcnt lgkmcnt(9)\n\t" \
  "v_pk_mul_f32 v[68:69], v[220:221], v[204:205]\n\t" \
  "v_pk_mul_f32 v[84:85], v[222:223], v[204:205]\n\t" \
  "v_pk_fma_f32 v[132:133], v[218:219], v[196:197], v[132:133] neg_lo:[1,0,0] neg_hi:[1,0,0]\n\t" \
  "v_pk_fma_f32 v[148:149], v[218:219], v[188:189], v[148:149] neg_lo:[1,0,1] neg_hi:[1,0,1]\n\t" \
  "s_waitcnt lgkmcnt(8)\n\t" \
  "v_pk_fma_f32 v[68:69], v[222:223], v[212:213], v[68:69]\n\t" \
  "v_pk_fma_f32 v[84:85], v[220:221], v[212:213], v[84:85] neg_lo:[1,0,0] neg_hi:[1,0,0]\n\t" \
  "v_pk_fma_f32 v[204:205], v[84:85], s[72:73], v[132:133] op_sel_hi:[1,0,1] neg_lo:[1,0,0] neg_hi:[1,0,0]\n\t" \
  "v_pk_fma_f32 v[212:213], v[68:69], s[72:73], v[148:149] op_sel_hi:[1,0,1] neg_lo:[1,0,1] neg_hi:[1,0,1]\n\t" \
  "v_cvt_pk_bf16_f32 v174, v204, v205\n\t" \
  "v_cvt_pk_bf16_f32 v182, v212, v213\n\t" \
  "v_pk_fma_f32 v[216:217], v[150:151], s[72:73], v[70:71] op_sel_hi:[1,0,1]\n\t" \
  "v_pk_fma_f32 v[218:219], v[134:135], s[72:73], v[86:87] op_sel_hi:[1,0,1]\n\t" \
  "v_pk_fma_f32 v[220:221], v[86:87], s[72:73], v[134:135] op_sel_hi:[1,0,1] neg_lo:[1,0,0] neg_hi:[1,0,0]\n\t" \
  "v_pk_fma_f32 v[222:223], v[70:71], s[72:73], v[150:151] op_sel_hi:[1,0,1] neg_lo:[1,0,0] neg_hi:[1,0,0]\n\t" \
  "s_waitcnt lgkmcnt(3)\n\t" \
  "v_pk_mul_f32 v[134:135], v[216:217], v[190:191]\n\t" \
  "s_waitcnt lgkmcnt(2)\n\t" \
  "v_pk_mul_f32 v[150:151], v[216:217], v[198:199]\n\t" \
  "s_waitcnt lgkmcnt(1)\n\t" \
  "v_pk_mul_f32 v[70:71], v[220:221], v[206:207]\n\t" \
  "v_pk_mul_f32 v[86:87], v[222:223], v[206:207]\n\t" \
  "v_pk_fma_f32 v[134:135], v[218:219], v[198:199], v[134:135] neg_lo:[1,0,0] neg_hi:[1,0,0]\n\t" \
  "v_pk_fma_f32 v[150:151], v[218:219], v[190:191], v[150:151] neg_lo:[1,0,1] neg_hi:[1,0,1]\n\t" \
  "s_waitcnt lgkmcnt(0)\n\t" \
  "v_pk_fma_f32 v[70:71], v[222:223], v[214:215], v[70:71]\n\t" \
  "v_pk_fma_f32 v[86:87], v[220:221], v[214:215], v[86:87] neg_lo:[1,0,0] neg_hi:[1,0,0]\n\t" \
  "v_pk_fma_f32 v[206:207], v[86:87], s[72:73], v[134:135] op_sel_hi:[1,0,1] neg_lo:[1,0,0] neg_hi:[1,0,0]\n\t" \
  "v_pk_fma_f32 v[214:215], v[70:71], s[72:73], v[150:151] op_sel_hi:[1,0,1] neg_lo:[1,0,1] neg_hi:[1,0,1]\n\t" \
  "v_cvt_pk_bf16_f32 v175, v206, v207\n\t" \
  "v_cvt_pk_bf16_f32 v183, v214, v215\n\t" \
  "v_mfma_f32_32x32x16_bf16 v[88:103], v[152:155], v[0:3], 0\n\t" \
  "v_mfma_f32_32x32x16_bf16 v[104:119], v[160:163], v[0:3], 0\n\t" \
  "v_mfma_f32_32x32x16_bf16 v[88:103], v[160:163], v[16:19], v[88:103]\n\t" \
  "v_mfma_f32_32x32x16_bf16 v[104:119], v[152:155], v[8:11], v[104:119]\n\t" \
  "s_nop 10\n\t" \
  "s_branch 8b\n\t" \
  "9:\n\t"
#define FFT_P3 \
  ".p2align 3\n\t" \
  "s_nop 4\n\t" \
  "s_mov_b32 s52, 0\n\t" \
  "s_add_u32 m0, s51, 0\n\t" \
  "s_nop 0\n\t" \
  "buffer_load_dwordx4 v136, s[44:47], s52 offen offset:0 sc1 lds\n\t" \
  "buffer_load_dwordx4 v137, s[44:47], s52 offen offset:1024 sc1 lds\n\t" \
  "buffer_load_dwordx4 v138, s[44:47], s52 offen offset:2048 sc1 lds\n\t" \
  "buffer_load_dwordx4 v139, s[44:47], s52 offen offset:3072 sc1 lds\n\t" \
  "s_mov_b32 s52, 65536\n\t" \
  "s_add_u32 m0, s51, 4096\n\t" \
  "s_nop 0\n\t" \
  "buffer_load_dwordx4 v136, s[44:47], s52 offen offset:0 sc1 lds\n\t" \
  "buffer_load_dwordx4 v137, s[44:47], s52 offen offset:1024 sc1 lds\n\t" \
  "buffer_load_dwordx4 v138, s[44:47], s52 offen offset:2048 sc1 lds\n\t" \
  "buffer_load_dwordx4 v139, s[44:47], s52 offen offset:3072 sc1 lds\n\t" \
  "s_mov_b32 s52, 0\n\t" \
  "s_add_u32 m0, s51, 8192\n\t" \
  "s_nop 0\n\t" \
  "s_nop 0\n\t" \
  "buffer_load_dwordx4 v140, s[44:47], s52 offen offset:0 sc1 lds\n\t" \
  "buffer_load_dwordx4 v141, s[44:47], s52 offen offset:1024 sc1 lds\n\t" \
  "buffer_load_dwordx4 v142, s[44:47], s52 offen offset:2048 sc1 lds\n\t" \
  "buffer_load_dwordx4 v143, s[44:47], s52 offen offset:3072 sc1 lds\n\t" \
  "s_mov_b32 s52, 65536\n\t" \
  "s_add_u32 m0, s51, 12288\n\t" \
  "s_nop 0\n\t" \
  "s_nop 0\n\t" \
  "buffer_load_dwordx4 v140, s[44:47], s52 offen offset:0 sc1 lds\n\t" \
  "buffer_load_dwordx4 v141, s[44:47], s52 offen offset:1024 sc1 lds\n\t" \
  "buffer_load_dwordx4 v142, s[44:47], s52 offen offset:2048 sc1 lds\n\t" \
  "buffer_load_dwordx4 v143, s[44:47], s52 offen offset:3072 sc1 lds\n\t" \
  "v_fmamk_f32 v200, v224, 0x00000000, v225\n\t" \
  "v_fmamk_f32 v201, v224, 0x00000000, v150\n\t" \
  "v_fmamk_f32 v202, v224, 0x40000000, v225\n\t" \
  "v_fmamk_f32 v203, v224, 0x40000000, v150\n\t" \
  "v_cos_f32 v88, v200\n\t" \
  "v_sin_f32 v92, v200\n\t" \
  "v_cos_f32 v89, v201\n\t" \
  "v_sin_f32 v93, v201\n\t" \
  "v_cos_f32 v90, v202\n\t" \
  "v_sin_f32 v94, v202\n\t" \
  "v_cos_f32 v91, v203\n\t" \
  "v_sin_f32 v95, v203\n\t" \
  "s_waitcnt vmcnt(8)\n\t" \
  "v_xor_b32 v216, 0, v227\n\t" \
  "v_xor_b32_e64 v217, 0, v228\n\t" \
  "v_xor_b32 v218, 128, v227\n\t" \
  "v_xor_b32 v219, 128, v228\n\t" \
  "ds_read_b64_tr_b16 v[24:25], v216 offset:0\n\t" \
  "ds_read_b64_tr_b16 v[26:27], v217 offset:2048\n\t" \
  "ds_read_b64_tr_b16 v[32:33], v218 offset:0\n\t" \
  "ds_read_b64_tr_b16 v[34:35], v219 offset:2048\n\t" \
  "ds_read_b64_tr_b16 v[28:29], v216 offset:4096\n\t" \
  "ds_read_b64_tr_b16 v[30:31], v217 offset:6144\n\t" \
  "ds_read_b64_tr_b16 v[36:37], v218 offset:4096\n\t" \
  "ds_read_b64_tr_b16 v[38:39], v219 offset:6144\n\t" \
  "v_xor_b32 v216, 64, v227\n\t" \
  "v_xor_b32 v217, 64, v228\n\t" \
  "v_xor_b32 v218, 192, v227\n\t" \
  "v_xor_b32 v219, 192, v228\n\t" \
  "ds_read_b64_tr_b16 v[40:41], v216 offset:0\n\t" \
  "ds_read_b64_tr_b16 v[42:43], v217 offset:2048\n\t" \
  "ds_read_b64_tr_b16 v[48:49], v218 offset:0\n\t" \
  "ds_read_b64_tr_b16 v[50:51], v219 offset:2048\n\t" \
  "ds_read_b64_tr_b16 v[44:45], v216 offset:4096\n\t" \
  "ds_read_b64_tr_b16 v[46:47], v217 offset:6144\n\t" \
  "ds_read_b64_tr_b16 v[52:53], v218 offset:4096\n\t" \
  "ds_read_b64_tr_b16 v[54:55], v219 offset:6144\n\t" \
  "s_waitcnt lgkmcnt(14)\n\t" \
  "v_mfma_f32_32x32x16_bf16 v[136:151], v[24:27], v[0:3], 0\n\t" \
  "s_waitcnt lgkmcnt(12)\n\t" \
  "v_mfma_f32_32x32x16_bf16 v[152:167], v[32:35], v[0:3], 0\n\t" \
  "v_mfma_f32_32x32x16_bf16 v[136:151], v[32:35], v[16:19], v[136:151]\n\t" \
  "v_mfma_f32_32x32x16_bf16 v[152:167], v[24:27], v[8:11], v[152:167]\n\t" \
  "s_waitcnt lgkmcnt(10)\n\t" \
  "v_mfma_f32_32x32x16_bf16 v[136:151], v[28:31], v[4:7], v[136:151]\n\t" \
  "s_waitcnt lgkmcnt(8)\n\t" \
  "v_mfma_f32_32x32x16_bf16 v[152:167], v[36:39], v[4:7], v[152:167]\n\t" \
  "v_mfma_f32_32x32x16_bf16 v[136:151], v[36:39], v[20:23], v[136:151]\n\t" \
  "v_mfma_f32_32x32x16_bf16 v[152:167], v[28:31], v[12:15], v[152:167]\n\t" \
  "s_waitcnt lgkmcnt(6)\n\t" \
  "v_mfma_f32_32x32x16_bf16 v[168:183], v[40:43], v[0:3], 0\n\t" \
  "s_waitcnt lgkmcnt(4)\n\t" \
  "v_mfma_f32_32x32x16_bf16 v[184:199], v[48:51], v[0:3], 0\n\t" \
  "s_nop 6\n\t" \
  "v_mul_f32 v104, v136, v88\n\t" \
  "v_mul_f32 v108, v152, v88\n\t" \
  "v_mul_f32 v105, v137, v88\n\t" \
  "v_mul_f32_e64 v109, v153, v88\n\t" \
  "v_fma_f32 v104, -v152, v92, v104\n\t" \
  "v_fmac_f32_e64 v108, v136, v92\n\t" \
  "v_fma_f32 v105, -v153, v92, v105\n\t" \
  "v_fmac_f32 v109, v137, v92\n\t" \
  "v_mul_f32 v106, v138, v88\n\t" \
  "v_mul_f32 v110, v154, v88\n\t" \
  "v_mul_f32 v107, v139, v88\n\t" \
  "v_mul_f32_e64 v111, v155, v88\n\t" \
  "v_fma_f32 v106, -v154, v92, v106\n\t" \
  "v_mfma_f32_32x32x16_bf16 v[168:183], v[48:51], v[16:19], v[168:183]\n\t" \
  "v_fmac_f32_e64 v110, v138, v92\n\t" \
  "v_fma_f32 v107, -v155, v92, v107\n\t" \
  "v_fmac_f32_e64 v111, v139, v92\n\t" \
  "v_cvt_pk_bf16_f32 v200, v104, v105\n\t" \
  "v_cvt_pk_bf16_f32 v201, v106, v107\n\t" \
  "v_xor_b32_e64 v202, 0, v229\n\t" \
  "ds_write_b64 v202, v[200:201] offset:0\n\t" \
  "v_cvt_pk_bf16_f32 v204, v108, v109\n\t" \
  "v_cvt_pk_bf16_f32 v205, v110, v111\n\t" \
  "v_xor_b32 v206, 128, v229\n\t" \
  "ds_write_b64 v206, v[204:205] offset:0\n\t" \
  "v_mul_f32 v112, v140, v88\n\t" \
  "v_mul_f32 v116, v156, v88\n\t" \
  "v_mul_f32_e64 v113, v141, v88\n\t" \
  "v_mfma_f32_32x32x16_bf16 v[184:199], v[40:43], v[8:11], v[184:199]\n\t" \
  "v_mul_f32_e64 v117, v157, v88\n\t" \
  "v_fma_f32 v112, -v156, v92, v112\n\t" \
  "v_fmac_f32_e64 v116, v140, v92\n\t" \
  "v_fma_f32 v113, -v157, v92, v113\n\t" \
  "v_fmac_f32 v117, v141, v92\n\t" \
  "v_mul_f32 v114, v142, v88\n\t" \
  "v_mul_f32 v118, v158, v88\n\t" \
  "v_mul_f32 v115, v143, v88\n\t" \
  "v_mul_f32_e64 v119, v159, v88\n\t" \
  "v_fma_f32 v114, -v158, v92, v114\n\t" \
  "v_fmac_f32_e64 v118, v142, v92\n\t" \
  "v_fma_f32 v115, -v159, v92, v115\n\t" \
  "v_fmac_f32_e64 v119, v143, v92\n\t" \
  "v_cvt_pk_bf16_f32 v208, v112, v113\n\t" \
  "s_waitcnt lgkmcnt(4)\n\t" \
  "v_mfma_f32_32x32x16_bf16 v[168:183], v[44:47], v[4:7], v[168:183]\n\t" \
  "v_cvt_pk_bf16_f32 v209, v114, v115\n\t" \
  "v_xor_b32 v210, 16, v229\n\t" \
  "ds_write_b64 v210, v[208:209] offset:0\n\t" \
  "v_cvt_pk_bf16_f32 v212, v116, v117\n\t" \
  "v_cvt_pk_bf16_f32 v213, v118, v119\n\t" \
  "v_xor_b32 v214, 144, v229\n\t" \
  "ds_write_b64 v214, v[212:213] offset:0\n\t" \
  "v_mul_f32 v104, v144, v88\n\t" \
  "v_mul_f32 v108, v160, v88\n\t" \
  "v_mul_f32 v105, v145, v88\n\t" \
  "v_mul_f32 v109, v161, v88\n\t" \
  "v_fma_f32 v104, -v160, v92, v104\n\t" \
  "v_fmac_f32 v108, v144, v92\n\t" \
  "s_waitcnt lgkmcnt(4)\n\t" \
  "v_mfma_f32_32x32x16_bf16 v[184:199], v[52:55], v[4:7], v[184:199]\n\t" \
  "v_fma_f32 v105, -v161, v92, v105\n\t" \
  "v_fmac_f32 v109, v145, v92\n\t" \
  "v_mul_f32 v106, v146, v88\n\t" \
  "v_mul_f32 v110, v162, v88\n\t" \
  "v_mul_f32 v107, v147, v88\n\t" \
  "v_mul_f32_e64 v111, v163, v88\n\t" \
  "v_fma_f32 v106, -v162, v92, v106\n\t" \
  "v_fmac_f32_e64 v110, v146, v92\n\t" \
  "v_fma_f32 v107, -v163, v92, v107\n\t" \
  "v_fmac_f32_e64 v111, v147, v92\n\t" \
  "v_cvt_pk_bf16_f32 v200, v104, v105\n\t" \
  "v_cvt_pk_bf16_f32 v201, v106, v107\n\t" \
  "v_xor_b32_e64 v202, 32, v229\n\t" \
  "ds_write_b64 v202, v[200:201] offset:0\n\t" \
  "v_mfma_f32_32x32x16_bf16 v[168:183], v[52:55], v[20:23], v[168:183]\n\t" \
  "v_cvt_pk_bf16_f32 v204, v108, v109\n\t" \
  "v_cvt_pk_bf16_f32 v205, v110, v111\n\t" \
  "v_xor_b32 v206, 160, v229\n\t" \
  "ds_write_b64 v206, v[204:205] offset:0\n\t" \
  "v_mul_f32 v112, v148, v88\n\t" \
  "v_mul_f32 v116, v164, v88\n\t" \
  "v_mul_f32 v113, v149, v88\n\t" \
  "v_mul_f32 v117, v165, v88\n\t" \
  "v_fma_f32 v112, -v164, v92, v112\n\t" \
  "v_fmac_f32_e64 v116, v148, v92\n\t" \
  "v_fma_f32 v113, -v165, v92, v113\n\t" \
  "v_fmac_f32 v117, v149, v92\n\t" \
  "v_mul_f32 v114, v150, v88\n\t" \
  "v_mul_f32_e64 v118, v166, v88\n\t" \
  "v_mfma_f32_32x32x16_bf16 v[184:199], v[44:47], v[12:15], v[184:199]\n\t" \
  "v_mul_f32 v115, v151, v88\n\t" \
  "v_mul_f32 v119, v167, v88\n\t" \
  "v_fma_f32 v114, -v166, v92, v114\n\t" \
  "v_fmac_f32_e64 v118, v150, v92\n\t" \
  "v_fma_f32 v115, -v167, v92, v115\n\t" \
  "v_fmac_f32_e64 v119, v151, v92\n\t" \
  "v_cvt_pk_bf16_f32 v208, v112, v113\n\t" \
  "v_cvt_pk_bf16_f32 v209, v114, v115\n\t" \
  "v_xor_b32_e64 v210, 48, v229\n\t" \
  "ds_write_b64 v210, v[208:209] offset:0\n\t" \
  "v_cvt_pk_bf16_f32 v212, v116, v117\n\t" \
  "v_cvt_pk_bf16_f32 v213, v118, v119\n\t" \
  "v_xor_b32 v214, 176, v229\n\t" \
  "ds_write_b64 v214, v[212:213] offset:0\n\t" \
  "v_mul_f32 v104, v168, v90\n\t" \
  "v_mul_f32 v108, v184, v90\n\t" \
  "v_mul_f32 v105, v169, v90\n\t" \
  "v_mul_f32 v109, v185, v90\n\t" \
  "v_fma_f32 v104, -v184, v94, v104\n\t" \
  "v_fmac_f32_e64 v108, v168, v94\n\t" \
  "v_fma_f32 v105, -v185, v94, v105\n\t" \
  "v_fmac_f32 v109, v169, v94\n\t" \
  "v_mul_f32 v106, v170, v90\n\t" \
  "v_mul_f32 v110, v186, v90\n\t" \
  "v_mul_f32 v107, v171, v90\n\t" \
  "v_mul_f32_e64 v111, v187, v90\n\t" \
  "v_fma_f32 v106, -v186, v94, v106\n\t" \
  "v_fmac_f32_e64 v110, v170, v94\n\t" \
  "v_fma_f32 v107, -v187, v94, v107\n\t" \
  "v_fmac_f32_e64 v111, v171, v94\n\t" \
  "v_cvt_pk_bf16_f32 v200, v104, v105\n\t" \
  "v_cvt_pk_bf16_f32 v201, v106, v107\n\t" \
  "v_xor_b32_e64 v202, 64, v229\n\t" \
  "ds_write_b64 v202, v[200:201] offset:0\n\t" \
  "v_cvt_pk_bf16_f32 v204, v108, v109\n\t" \
  "v_cvt_pk_bf16_f32 v205, v110, v111\n\t" \
  "v_xor_b32 v206, 192, v229\n\t" \
  "ds_write_b64 v206, v[204:205] offset:0\n\t" \
  "s_waitcnt vmcnt(0)\n\t" \
  "v_xor_b32 v216, 128, v227\n\t" \
  "v_xor_b32 v217, 128, v228\n\t" \
  "v_xor_b32 v218, 0, v227\n\t" \
  "v_xor_b32_e64 v219, 0, v228\n\t" \
  "ds_read_b64_tr_b16 v[56:57], v216 offset:8192\n\t" \
  "ds_read_b64_tr_b16 v[58:59], v217 offset:10240\n\t" \
  "ds_read_b64_tr_b16 v[64:65], v218 offset:8192\n\t" \
  "ds_read_b64_tr_b16 v[66:67], v219 offset:10240\n\t" \
  "ds_read_b64_tr_b16 v[60:61], v216 offset:12288\n\t" \
  "ds_read_b64_tr_b16 v[62:63], v217 offset:14336\n\t" \
  "ds_read_b64_tr_b16 v[68:69], v218 offset:12288\n\t" \
  "ds_read_b64_tr_b16 v[70:71], v219 offset:14336\n\t" \
  "v_xor_b32 v216, 192, v227\n\t" \
  "v_xor_b32 v217, 192, v228\n\t" \
  "v_xor_b32 v218, 64, v227\n\t" \
  "v_xor_b32 v219, 64, v228\n\t" \
  "ds_read_b64_tr_b16 v[72:73], v216 offset:8192\n\t" \
  "ds_read_b64_tr_b16 v[74:75], v217 offset:10240\n\t" \
  "ds_read_b64_tr_b16 v[80:81], v218 offset:8192\n\t" \
  "ds_read_b64_tr_b16 v[82:83], v219 offset:10240\n\t" \
  "ds_read_b64_tr_b16 v[76:77], v216 offset:12288\n\t" \
  "ds_read_b64_tr_b16 v[78:79], v217 offset:14336\n\t" \
  "ds_read_b64_tr_b16 v[84:85], v218 offset:12288\n\t" \
  "ds_read_b64_tr_b16 v[86:87], v219 offset:14336\n\t" \
  "v_mul_f32 v112, v172, v90\n\t" \
  "v_mul_f32 v116, v188, v90\n\t" \
  "v_mul_f32 v113, v173, v90\n\t" \
  "v_mul_f32 v117, v189, v90\n\t" \
  "v_fma_f32 v112, -v188, v94, v112\n\t" \
  "v_fmac_f32_e64 v116, v172, v94\n\t" \
  "v_fma_f32 v113, -v189, v94, v113\n\t" \
  "v_fmac_f32 v117, v173, v94\n\t" \
  "v_mul_f32 v114, v174, v90\n\t" \
  "v_mul_f32 v118, v190, v90\n\t" \
  "v_mul_f32 v115, v175, v90\n\t" \
  "v_mul_f32_e64 v119, v191, v90\n\t" \
  "v_fma_f32 v114, -v190, v94, v114\n\t" \
  "v_fmac_f32_e64 v118, v174, v94\n\t" \
  "v_fma_f32 v115, -v191, v94, v115\n\t" \
  "v_fmac_f32_e64 v119, v175, v94\n\t" \
  "v_cvt_pk_bf16_f32 v208, v112, v113\n\t" \
  "v_cvt_pk_bf16_f32 v209, v114, v115\n\t" \
  "v_xor_b32 v210, 80, v229\n\t" \
  "ds_write_b64 v210, v[208:209] offset:0\n\t" \
  "v_cvt_pk_bf16_f32 v212, v116, v117\n\t" \
  "v_cvt_pk_bf16_f32 v213, v118, v119\n\t" \
  "v_xor_b32 v214, 208, v229\n\t" \
  "ds_write_b64 v214, v[212:213] offset:0\n\t" \
  "s_waitcnt lgkmcnt(14)\n\t" \
  "v_mfma_f32_32x32x16_bf16 v[136:151], v[56:59], v[0:3], 0\n\t" \
  "v_mul_f32 v104, v176, v90\n\t" \
  "v_mul_f32 v108, v192, v90\n\t" \
  "v_mul_f32 v105, v177, v90\n\t" \
  "v_mul_f32_e64 v109, v193, v90\n\t" \
  "v_fma_f32 v104, -v192, v94, v104\n\t" \
  "v_fmac_f32_e64 v108, v176, v94\n\t" \
  "v_mfma_f32_32x32x16_bf16 v[152:167], v[64:67], v[0:3], 0\n\t" \
  "v_fma_f32 v105, -v193, v94, v105\n\t" \
  "v_fmac_f32 v109, v177, v94\n\t" \
  "v_mul_f32 v106, v178, v90\n\t" \
  "v_mul_f32 v110, v194, v90\n\t" \
  "v_mul_f32 v107, v179, v90\n\t" \
  "v_mul_f32_e64 v111, v195, v90\n\t" \
  "v_mfma_f32_32x32x16_bf16 v[136:151], v[64:67], v[16:19], v[136:151]\n\t" \
  "v_fma_f32 v106, -v194, v94, v106\n\t" \
  "v_fmac_f32_e64 v110, v178, v94\n\t" \
  "v_fma_f32 v107, -v195, v94, v107\n\t" \
  "v_fmac_f32_e64 v111, v179, v94\n\t" \
  "v_cvt_pk_bf16_f32 v200, v104, v105\n\t" \
  "v_cvt_pk_bf16_f32 v201, v106, v107\n\t" \
  "v_mfma_f32_32x32x16_bf16 v[152:167], v[56:59], v[8:11], v[152:167]\n\t" \
  "v_xor_b32 v202, 96, v229\n\t" \
  "ds_write_b64 v202, v[200:201] offset:0\n\t" \
  "v_cvt_pk_bf16_f32 v204, v108, v109\n\t" \
  "v_cvt_pk_bf16_f32 v205, v110, v111\n\t" \
  "v_xor_b32 v206, 224, v229\n\t" \
  "ds_write_b64 v206, v[204:205] offset:0\n\t" \
  "s_waitcnt lgkmcnt(14)\n\t" \
  "v_mfma_f32_32x32x16_bf16 v[136:151], v[60:63], v[4:7], v[136:151]\n\t" \
  "v_mul_f32 v112, v180, v90\n\t" \
  "v_mul_f32 v116, v196, v90\n\t" \
  "v_mul_f32 v113, v181, v90\n\t" \
  "v_mul_f32_e64 v117, v197, v90\n\t" \
  "v_fma_f32 v112, -v196, v94, v112\n\t" \
  "v_fmac_f32 v116, v180, v94\n\t" \
  "s_waitcnt lgkmcnt(12)\n\t" \
  "v_mfma_f32_32x32x16_bf16 v[152:167], v[68:71], v[4:7], v[152:167]\n\t" \
  "v_fma_f32 v113, -v197, v94, v113\n\t" \
  "v_fmac_f32 v117, v181, v94\n\t" \
  "v_mul_f32 v114, v182, v90\n\t" \
  "v_mul_f32 v118, v198, v90\n\t" \
  "v_mul_f32 v115, v183, v90\n\t" \
  "v_mul_f32_e64 v119, v199, v90\n\t" \
  "v_mfma_f32_32x32x16_bf16 v[136:151], v[68:71], v[20:23], v[136:151]\n\t" \
  "v_fma_f32 v114, -v198, v94, v114\n\t" \
  "v_fmac_f32_e64 v118, v182, v94\n\t" \
  "v_fma_f32 v115, -v199, v94, v115\n\t" \
  "v_fmac_f32_e64 v119, v183, v94\n\t" \
  "v_cvt_pk_bf16_f32 v208, v112, v113\n\t" \
  "v_cvt_pk_bf16_f32 v209, v114, v115\n\t" \
  "v_mfma_f32_32x32x16_bf16 v[152:167], v[60:63], v[12:15], v[152:167]\n\t" \
  "v_xor_b32 v210, 112, v229\n\t" \
  "ds_write_b64 v210, v[208:209] offset:0\n\t" \
  "v_cvt_pk_bf16_f32 v212, v116, v117\n\t" \
  "v_cvt_pk_bf16_f32 v213, v118, v119\n\t" \
  "v_xor_b32 v214, 240, v229\n\t" \
  "ds_write_b64 v214, v[212:213] offset:0\n\t" \
  "s_waitcnt lgkmcnt(12)\n\t" \
  "v_mfma_f32_32x32x16_bf16 v[168:183], v[72:75], v[0:3], 0\n\t" \
  "s_waitcnt lgkmcnt(10)\n\t" \
  "v_mfma_f32_32x32x16_bf16 v[184:199], v[80:83], v[0:3], 0\n\t" \
  "v_mul_f32 v104, v136, v89\n\t" \
  "s_nop 0\n\t" \
  "v_mul_f32 v108, v152, v89\n\t" \
  "v_mul_f32 v105, v137, v89\n\t" \
  "v_mul_f32_e64 v109, v153, v89\n\t" \
  "v_fma_f32 v104, -v152, v93, v104\n\t" \
  "v_fmac_f32_e64 v108, v136, v93\n\t" \
  "v_fma_f32 v105, -v153, v93, v105\n\t" \
  "v_fmac_f32 v109, v137, v93\n\t" \
  "v_mul_f32 v106, v138, v89\n\t" \
  "v_mul_f32 v110, v154, v89\n\t" \
  "v_mul_f32 v107, v139, v89\n\t" \
  "v_mul_f32_e64 v111, v155, v89\n\t" \
  "v_fma_f32 v106, -v154, v93, v106\n\t" \
  "v_mfma_f32_32x32x16_bf16 v[168:183], v[80:83], v[16:19], v[168:183]\n\t" \
  "v_fmac_f32_e64 v110, v138, v93\n\t" \
  "v_fma_f32 v107, -v155, v93, v107\n\t" \
  "v_fmac_f32_e64 v111, v139, v93\n\t" \
  "v_cvt_pk_bf16_f32 v200, v104, v105\n\t" \
  "v_cvt_pk_bf16_f32 v201, v106, v107\n\t" \
  "v_xor_b32 v202, 128, v229\n\t" \
  "ds_write_b64 v202, v[200:201] offset:8192\n\t" \
  "v_cvt_pk_bf16_f32 v204, v108, v109\n\t" \
  "v_cvt_pk_bf16_f32 v205, v110, v111\n\t" \
  "v_xor_b32_e64 v206, 0, v229\n\t" \
  "ds_write_b64 v206, v[204:205] offset:8192\n\t" \
  "v_mul_f32 v112, v140, v89\n\t" \
  "v_mul_f32 v116, v156, v89\n\t" \
  "v_mul_f32_e64 v113, v141, v89\n\t" \
  "v_mfma_f32_32x32x16_bf16 v[184:199], v[72:75], v[8:11], v[184:199]\n\t" \
  "v_mul_f32_e64 v117, v157, v89\n\t" \
  "v_fma_f32 v112, -v156, v93, v112\n\t" \
  "v_fmac_f32_e64 v116, v140, v93\n\t" \
  "v_fma_f32 v113, -v157, v93, v113\n\t" \
  "v_fmac_f32 v117, v141, v93\n\t" \
  "v_mul_f32 v114, v142, v89\n\t" \
  "v_mul_f32 v118, v158, v89\n\t" \
  "v_mul_f32 v115, v143, v89\n\t" \
  "v_mul_f32_e64 v119, v159, v89\n\t" \
  "v_fma_f32 v114, -v158, v93, v114\n\t" \
  "v_fmac_f32_e64 v118, v142, v93\n\t" \
  "v_fma_f32 v115, -v159, v93, v115\n\t" \
  "v_fmac_f32_e64 v119, v143, v93\n\t" \
  "v_cvt_pk_bf16_f32 v208, v112, v113\n\t" \
  "s_waitcnt lgkmcnt(10)\n\t" \
  "s_nop 0\n\t" \
  "v_mfma_f32_32x32x16_bf16 v[168:183], v[76:79], v[4:7], v[168:183]\n\t" \
  "v_cvt_pk_bf16_f32 v209, v114, v115\n\t" \
  "v_xor_b32 v210, 144, v229\n\t" \
  "ds_write_b64 v210, v[208:209] offset:8192\n\t" \
  "v_cvt_pk_bf16_f32 v212, v116, v117\n\t" \
  "v_cvt_pk_bf16_f32 v213, v118, v119\n\t" \
  "v_xor_b32_e64 v214, 16, v229\n\t" \
  "ds_write_b64 v214, v[212:213] offset:8192\n\t" \
  "v_mul_f32 v104, v144, v89\n\t" \
  "v_mul_f32 v108, v160, v89\n\t" \
  "v_mul_f32 v105, v145, v89\n\t" \
  "v_mul_f32 v109, v161, v89\n\t" \
  "v_fma_f32 v104, -v160, v93, v104\n\t" \
  "v_fmac_f32 v108, v144, v93\n\t" \
  "s_waitcnt lgkmcnt(10)\n\t" \
  "v_mfma_f32_32x32x16_bf16 v[184:199], v[84:87], v[4:7], v[184:199]\n\t" \
  "v_fma_f32 v105, -v161, v93, v105\n\t" \
  "v_fmac_f32 v109, v145, v93\n\t" \
  "v_mul_f32 v106, v146, v89\n\t" \
  "v_mul_f32 v110, v162, v89\n\t" \
  "v_mul_f32 v107, v147, v89\n\t" \
  "v_mul_f32_e64 v111, v163, v89\n\t" \
  "v_fma_f32 v106, -v162, v93, v106\n\t" \
  "v_fmac_f32_e64 v110, v146, v93\n\t" \
  "v_fma_f32 v107, -v163, v93, v107\n\t" \
  "v_fmac_f32_e64 v111, v147, v93\n\t" \
  "v_cvt_pk_bf16_f32 v200, v104, v105\n\t" \
  "v_cvt_pk_bf16_f32 v201, v106, v107\n\t" \
  "v_xor_b32 v202, 160, v229\n\t" \
  "ds_write_b64 v202, v[200:201] offset:8192\n\t" \
  "v_mfma_f32_32x32x16_bf16 v[168:183], v[84:87], v[20:23], v[168:183]\n\t" \
  "v_cvt_pk_bf16_f32 v204, v108, v109\n\t" \
  "v_cvt_pk_bf16_f32 v205, v110, v111\n\t" \
  "v_xor_b32_e64 v206, 32, v229\n\t" \
  "ds_write_b64 v206, v[204:205] offset:8192\n\t" \
  "v_mul_f32 v112, v148, v89\n\t" \
  "v_mul_f32 v116, v164, v89\n\t" \
  "v_mul_f32 v113, v149, v89\n\t" \
  "v_mul_f32 v117, v165, v89\n\t" \
  "v_fma_f32 v112, -v164, v93, v112\n\t" \
  "v_fmac_f32_e64 v116, v148, v93\n\t" \
  "v_fma_f32 v113, -v165, v93, v113\n\t" \
  "v_fmac_f32 v117, v149, v93\n\t" \
  "v_mul_f32 v114, v150, v89\n\t" \
  "v_mul_f32_e64 v118, v166, v89\n\t" \
  "v_mfma_f32_32x32x16_bf16 v[184:199], v[76:79], v[12:15], v[184:199]\n\t" \
  "v_mul_f32 v115, v151, v89\n\t" \
  "v_mul_f32 v119, v167, v89\n\t" \
  "v_fma_f32 v114, -v166, v93, v114\n\t" \
  "v_fmac_f32_e64 v118, v150, v93\n\t" \
  "v_fma_f32 v115, -v167, v93, v115\n\t" \
  "v_fmac_f32_e64 v119, v151, v93\n\t" \
  "v_cvt_pk_bf16_f32 v208, v112, v113\n\t" \
  "v_cvt_pk_bf16_f32 v209, v114, v115\n\t" \
  "v_xor_b32 v210, 176, v229\n\t" \
  "ds_write_b64 v210, v[208:209] offset:8192\n\t" \
  "v_cvt_pk_bf16_f32 v212, v116, v117\n\t" \
  "v_cvt_pk_bf16_f32 v213, v118, v119\n\t" \
  "v_xor_b32_e64 v214, 48, v229\n\t" \
  "ds_write_b64 v214, v[212:213] offset:8192\n\t" \
  "v_xor_b32_e64 v216, 0, v230\n\t" \
  "v_add_u32 v220, 81920, v216\n\t" \
  "v_xor_b32_e64 v217, 64, v230\n\t" \
  "v_add_u32 v221, 81920, v217\n\t" \
  "v_xor_b32 v218, 128, v230\n\t" \
  "v_add_u32 v222, 81920, v218\n\t" \
  "v_xor_b32 v219, 192, v230\n\t" \
  "v_add_u32 v223, 81920, v219\n\t" \
  "v_mul_f32 v104, v168, v91\n\t" \
  "v_mul_f32 v108, v184, v91\n\t" \
  "v_mul_f32 v105, v169, v91\n\t" \
  "v_mul_f32 v109, v185, v91\n\t" \
  "v_fma_f32 v104, -v184, v95, v104\n\t" \
  "v_fmac_f32_e64 v108, v168, v95\n\t" \
  "v_fma_f32 v105, -v185, v95, v105\n\t" \
  "v_fmac_f32 v109, v169, v95\n\t" \
  "v_mul_f32 v106, v170, v91\n\t" \
  "v_mul_f32 v110, v186, v91\n\t" \
  "v_mul_f32 v107, v171, v91\n\t" \
  "v_mul_f32_e64 v111, v187, v91\n\t" \
  "v_fma_f32 v106, -v186, v95, v106\n\t" \
  "v_fmac_f32_e64 v110, v170, v95\n\t" \
  "v_fma_f32 v107, -v187, v95, v107\n\t" \
  "v_fmac_f32_e64 v111, v171, v95\n\t" \
  "v_cvt_pk_bf16_f32 v200, v104, v105\n\t" \
  "v_cvt_pk_bf16_f32 v201, v106, v107\n\t" \
  "v_xor_b32 v202, 192, v229\n\t" \
  "ds_write_b64 v202, v[200:201] offset:8192\n\t" \
  "v_cvt_pk_bf16_f32 v204, v108, v109\n\t" \
  "v_cvt_pk_bf16_f32 v205, v110, v111\n\t" \
  "v_xor_b32_e64 v206, 64, v229\n\t" \
  "ds_write_b64 v206, v[204:205] offset:8192\n\t" \
  "v_mul_f32 v112, v172, v91\n\t" \
  "v_mul_f32 v116, v188, v91\n\t" \
  "v_mul_f32 v113, v173, v91\n\t" \
  "v_mul_f32 v117, v189, v91\n\t" \
  "v_fma_f32 v112, -v188, v95, v112\n\t" \
  "v_fmac_f32_e64 v116, v172, v95\n\t" \
  "v_fma_f32 v113, -v189, v95, v113\n\t" \
  "v_fmac_f32 v117, v173, v95\n\t" \
  "v_mul_f32 v114, v174, v91\n\t" \
  "v_mul_f32 v118, v190, v91\n\t" \
  "v_mul_f32 v115, v175, v91\n\t" \
  "v_mul_f32_e64 v119, v191, v91\n\t" \
  "v_fma_f32 v114, -v190, v95, v114\n\t" \
  "v_fmac_f32_e64 v118, v174, v95\n\t" \
  "v_fma_f32 v115, -v191, v95, v115\n\t" \
  "v_fmac_f32_e64 v119, v175, v95\n\t" \
  "v_cvt_pk_bf16_f32 v208, v112, v113\n\t" \
  "v_cvt_pk_bf16_f32 v209, v114, v115\n\t" \
  "v_xor_b32 v210, 208, v229\n\t" \
  "ds_write_b64 v210, v[208:209] offset:8192\n\t" \
  "v_cvt_pk_bf16_f32 v212, v116, v117\n\t" \
  "v_cvt_pk_bf16_f32 v213, v118, v119\n\t" \
  "v_xor_b32 v214, 80, v229\n\t" \
  "ds_write_b64 v214, v[212:213] offset:8192\n\t" \
  "v_mul_f32 v104, v176, v91\n\t" \
  "v_mul_f32 v108, v192, v91\n\t" \
  "v_mul_f32 v105, v177, v91\n\t" \
  "v_mul_f32 v109, v193, v91\n\t" \
  "v_fma_f32 v104, -v192, v95, v104\n\t" \
  "v_fmac_f32_e64 v108, v176, v95\n\t" \
  "v_fma_f32 v105, -v193, v95, v105\n\t" \
  "v_fmac_f32 v109, v177, v95\n\t" \
  "v_mul_f32 v106, v178, v91\n\t" \
  "v_mul_f32 v110, v194, v91\n\t" \
  "v_mul_f32 v107, v179, v91\n\t" \
  "v_mul_f32_e64 v111, v195, v91\n\t" \
  "v_fma_f32 v106, -v194, v95, v106\n\t" \
  "v_fmac_f32_e64 v110, v178, v95\n\t" \
  "v_fma_f32 v107, -v195, v95, v107\n\t" \
  "v_fmac_f32_e64 v111, v179, v95\n\t" \
  "v_cvt_pk_bf16_f32 v200, v104, v105\n\t" \
  "v_cvt_pk_bf16_f32 v201, v106, v107\n\t" \
  "v_xor_b32 v202, 224, v229\n\t" \
  "ds_write_b64 v202, v[200:201] offset:8192\n\t" \
  "v_cvt_pk_bf16_f32 v204, v108, v109\n\t" \
  "v_cvt_pk_bf16_f32 v205, v110, v111\n\t" \
  "v_xor_b32 v206, 96, v229\n\t" \
  "ds_write_b64 v206, v[204:205] offset:8192\n\t" \
  "v_mul_f32 v112, v180, v91\n\t" \
  "v_mul_f32 v116, v196, v91\n\t" \
  "v_mul_f32 v113, v181, v91\n\t" \
  "v_mul_f32 v117, v197, v91\n\t" \
  "v_fma_f32 v112, -v196, v95, v112\n\t" \
  "v_fmac_f32_e64 v116, v180, v95\n\t" \
  "v_fma_f32 v113, -v197, v95, v113\n\t" \
  "v_fmac_f32 v117, v181, v95\n\t" \
  "v_mul_f32 v114, v182, v91\n\t" \
  "v_mul_f32 v118, v198, v91\n\t" \
  "v_mul_f32 v115, v183, v91\n\t" \
  "v_mul_f32_e64 v119, v199, v91\n\t" \
  "v_fma_f32 v114, -v198, v95, v114\n\t" \
  "v_fmac_f32_e64 v118, v182, v95\n\t" \
  "v_fma_f32 v115, -v199, v95, v115\n\t" \
  "v_fmac_f32_e64 v119, v183, v95\n\t" \
  "v_cvt_pk_bf16_f32 v208, v112, v113\n\t" \
  "v_cvt_pk_bf16_f32 v209, v114, v115\n\t" \
  "v_xor_b32 v210, 240, v229\n\t" \
  "ds_write_b64 v210, v[208:209] offset:8192\n\t" \
  "v_cvt_pk_bf16_f32 v212, v116, v117\n\t" \
  "v_cvt_pk_bf16_f32 v213, v118, v119\n\t" \
  "v_xor_b32 v214, 112, v229\n\t" \
  "ds_write_b64 v214, v[212:213] offset:8192\n\t" \
  "s_waitcnt lgkmcnt(0)\n\t" \
  "s_barrier\n\t" \
  "ds_read_b64_tr_b16 v[24:25], v216 offset:0\n\t" \
  "ds_read_b64_tr_b16 v[26:27], v216 offset:40960\n\t" \
  "ds_read_b64_tr_b16 v[32:33], v218 offset:0\n\t" \
  "ds_read_b64_tr_b16 v[34:35], v218 offset:40960\n\t" \
  "ds_read_b64_tr_b16 v[28:29], v220 offset:0\n\t" \
  "ds_read_b64_tr_b16 v[30:31], v220 offset:40960\n\t" \
  "ds_read_b64_tr_b16 v[36:37], v222 offset:0\n\t" \
  "ds_read_b64_tr_b16 v[38:39], v222 offset:40960\n\t" \
  "ds_read_b64_tr_b16 v[40:41], v217 offset:256\n\t" \
  "ds_read_b64_tr_b16 v[42:43], v217 offset:41216\n\t" \
  "ds_read_b64_tr_b16 v[48:49], v219 offset:256\n\t" \
  "ds_read_b64_tr_b16 v[50:51], v219 offset:41216\n\t" \
  "ds_read_b64_tr_b16 v[44:45], v221 offset:256\n\t" \
  "ds_read_b64_tr_b16 v[46:47], v221 offset:41216\n\t" \
  "ds_read_b64_tr_b16 v[52:53], v223 offset:256\n\t" \
  "ds_read_b64_tr_b16 v[54:55], v223 offset:41216\n\t" \
  "ds_read_b64_tr_b16 v[56:57], v218 offset:512\n\t" \
  "ds_read_b64_tr_b16 v[58:59], v218 offset:41472\n\t" \
  "ds_read_b64_tr_b16 v[64:65], v216 offset:512\n\t" \
  "ds_read_b64_tr_b16 v[66:67], v216 offset:41472\n\t" \
  "ds_read_b64_tr_b16 v[60:61], v222 offset:512\n\t" \
  "ds_read_b64_tr_b16 v[62:63], v222 offset:41472\n\t" \
  "ds_read_b64_tr_b16 v[68:69], v220 offset:512\n\t" \
  "ds_read_b64_tr_b16 v[70:71], v220 offset:41472\n\t" \
  "ds_read_b64_tr_b16 v[72:73], v219 offset:768\n\t" \
  "ds_read_b64_tr_b16 v[74:75], v219 offset:41728\n\t" \
  "ds_read_b64_tr_b16 v[80:81], v217 offset:768\n\t" \
  "ds_read_b64_tr_b16 v[82:83], v217 offset:41728\n\t" \
  "ds_read_b64_tr_b16 v[76:77], v223 offset:768\n\t" \
  "ds_read_b64_tr_b16 v[78:79], v223 offset:41728\n\t" \
  "ds_read_b64_tr_b16 v[84:85], v221 offset:768\n\t" \
  "ds_read_b64_tr_b16 v[86:87], v221 offset:41728\n\t" \
  "s_waitcnt lgkmcnt(14)\n\t" \
  "s_nop 0\n\t" \
  "v_mfma_f32_32x32x16_bf16 v[136:151], v[24:27], v[0:3], 0\n\t" \
  "v_mfma_f32_32x32x16_bf16 v[152:167], v[32:35], v[0:3], 0\n\t" \
  "v_mfma_f32_32x32x16_bf16 v[136:151], v[32:35], v[16:19], v[136:151]\n\t" \
  "v_mfma_f32_32x32x16_bf16 v[152:167], v[24:27], v[8:11], v[152:167]\n\t" \
  "v_mfma_f32_32x32x16_bf16 v[136:151], v[28:31], v[4:7], v[136:151]\n\t" \
  "v_mfma_f32_32x32x16_bf16 v[152:167], v[36:39], v[4:7], v[152:167]\n\t" \
  "v_mfma_f32_32x32x16_bf16 v[136:151], v[36:39], v[20:23], v[136:151]\n\t" \
  "v_mfma_f32_32x32x16_bf16 v[152:167], v[28:31], v[12:15], v[152:167]\n\t" \
  "v_mfma_f32_32x32x16_bf16 v[168:183], v[40:43], v[0:3], 0\n\t" \
  "v_mfma_f32_32x32x16_bf16 v[184:199], v[48:51], v[0:3], 0\n\t" \
  "s_add_u32 s52, s48, 0\n\t" \
  "s_add_u32 s53, s48, 32768\n\t" \
  "s_add_u32 s54, s48, 65536\n\t" \
  "s_add_u32 s55, s48, 98304\n\t" \
  "s_nop 4\n\t" \
  "v_cvt_pk_bf16_f32 v200, v136, v137\n\t" \
  "v_cvt_pk_bf16_f32 v201, v138, v139\n\t" \
  "v_mfma_f32_32x32x16_bf16 v[168:183], v[48:51], v[16:19], v[168:183]\n\t" \
  "ds_write_b64 v232, v[200:201]\n\t" \
  "v_cvt_pk_bf16_f32 v202, v152, v153\n\t" \
  "v_cvt_pk_bf16_f32 v203, v154, v155\n\t" \
  "ds_write_b64 v236, v[202:203]\n\t" \
  "v_cvt_pk_bf16_f32 v204, v140, v141\n\t" \
  "v_cvt_pk_bf16_f32 v205, v142, v143\n\t" \
  "v_mfma_f32_32x32x16_bf16 v[184:199], v[40:43], v[8:11], v[184:199]\n\t" \
  "ds_write_b64 v233, v[204:205]\n\t" \
  "v_cvt_pk_bf16_f32 v206, v156, v157\n\t" \
  "v_cvt_pk_bf16_f32 v207, v158, v159\n\t" \
  "ds_write_b64 v237, v[206:207]\n\t" \
  "v_cvt_pk_bf16_f32 v208, v144, v145\n\t" \
  "v_cvt_pk_bf16_f32 v209, v146, v147\n\t" \
  "ds_write_b64 v234, v[208:209]\n\t" \
  "v_mfma_f32_32x32x16_bf16 v[168:183], v[44:47], v[4:7], v[168:183]\n\t" \
  "v_cvt_pk_bf16_f32 v210, v160, v161\n\t" \
  "v_cvt_pk_bf16_f32 v211, v162, v163\n\t" \
  "ds_write_b64 v238, v[210:211]\n\t" \
  "v_cvt_pk_bf16_f32 v212, v148, v149\n\t" \
  "v_cvt_pk_bf16_f32 v213, v150, v151\n\t" \
  "ds_write_b64 v235, v[212:213]\n\t" \
  "v_mfma_f32_32x32x16_bf16 v[184:199], v[52:55], v[4:7], v[184:199]\n\t" \
  "v_cvt_pk_bf16_f32 v214, v164, v165\n\t" \
  "v_cvt_pk_bf16_f32 v215, v166, v167\n\t" \
  "ds_write_b64 v239, v[214:215]\n\t" \
  "ds_read_b128 v[88:91], v240 offset:0\n\t" \
  "ds_read_b128 v[92:95], v240 offset:1024\n\t" \
  "ds_read_b128 v[96:99], v240 offset:2048\n\t" \
  "ds_read_b128 v[100:103], v240 offset:3072\n\t" \
  "v_mfma_f32_32x32x16_bf16 v[168:183], v[52:55], v[20:23], v[168:183]\n\t" \
  "v_mfma_f32_32x32x16_bf16 v[184:199], v[44:47], v[12:15], v[184:199]\n\t" \
  "s_waitcnt lgkmcnt(3)\n\t" \
  "buffer_store_dwordx4 v[88:91], v241, s[40:43], s52 offen sc1\n\t" \
  "s_waitcnt lgkmcnt(2)\n\t" \
  "buffer_store_dwordx4 v[92:95], v241, s[40:43], s53 offen sc1\n\t" \
  "s_waitcnt lgkmcnt(1)\n\t" \
  "buffer_store_dwordx4 v[96:99], v241, s[40:43], s54 offen sc1\n\t" \
  "s_waitcnt lgkmcnt(0)\n\t" \
  "buffer_store_dwordx4 v[100:103], v241, s[40:43], s55 offen sc1\n\t" \
  "v_mfma_f32_32x32x16_bf16 v[136:151], v[56:59], v[0:3], 0\n\t" \
  "v_mfma_f32_32x32x16_bf16 v[152:167], v[64:67], v[0:3], 0\n\t" \
  "s_add_u32 s52, s48, 128\n\t" \
  "s_add_u32 s53, s48, 32896\n\t" \
  "s_add_u32 s54, s48, 65664\n\t" \
  "s_add_u32 s55, s48, 98432\n\t" \
  "v_cvt_pk_bf16_f32 v200, v168, v169\n\t" \
  "v_cvt_pk_bf16_f32 v201, v170, v171\n\t" \
  "v_mfma_f32_32x32x16_bf16 v[136:151], v[64:67], v[16:19], v[136:151]\n\t" \
  "ds_write_b64 v232, v[200:201]\n\t" \
  "v_cvt_pk_bf16_f32 v202, v184, v185\n\t" \
  "v_cvt_pk_bf16_f32 v203, v186, v187\n\t" \
  "ds_write_b64 v236, v[202:203]\n\t" \
  "v_cvt_pk_bf16_f32 v204, v172, v173\n\t" \
  "v_cvt_pk_bf16_f32 v205, v174, v175\n\t" \
  "v_mfma_f32_32x32x16_bf16 v[152:167], v[56:59], v[8:11], v[152:167]\n\t" \
  "ds_write_b64 v233, v[204:205]\n\t" \
  "v_cvt_pk_bf16_f32 v206, v188, v189\n\t" \
  "v_cvt_pk_bf16_f32 v207, v190, v191\n\t" \
  "ds_write_b64 v237, v[206:207]\n\t" \
  "v_cvt_pk_bf16_f32 v208, v176, v177\n\t" \
  "v_cvt_pk_bf16_f32 v209, v178, v179\n\t" \
  "ds_write_b64 v234, v[208:209]\n\t" \
  "v_mfma_f32_32x32x16_bf16 v[136:151], v[60:63], v[4:7], v[136:151]\n\t" \
  "v_cvt_pk_bf16_f32 v210, v192, v193\n\t" \
  "v_cvt_pk_bf16_f32 v211, v194, v195\n\t" \
  "ds_write_b64 v238, v[210:211]\n\t" \
  "v_cvt_pk_bf16_f32 v212, v180, v181\n\t" \
  "v_cvt_pk_bf16_f32 v213, v182, v183\n\t" \
  "ds_write_b64 v235, v[212:213]\n\t" \
  "v_mfma_f32_32x32x16_bf16 v[152:167], v[68:71], v[4:7], v[152:167]\n\t" \
  "v_cvt_pk_bf16_f32 v214, v196, v197\n\t" \
  "v_cvt_pk_bf16_f32 v215, v198, v199\n\t" \
  "ds_write_b64 v239, v[214:215]\n\t" \
  "ds_read_b128 v[88:91], v240 offset:0\n\t" \
  "ds_read_b128 v[92:95], v240 offset:1024\n\t" \
  "ds_read_b128 v[96:99], v240 offset:2048\n\t" \
  "ds_read_b128 v[100:103], v240 offset:3072\n\t" \
  "v_mfma_f32_32x32x16_bf16 v[136:151], v[68:71], v[20:23], v[136:151]\n\t" \
  "v_mfma_f32_32x32x16_bf16 v[152:167], v[60:63], v[12:15], v[152:167]\n\t" \
  "s_waitcnt lgkmcnt(3)\n\t" \
  "buffer_store_dwordx4 v[88:91], v241, s[40:43], s52 offen sc1\n\t" \
  "s_waitcnt lgkmcnt(2)\n\t" \
  "buffer_store_dwordx4 v[92:95], v241, s[40:43], s53 offen sc1\n\t" \
  "s_waitcnt lgkmcnt(1)\n\t" \
  "buffer_store_dwordx4 v[96:99], v241, s[40:43], s54 offen sc1\n\t" \
  "s_waitcnt lgkmcnt(0)\n\t" \
  "buffer_store_dwordx4 v[100:103], v241, s[40:43], s55 offen sc1\n\t" \
  "v_mfma_f32_32x32x16_bf16 v[168:183], v[72:75], v[0:3], 0\n\t" \
  "v_mfma_f32_32x32x16_bf16 v[184:199], v[80:83], v[0:3], 0\n\t" \
  "s_add_u32 s52, s48, 256\n\t" \
  "s_add_u32 s53, s48, 33024\n\t" \
  "s_add_u32 s54, s48, 65792\n\t" \
  "s_add_u32 s55, s48, 98560\n\t" \
  "v_cvt_pk_bf16_f32 v200, v136, v137\n\t" \
  "v_cvt_pk_bf16_f32 v201, v138, v139\n\t" \
  "v_mfma_f32_32x32x16_bf16 v[168:183], v[80:83], v[16:19], v[168:183]\n\t" \
  "ds_write_b64 v232, v[200:201]\n\t" \
  "v_cvt_pk_bf16_f32 v202, v152, v153\n\t" \
  "v_cvt_pk_bf16_f32 v203, v154, v155\n\t" \
  "ds_write_b64 v236, v[202:203]\n\t" \
  "v_cvt_pk_bf16_f32 v204, v140, v141\n\t" \
  "v_cvt_pk_bf16_f32 v205, v142, v143\n\t" \
  "v_mfma_f32_32x32x16_bf16 v[184:199], v[72:75], v[8:11], v[184:199]\n\t" \
  "ds_write_b64 v233, v[204:205]\n\t" \
  "v_cvt_pk_bf16_f32 v206, v156, v157\n\t" \
  "v_cvt_pk_bf16_f32 v207, v158, v159\n\t" \
  "ds_write_b64 v237, v[206:207]\n\t" \
  "v_cvt_pk_bf16_f32 v208, v144, v145\n\t" \
  "v_cvt_pk_bf16_f32 v209, v146, v147\n\t" \
  "ds_write_b64 v234, v[208:209]\n\t" \
  "v_mfma_f32_32x32x16_bf16 v[168:183], v[76:79], v[4:7], v[168:183]\n\t" \
  "v_cvt_pk_bf16_f32 v210, v160, v161\n\t" \
  "v_cvt_pk_bf16_f32 v211, v162, v163\n\t" \
  "ds_write_b64 v238, v[210:211]\n\t" \
  "v_cvt_pk_bf16_f32 v212, v148, v149\n\t" \
  "v_cvt_pk_bf16_f32 v213, v150, v151\n\t" \
  "ds_write_b64 v235, v[212:213]\n\t" \
  "v_mfma_f32_32x32x16_bf16 v[184:199], v[84:87], v[4:7], v[184:199]\n\t" \
  "v_cvt_pk_bf16_f32 v214, v164, v165\n\t" \
  "v_cvt_pk_bf16_f32 v215, v166, v167\n\t" \
  "ds_write_b64 v239, v[214:215]\n\t" \
  "ds_read_b128 v[88:91], v240 offset:0\n\t" \
  "ds_read_b128 v[92:95], v240 offset:1024\n\t" \
  "ds_read_b128 v[96:99], v240 offset:2048\n\t" \
  "ds_read_b128 v[100:103], v240 offset:3072\n\t" \
  "v_mfma_f32_32x32x16_bf16 v[168:183], v[84:87], v[20:23], v[168:183]\n\t" \
  "v_mfma_f32_32x32x16_bf16 v[184:199], v[76:79], v[12:15], v[184:199]\n\t" \
  "s_waitcnt lgkmcnt(3)\n\t" \
  "buffer_store_dwordx4 v[88:91], v241, s[40:43], s52 offen sc1\n\t" \
  "s_waitcnt lgkmcnt(2)\n\t" \
  "buffer_store_dwordx4 v[92:95], v241, s[40:43], s53 offen sc1\n\t" \
  "s_waitcnt lgkmcnt(1)\n\t" \
  "buffer_store_dwordx4 v[96:99], v241, s[40:43], s54 offen sc1\n\t" \
  "s_waitcnt lgkmcnt(0)\n\t" \
  "buffer_store_dwordx4 v[100:103], v241, s[40:43], s55 offen sc1\n\t" \
  "s_add_u32 s52, s48, 384\n\t" \
  "s_add_u32 s53, s48, 33152\n\t" \
  "s_add_u32 s54, s48, 65920\n\t" \
  "s_add_u32 s55, s48, 98688\n\t" \
  "v_cvt_pk_bf16_f32 v200, v168, v169\n\t" \
  "v_cvt_pk_bf16_f32 v201, v170, v171\n\t" \
  "ds_write_b64 v232, v[200:201]\n\t" \
  "v_cvt_pk_bf16_f32 v202, v184, v185\n\t" \
  "v_cvt_pk_bf16_f32 v203, v186, v187\n\t" \
  "ds_write_b64 v236, v[202:203]\n\t" \
  "v_cvt_pk_bf16_f32 v204, v172, v173\n\t" \
  "v_cvt_pk_bf16_f32 v205, v174, v175\n\t" \
  "ds_write_b64 v233, v[204:205]\n\t" \
  "v_cvt_pk_bf16_f32 v206, v188, v189\n\t" \
  "v_cvt_pk_bf16_f32 v207, v190, v191\n\t" \
  "ds_write_b64 v237, v[206:207]\n\t" \
  "v_cvt_pk_bf16_f32 v208, v176, v177\n\t" \
  "v_cvt_pk_bf16_f32 v209, v178, v179\n\t" \
  "ds_write_b64 v234, v[208:209]\n\t" \
  "v_cvt_pk_bf16_f32 v210, v192, v193\n\t" \
  "v_cvt_pk_bf16_f32 v211, v194, v195\n\t" \
  "ds_write_b64 v238, v[210:211]\n\t" \
  "v_cvt_pk_bf16_f32 v212, v180, v181\n\t" \
  "v_cvt_pk_bf16_f32 v213, v182, v183\n\t" \
  "ds_write_b64 v235, v[212:213]\n\t" \
  "v_cvt_pk_bf16_f32 v214, v196, v197\n\t" \
  "v_cvt_pk_bf16_f32 v215, v198, v199\n\t" \
  "ds_write_b64 v239, v[214:215]\n\t" \
  "ds_read_b128 v[88:91], v240 offset:0\n\t" \
  "ds_read_b128 v[92:95], v240 offset:1024\n\t" \
  "ds_read_b128 v[96:99], v240 offset:2048\n\t" \
  "ds_read_b128 v[100:103], v240 offset:3072\n\t" \
  "s_waitcnt lgkmcnt(3)\n\t" \
  "buffer_store_dwordx4 v[88:91], v241, s[40:43], s52 offen sc1\n\t" \
  "s_waitcnt lgkmcnt(2)\n\t" \
  "buffer_store_dwordx4 v[92:95], v241, s[40:43], s53 offen sc1\n\t" \
  "s_waitcnt lgkmcnt(1)\n\t" \
  "buffer_store_dwordx4 v[96:99], v241, s[40:43], s54 offen sc1\n\t" \
  "s_waitcnt lgkmcnt(0)\n\t" \
  "buffer_store_dwordx4 v[100:103], v241, s[40:43], s55 offen sc1\n\t" \
  "s_waitcnt vmcnt(0)\n\t" \
  "s_barrier\n\t"

constexpr float LAM = 1.6244575f;
#define WAITV(n) asm volatile("s_waitcnt vmcnt(" #n ")" ::: "memory")
#define WAITL0() asm volatile("s_waitcnt lgkmcnt(0)" ::: "memory")
#define BAR() asm volatile("s_waitcnt lgkmcnt(0)\n\ts_barrier" ::: "memory")
#define SGB(m, n) __builtin_amdgcn_sched_group_barrier(m, n, 0)
#define SCHEDB() __builtin_amdgcn_sched_barrier(0)
DI int lane_id() { int l; asm volatile("v_mbcnt_lo_u32_b32 %0, -1, 0\n\tv_mbcnt_hi_u32_b32 %0, -1, %0" : "=v"(l)); return l; }
DI u32x4 mksrd(const void* p, unsigned bytes) { u32x4 s; s[0] = rfl((unsigned)(size_t)p); s[1] = rfl((unsigned)((size_t)p >> 32)) & 0xffffu; s[2] = bytes; s[3] = 0x20000u; return s; }
template <unsigned S1, unsigned S2, unsigned SP> DI void col_src(u32x16& o, int w, int lane, int cp) {
  const int n1l = lane >> 4, ch = lane & 3;
#pragma unroll
  for (int par = 0; par < 2; ++par) { const int u = ((lane >> 2) & 3) ^ n1l ^ (2 * par);
    const unsigned b = n1l * S1 + (4 * w + 2 * (u & 1) + (par ^ cp)) * S2 + (u >> 1) * SP + 3072;
#pragma unroll
    for (int i = 0; i < 4; ++i) o[4 * par + i] = b + i * (4 * S1 - 1024) + ((ch ^ i) << 4); }
  o[14] = __float_as_uint((float)((lane & 31) * (4 * w + (1 ^ cp))) * (1.f / 1024.f));
}
#define DBGDECL
#define DBGOUT0
#define DBGOUT1
#define DBGSAVE(k)
typedef unsigned u32x32 __attribute__((ext_vector_type(32)));
#define CL10(p, a) p #a "0", p #a "1", p #a "2", p #a "3", p #a "4", p #a "5", p #a "6", p #a "7", p #a "8", p #a "9"
#define V24_183 "v24", "v25", "v26", "v27", "v28", "v29", CL10("v", 3), CL10("v", 4), CL10("v", 5), CL10("v", 6), CL10("v", 7), CL10("v", 8), CL10("v", 9), CL10("v", 10), CL10("v", 11), CL10("v", 12), CL10("v", 13), CL10("v", 14), CL10("v", 15), CL10("v", 16), CL10("v", 17), "v180", "v181", "v182", "v183"
#define V24_223 V24_183, "v184", "v185", "v186", "v187", "v188", "v189", CL10("v", 19), CL10("v", 20), CL10("v", 21), "v220", "v221", "v222", "v223"
#define V56_227 "v56", "v57", "v58", "v59", CL10("v", 6), CL10("v", 7), CL10("v", 8), CL10("v", 9), CL10("v", 10), CL10("v", 11), CL10("v", 12), CL10("v", 13), CL10("v", 14), CL10("v", 15), CL10("v", 16), CL10("v", 17), CL10("v", 18), CL10("v", 19), CL10("v", 20), CL10("v", 21), "v220", "v221", "v222", "v223", "v224", "v225", "v226", "v227"
DI u32x8 col_addrs(unsigned lb, int w, int lane, int cp) {
  const int r = lane & 31, h = lane >> 5, blk = (lane >> 4) & 1, q = (lane & 15) >> 2, p = lane & 3, chn = 2 * blk + (p >> 1);
  const unsigned b1 = lb + w * 20480 + (4 * h + q) * 256 + 8 * (p & 1) + (q << 6);
  const float rf = (float)r * (1.f / 1024.f); u32x8 ad;
  ad[0] = __float_as_uint(rf); ad[1] = __float_as_uint(rf * (float)(4 * w + cp)); ad[2] = __float_as_uint(rf * (float)(128 * h));
  ad[3] = b1 + ((chn ^ h) << 4); ad[4] = b1 + ((chn ^ (2 + h)) << 4);
  ad[5] = lb + w * 20480 + r * 256 + 8 * h + ((r & 3) << 6) + (((r >> 2) & 3) << 4);
  ad[6] = lb + h * 20480 + ((q ^ cp) & 1) * 8192 + w * 1024 + ((chn ^ (w & 3)) << 4) + 8 * (p & 1) + (((q >> 1) ^ (2 * ((q ^ cp) & 1))) << 6); ad[7] = ad[6];
  return ad;
}
DI void emit_addrs(u32x16& em, unsigned bnc, unsigned RS, int lane) {
  const int r = lane & 31, h = lane >> 5;
#pragma unroll
  for (int k = 0; k < 8; ++k) em[k] = bnc + r * 128 + 8 * h + ((k ^ (r & 7)) << 4);
  em[8] = bnc + (lane >> 3) * 128 + (((lane & 7) ^ (lane >> 3)) << 4); em[9] = (lane >> 3) * RS + (lane & 7) * 16;
  em[10] = (lane >> 3) * 128 + ((((lane >> 2) ^ (lane >> 4)) & 1) << 6) + (lane & 3) * 16; em[11] = lane * 16; em[12] = em[13] = em[14] = em[15] = em[11];
}
#define RLX __ATOMIC_RELAXED, __HIP_MEMORY_SCOPE_AGENT
constexpr unsigned SPIN_CAP = 1u << 16;
DI void echo(unsigned* rec) { unsigned* p = rec + 8 * lane_id(); unsigned v[4];
#pragma unroll
  for (int k = 0; k < 4; ++k) v[k] = __hip_atomic_load(p + 2 * k, RLX);
#pragma unroll
  for (int k = 0; k < 4; ++k) __hip_atomic_store(p + 2 * k + 1, v[k], RLX); }
DI void wait_ack(unsigned* rec, int id, unsigned n, unsigned ack, bool coord) { unsigned spins = 0;
#pragma unroll 1
  while (ack != n && ++spins < SPIN_CAP) { if (coord) echo(rec); __builtin_amdgcn_s_sleep(1); ack = rfl(__hip_atomic_load(rec + 2 * id + 1, RLX)); } }
DI void arrive(unsigned* c) { if (lane_id() == 0) __hip_atomic_fetch_add(c, 1u, RLX); }
DI void poll(unsigned* c, unsigned need, unsigned* rec, bool coord) { unsigned spins = 0;
#pragma unroll 1
  while (rfl(__hip_atomic_load(c, RLX)) < need && ++spins < SPIN_CAP) { if (coord && (spins & 15) == 0) echo(rec); __builtin_amdgcn_s_sleep(1); } }
DI void acquire_bar(int w) { asm volatile("s_barrier" ::: "memory"); }
DI float gate_row(const u32x16& xg);
#define V24_135 "v24", "v25", "v26", "v27", "v28", "v29", CL10("v", 3), CL10("v", 4), CL10("v", 5), CL10("v", 6), CL10("v", 7), V80_135
#define V80_135 CL10("v", 8), CL10("v", 9), CL10("v", 10), CL10("v", 11), CL10("v", 12), "v130", "v131", "v132", "v133", "v134", "v135"
#define V88_135 "v88", "v89", CL10("v", 9), CL10("v", 10), CL10("v", 11), CL10("v", 12), "v130", "v131", "v132", "v133", "v134", "v135"
#define V152_223 "v152", "v153", "v154", "v155", "v156", "v157", "v158", "v159", CL10("v", 16), CL10("v", 17), CL10("v", 18), CL10("v", 19), V200_223
#define V200_223 CL10("v", 20), CL10("v", 21), "v220", "v221", "v222", "v223"
DI void fft_block(const u16* gw, float* g, const u16* x, const u16* wr, const u16* wi, char* ubuf, char* vbuf, char* abuf, unsigned* rec, unsigned* cnt, unsigned lb, int bid, int c, int w STAMP_ARG) {
  int lane = lane_id(); const int W = c * 8 + w; const bool coord = bid == 0 && w == 0;
  if (coord && lane < 19) __hip_atomic_store(cnt + 64 * lane, 0u, RLX);
  const unsigned ureg = lb + w * 20480;
  u32x8 m0, m1, m2, sw = __builtin_shufflevector(mksrd(wr, 2048 * 1026), mksrd(wi, 2048 * 1026), 0, 1, 2, 3, 4, 5, 6, 7);
  u32x16 em; u32x2 na;
  { u32x16 so; col_src<131072, 4096, 2048>(so, w, lane, c & 1);
    so[10] = so[11] = so[0];
    so[12] = bid * 8; so[13] = (w == 0 && lane == 0) ? bid * 8 : 0x40000000u; so[8] = so[9] = so[15] = so[0];
    u32x8 ad = col_addrs(lb, w, lane, c & 1); ad[7] = coord ? lane * 32 : 0x40000000u; emit_addrs(em, ureg + 16384, 131072, lane);
    const u32x8 su = __builtin_shufflevector(mksrd(ubuf + c * 128, 4194304 - c * 128), mksrd((const char*)x + c * 64 - 3072, 4194304 + 3072 - c * 64), 0, 1, 2, 3, 4, 5, 6, 7);
    const u32x4 sc = {(unsigned)w * 16384u, 0u, 0u, ureg}, srec = mksrd(rec, 2048);
    STAMP(1);
    DBGDECL asm volatile(FFT_P1 : DBGOUT1 "={v[0:7]}"(m0), "={v[8:15]}"(m1), "={v[16:23]}"(m2), "={v[248:249]}"(na), "+{v[136:151]}"(so)
                 : "{v[224:231]}"(ad), "{v[232:247]}"(em), "{s[40:47]}"(su), "{s[48:51]}"(sc), "{s[68:71]}"(srec)
                 : "memory", "scc", "vcc", "s52", "s53", "s54", "s55", "s64", "s65", "s66", "s67", V24_135, V152_223); DBGSAVE(0) }
  STAMP(2);
  if (w == 0) { wait_ack(rec, bid, rfl(na[0]), rfl(na[1]), coord); arrive(cnt + 64 * 16); }
  lane = lane_id();
  {
    asm volatile(FFT_W :: "{v[232:247]}"(em), "{s49}"(2u * W + 1u), "{s50}"(ureg + 8192), "{s[56:63]}"(sw) : "memory", "scc", "s64", "s65", "s66", "s67"); }
  { u32x32 tw; u32x4 ra;
    {
      const int it = (lane >> 3) & 3, p = it == 0 ? 2 * W + 1 : it == 1 ? 2 * W + 2 : 0, m = (lane >> 1) & 1, cl = (lane >> 2) & 1;
      const int rd = p + 1024 * m, rm = (2048 - rd) & 2047; const u16* src = (lane & 1) ? wi : wr;
      const unsigned evd = src[rd * 513 + cl * 512], evm = src[rm * 513 + cl * 512];
      const float K0 = 1.4142135624f / (1448.154688f * LAM * LAM * LAM * LAM);
#pragma unroll
      for (int i = 0; i < 16; ++i) { const float a = (float)((lane & 31) * ((i & 3) + 8 * (i >> 2) + 4 * (lane >> 5))) * (1.f / 1024.f);
        tw[i] = __float_as_uint(K0 * __builtin_amdgcn_cosf(a)); tw[16 + i] = __float_as_uint(K0 * __builtin_amdgcn_sinf(a)); }
      asm volatile("" : "+v"(tw));
      const float d = __uint_as_float(evd << 16), mm = __uint_as_float(evm << 16); em[13] = __float_as_uint((lane & 1) ? (cl ? 0.5f * (mm - d) : 0.5f * (d - mm)) : 0.5f * (d + mm)); }
    { const int it = lane & 1, p = it == 0 ? 2 * W + 1 : (2 * W + 2) & 511, pp = 2 * W + 1;
      const float an = 0.125f - (float)p * (1.f / 4096.f), cc = __builtin_amdgcn_cosf(an), cp = it == 0 ? 1.f : __builtin_amdgcn_cosf(0.125f - (float)pp * (1.f / 4096.f));
      em[12] = __float_as_uint((lane & 4) ? cc / cp : __builtin_amdgcn_sinf(an) / cc); }
    const int r = lane & 31, h = lane >> 5, blk = (lane >> 4) & 1, q = (lane & 15) >> 2, pp = lane & 3;
    const unsigned b0 = ureg + (4 * h + q) * 128 + (2 * blk + (pp >> 1)) * 16 + 8 * (pp & 1);
    ra[0] = b0 + ((q >> 1) << 6); ra[1] = b0 + (((q >> 1) ^ 1) << 6); ra[2] = ureg + 8192 + 2 * r + 256 * h; ra[3] = ureg + 8192 + 2048 + 320 - 256 * h - 2 * r;
    u32x8 sv = __builtin_shufflevector(mksrd(vbuf, 4194304), mksrd(ubuf, 4194304), 0, 1, 2, 3, 4, 5, 6, 7);
    u32x4 sc = {2u * W + 1u, 2u * W + 2u, ureg + 8192, ureg}; unsigned p2 = 0, nit = 2;
    asm volatile("" : "+v"(ra), "+v"(em));
    STAMP(3);
    if (bid == 0 && w == 1) echo(rec);
    if (w == 0) poll(cnt + 64 * 16, 32, rec, coord);
    acquire_bar(w);
    STAMP(10);
    DBGDECL asm volatile(FFT_P2 : DBGOUT1 "+{v[24:55]}"(tw), "+{v[232:247]}"(em), "+{s[40:47]}"(sv), "+{s[48:51]}"(sc), "+{s[56:63]}"(sw)
                 : "{v[0:7]}"(m0), "{v[8:15]}"(m1), "{v[16:23]}"(m2), "{v[228:231]}"(ra), "{s68}"(p2), "{s70}"(nit)
                 : "memory", "scc", "s52", "s53", "s54", "s55", "s64", "s65", "s66", "s67", "s69", "s71", "s72", "s73", "s74", "s75", "s76", "s77", "s78", "s79", V56_227); DBGSAVE(1) }
  STAMP(11);
  if (w == 0) arrive(cnt + 64 * 17);
  lane = lane_id();
  { u32x16 xg; const int row = (bid >> 2) * 256 + 224 + (bid & 3) * 8 + w;
#pragma unroll
    for (int i = 0; i < 2; ++i) { const u32x4 a = *(const u32x4*)(x + row * 1024 + i * 512 + lane * 8), b = *(const u32x4*)(gw + i * 512 + lane * 8);
#pragma unroll
      for (int k = 0; k < 4; ++k) { xg[4 * i + k] = a[k]; xg[8 + 4 * i + k] = b[k]; } }
    u32x8 ad = col_addrs(lb, w, lane, 0);
    { const float gv = gate_row(xg); if (lane == 0) __hip_atomic_store(g + row, gv, RLX); }
    u32x16 so; col_src<4096, 128, 64>(so, w, lane, 0);
#pragma unroll
    for (int i = 8; i < 16; ++i) if (i != 14) so[i] = so[0];
    em[9] = (lane >> 3) * 4096 + (lane & 7) * 16;
    const u32x8 sa = __builtin_shufflevector(mksrd(abuf + c * 131072, 131072), mksrd(vbuf + c * 131072 - 3072, 131072 + 3072), 0, 1, 2, 3, 4, 5, 6, 7);
    const u32x4 sc = {(unsigned)w * 512u, 0u, 0u, ureg};
    asm volatile("" : "+v"(so), "+v"(em), "+v"(ad));
    STAMP(12);
    if (bid == 0 && w == 1) echo(rec);
    if (w == 0) poll(cnt + 64 * 17, 32, rec, coord);
    acquire_bar(w);
    STAMP(13);
    DBGDECL asm volatile(FFT_P3 : DBGOUT1 "+{v[136:151]}"(so)
                 : "{v[0:7]}"(m0), "{v[8:15]}"(m1), "{v[16:23]}"(m2), "{v[224:231]}"(ad), "{v[232:247]}"(em), "{s[40:47]}"(sa), "{s[48:51]}"(sc)
                 : "memory", "scc", "s52", "s53", "s54", "s55", V24_135, V152_223); DBGSAVE(2) }
}

#define V144_223 "v144", "v145", "v146", "v147", "v148", "v149", CL10("v", 15), CL10("v", 16), CL10("v", 17), CL10("v", 18), CL10("v", 19), CL10("v", 20), CL10("v", 21), "v220", "v221", "v222", "v223"
#define TOUCH_WAVE 7
#define V64_207 "v64", "v65", "v66", "v67", "v68", "v69", CL10("v", 7), V80_199, "v200", "v201", "v202", "v203", "v204", "v205", "v206", "v207"
#define V80_199 CL10("v", 8), CL10("v", 9), CL10("v", 10), CL10("v", 11), CL10("v", 12), CL10("v", 13), CL10("v", 14), CL10("v", 15), CL10("v", 16), CL10("v", 17), CL10("v", 18), CL10("v", 19)
DI float gate_row(const u32x16& xg) {
  float s = 0.f;
#pragma unroll
  for (int i = 0; i < 8; ++i) { const unsigned pr = cvtpk(blo(xg[i]) * blo(xg[8 + i]), bhi(xg[i]) * bhi(xg[8 + i])); s += blo(pr) + bhi(pr); }
#pragma unroll
  for (int o = 32; o; o >>= 1) s += __shfl_xor(s, o);
  const float z = blo(cvtpk(s, s));
  return blo(cvtpk(__builtin_amdgcn_rcpf(1.f + __builtin_amdgcn_exp2f(z * -1.44269504f)), 0.f));
}
#define V0_15 "v0", "v1", "v2", "v3", "v4", "v5", "v6", "v7", "v8", "v9", "v10", "v11", "v12", "v13", "v14", "v15"
#define V16_143 "v16", "v17", "v18", "v19", CL10("v", 2), CL10("v", 3), CL10("v", 4), CL10("v", 5), CL10("v", 6), CL10("v", 7), CL10("v", 8), CL10("v", 9), CL10("v", 10), CL10("v", 11), CL10("v", 12), CL10("v", 13), "v140", "v141", "v142", "v143"
#define GDBG_DECL
#define GDBG_OUT
#define GDBG_SAVE
#define SCALARS(bid, w) \
  const int xc = bid & 7, r = (bid >> 3) - 4, wrow = w >> 2, wc = w & 3; \
  const int kh = xc & 1, lin = (xc >> 1) * 28 + r, nt = lin & 7, mt = lin >> 3, mb0 = mt * 9 + (mt == 13); const bool ten = mt >= 12; \
  const u32x4 sd = mksrd((const char*)wd + kh * 56 * 65536 + nt * 256 - 3072, 56 * 65536 - 2048 + 256 + 3072); \
  const unsigned ring = rfl(lb + BRING + w * 2048);
__global__ __launch_bounds__(512, 2) void fused(const u16* x, const u16* wr, const u16* wi, const u16* wg, const u16* wu, const u16* wd, const u16* gw, char* ws, u16* out) {
  __shared__ __attribute__((aligned(1024))) char lds[163840];
  int w = rfl(threadIdx.x >> 6), bid = blockIdx.x;
  const unsigned lb = (unsigned)(size_t)lds;
  float* g = (float*)(ws + WS_G); unsigned* rec = (unsigned*)(ws + WS_REC); unsigned* cnt = (unsigned*)(ws + WS_CNT);
  STAMP(0);
  if (bid < 32) {
    fft_block(gw, g, x, wr, wi, ws + WS_U, ws + WS_V, ws + WS_A, rec, cnt, lb, bid, (bid & 7) * 4 + (bid >> 3), w STAMP_PASS);
    STAMP(8);
    if (w == 0) { arrive(cnt + 64 * 18);
      if (bid == 0) { unsigned spins = 0; const int lane = lane_id();
#pragma unroll 1
        while (!__all(__hip_atomic_load(cnt + 64 * (lane & 15), RLX) >= 14u) && ++spins < SPIN_CAP) { echo(rec); __builtin_amdgcn_s_sleep(8); } } }
    STAMP(6);
    return;
  }
  {
    SCALARS(bid, w)
    const int pm = (xc & 1) * 4 + (r & 3), pn = (xc >> 1) * 7 + (r >> 2), ca = pm * 2 + (pn >= 14), cp = ca ^ 8, grow = pm * 256 + pn * 8 + w;
    u32x8 sa = __builtin_shufflevector(mksrd((const char*)x + (size_t)pm * 256 * 2048 - 3072, 256 * 2048 + 3072), mksrd((const char*)(w < 4 ? wg : wu) + pn * 256 - 3072, 1023 * 7168 + 256 + 3072), 0, 1, 2, 3, 4, 5, 6, 7);
    const unsigned m0a = rfl(lb + w * 4096), m0b = rfl(lb + 65536 + w * 4096);
    u32x8 gv; u32x4 lv;
    asm volatile(G1_START : "={v[232:239]}"(gv), "={v[240:243]}"(lv) : "{s[40:47]}"(sa), "{s50}"(m0a), "{s51}"(m0b), "{s85}"(w) : "memory", "scc", "s52");
    const int lane = lv[0]; const unsigned l16 = lv[3];
    u32x4 tv = {l16, l16, l16, l16}; u32x2 pg = {l16, l16};
    if (w == TOUCH_WAVE) {
      { const int b = bid - 32, L = b * 42 + lane, Lw = L - 2048, mm = Lw >= 3584, l2 = Lw - mm * 3584, k = (int)((unsigned)l2 / 56u), M = min((b * 22 + lane - 42) * 12, 57344 - 12);
        const bool isw = lane < 42 && L >= 2048 && L < 9216, isx = lane < 42 && L < 2048, isd = lane >= 42;
        const char* a = isw ? (const char*)(mm ? wu : wg) + (size_t)(256 + k) * 7168 + (l2 - k * 56) * 128 : isd ? (const char*)wd + (size_t)M * 128 : (const char*)x + (isx ? L * 2048 + 512 : 0);
        tv[0] = (unsigned)(size_t)a; tv[1] = (unsigned)((size_t)a >> 32); tv[2] = isw ? 64 * 7168 : (isx || isd) ? 128 : 0; tv[3] = tv[2]; }
      { const char* pc; asm("s_getpc_b64 %0" : "=s"(pc)); const char* a = lane < 21 ? ws + ((size_t)lane << 21) + 4096 * (bid & 63) : lane < 25 ? (const char*)out + ((size_t)(lane - 21) << 20) : lane < 32 ? (const char*)wd + ((size_t)(lane - 25) << 20)
                      : lane < 39 ? (const char*)wg + ((size_t)(lane - 32) << 20) : lane < 46 ? (const char*)wu + ((size_t)(lane - 39) << 20) : lane < 48 ? (const char*)((lane & 1) ? wr : wi) + (1 << 20) : pc + (r * 16 + lane - 48) * 120;
        pg[0] = (unsigned)(size_t)a; pg[1] = (unsigned)((size_t)a >> 32); }
    }
    const u32x4 sh = mksrd(ws + WS_H + (size_t)((pn * 4 + wc) * 128 + pm * 16 + wrow * 8) * 1024, 8192);
    const unsigned tw1 = sgpr(w == TOUCH_WAVE ? ~0u : 0u);
    u32x4 sb = {0u, 0u, m0a, m0b}, se = mksrd(ws, WS_X + 256 * 25600);
    u32x16 sc = __builtin_shufflevector(__builtin_shufflevector((u32x4){(unsigned)(WS_G + grow * 4), sgpr(WS_X + (bid * 8 + w) * 3200 + 3072), 0u, 0u}, mksrd(x + grow * 1024, 2048), 0, 1, 2, 3, 4, 5, 6, 7),
                                        __builtin_shufflevector(sh, mksrd(gw, 2048), 0, 1, 2, 3, 4, 5, 6, 7), 0, 1, 2, 3, 4, 5, 6, 7, 8, 9, 10, 11, 12, 13, 14, 15);
    u32x8 sf = {ring, 0u, tw1, sgpr(2048 + (r * 8 + w) * 96), (unsigned)bid, (unsigned)w, (unsigned)(size_t)out, (unsigned)((size_t)out >> 32)};
    GDBG_DECL
    asm volatile(G1_LOOP G2_HEAD G2_LOOP
                 : GDBG_OUT "+{v[232:239]}"(gv), "+{v[240:243]}"(lv), "+{v[250:253]}"(tv), "+{s[40:47]}"(sa), "+{s[48:51]}"(sb), "+{s[56:71]}"(sc), "+{s[72:75]}"(se), "+{s[80:87]}"(sf)
                 : "{v[246:247]}"(pg), "{s[76:79]}"(sd), "{s93}"(sgpr(mb0 | nt << 8 | ca << 12 | (int)ten << 16 | cp << 20))
                 : "memory", "scc", "vcc", "s52", "s53", "s54", "s55", "s88", "s89", "s90", "s91", "s92", "v224", "v225", "v226", "v227", "v228", "v229", "v230", "v231", "v244", "v245", "v248", "v249", "v254", V0_15, V16_143, V144_223);
    GDBG_SAVE
    STAMP(6);
  }
}
extern "C" void kernel_launch(void* const* d_in, const int* in_sizes, int n_in, void* d_out, int out_size, void* d_ws, size_t ws_size, hipStream_t stream) {
  fused<<<256, 512, 0, stream>>>((const u16*)d_in[0], (const u16*)d_in[1], (const u16*)d_in[2], (const u16*)d_in[4], (const u16*)d_in[5], (const u16*)d_in[6], (const u16*)d_in[7], (char*)d_ws, (u16*)d_out);
}
```
